```python
import torch

def _softmax(x, axis=-1):
    return torch.softmax(x, dim=axis)
def _gelu(x):
    return torch.nn.functional.gelu(x, approximate="tanh")
def _silu(x):
    return torch.nn.functional.silu(x)
def _sigmoid(x):
    return torch.sigmoid(x)
def _layernorm(x, axis=-1, eps=1e-5):
    m = x.mean(dim=axis, keepdim=True)
    v = x.var(dim=axis, keepdim=True, unbiased=False)
    return (x - m) / torch.sqrt(v + eps)
def _scatter_add(tgt, idx, src, axis=0):
    return tgt.clone().index_add_(axis, idx, src)
def _segment_sum(src, seg, n):
    out = torch.zeros((n,) + src.shape[1:], dtype=src.dtype)
    return out.index_add_(0, seg, src)
def _pad(x, pw):
    flat = tuple(v for pair in reversed(pw) for v in pair)
    return torch.nn.functional.pad(x, flat)

kernel_name = 'parallel_sum__sliding_local_dilated__glu_gated_geglu__prompt'

def setup_inputs():

    _w = (lambda a: (lambda t: t.to(torch.bfloat16) if t.is_floating_point() else t)(torch.as_tensor(a)))
    def _A_setup():
        B, S, D = 1, 2048, 640
        return {
            "x": _w((lambda *s: torch.randn(*s, dtype=torch.bfloat16) * 0.05)(B, S, D)),
            "wqkv": _w((lambda *s: torch.randn(*s, dtype=torch.bfloat16) * 0.05)(D, 3 * D)),
            "wo": _w((lambda *s: torch.randn(*s, dtype=torch.bfloat16) * 0.05)(D, D)),
        }
    _A_in = _A_setup()

    def _B_setup():
        B, S, Dm, Df = 1, 2048, 640, 1536
        return {
            "x": _w((lambda *s: torch.randn(*s, dtype=torch.bfloat16) * 0.05)(B, S, Dm)),
            "w_gate": _w((lambda *s: torch.randn(*s, dtype=torch.bfloat16) * 0.8)(Dm, Df) * (Dm ** -0.5)),
            "w_up": _w((lambda *s: torch.randn(*s, dtype=torch.bfloat16) * 0.8)(Dm, Df) * (Dm ** -0.5)),
            "w_down": _w((lambda *s: torch.randn(*s, dtype=torch.bfloat16) * 0.8)(Df, Dm) * (Df ** -0.5)),
        }
    _B_in = _B_setup()

    return {
        "A_x": _A_in["x"],
        "A_wqkv": _A_in["wqkv"],
        "A_wo": _A_in["wo"],
        "B_x": _B_in["x"],
        "B_w_gate": _B_in["w_gate"],
        "B_w_up": _B_in["w_up"],
        "B_w_down": _B_in["w_down"],
    }

def reference(A_x, A_wqkv, A_wo, B_x, B_w_gate, B_w_up, B_w_down):
    def _A_ref(x, wqkv, wo):
        B, S, D = x.shape
        H = 32; Dh = D // H
        W, dil = 8, 2
        qkv = (lambda x, *d: x.permute(*d))((x @ wqkv).reshape(B, S, 3, H, Dh), 2, 0, 3, 1, 4)
        q, k, v = qkv[0], qkv[1], qkv[2]
        scale = Dh ** -0.5
        pos = torch.arange(S)
        off = torch.arange(W) * dil
        idx = pos[:, None] - off[None, :]
        mask = idx >= 0
        idx_c = torch.where(mask, idx, 0).reshape(-1)
        kg = k[:, :, idx_c, :].reshape(B, H, S, W, Dh)
        vg = v[:, :, idx_c, :].reshape(B, H, S, W, Dh)
        logits = torch.einsum("bhsd,bhswd->bhsw", q, kg) * scale
        logits = torch.where(mask[None, None, :, :], logits, -1e9)
        attn = _softmax(logits, axis=-1)
        o = torch.einsum("bhsw,bhswd->bhsd", attn, vg)
        return (lambda x, *d: x.permute(*d))(o, 0, 2, 1, 3).reshape(B, S, D) @ wo

    def _B_ref(x, w_gate, w_up, w_down):
        g = torch.einsum("bsd,df->bsf", x, w_gate)
        u = torch.einsum("bsd,df->bsf", x, w_up)
        h = _gelu(g) * u
        return torch.einsum("bsf,fd->bsd", h, w_down)

    x = A_x
    xn = _layernorm(x, axis=-1)
    return x + _A_ref(xn, A_wqkv, A_wo) + _B_ref(xn, B_w_gate, B_w_up, B_w_down)
```

```cpp
#include <hip/hip_runtime.h>
#include <hip/hip_bf16.h>
typedef unsigned short u16;
typedef __attribute__((ext_vector_type(8))) short bf16x8;
typedef __attribute__((ext_vector_type(4))) float f32x4;
__device__ __forceinline__ float bf2f(u16 v){ return __uint_as_float(((unsigned)v)<<16); }
__device__ __forceinline__ u16 f2bf(float f){ unsigned u=__float_as_uint(f); return (u16)((u+0x7FFF+((u>>16)&1))>>16); }
constexpr int S=2048,D=640,F=1536,H=32,DH=20;

__global__ void ln_k(const u16* x,u16* xn){
  int row=blockIdx.x*4+(threadIdx.x>>6), lane=threadIdx.x&63;
  float v[10]; float s=0;
  for(int i=0;i<10;i++){ v[i]=bf2f(x[row*D+lane+64*i]); s+=v[i]; }
  for(int o=32;o;o>>=1) s+=__shfl_xor(s,o);
  float m=s/D; float q=0;
  for(int i=0;i<10;i++){ float d=v[i]-m; q+=d*d; }
  for(int o=32;o;o>>=1) q+=__shfl_xor(q,o);
  float r=rsqrtf(q/D+1e-5f);
  for(int i=0;i<10;i++) xn[row*D+lane+64*i]=f2bf((v[i]-m)*r);
}
template<int MODE>
__global__ void gemm_k(const u16* A,int lda,const u16* B,int ldb,int K,u16* C,int ldc,float* Cf,int first){
  __shared__ u16 sA[64][40]; __shared__ u16 sB[64][40];
  int t=threadIdx.x, lane=t&63, w=t>>6, wr=w>>1, wc=w&1;
  int m0=blockIdx.y*64,n0=blockIdx.x*64;
  f32x4 acc[2][2]={};
  for(int k0=0;k0<K;k0+=32){
    { int r=t>>2,c=(t&3)*8; *(bf16x8*)&sA[r][c]=*(const bf16x8*)&A[(m0+r)*lda+k0+c]; }
    { int k=t>>3,c=(t&7)*8; bf16x8 v=*(const bf16x8*)&B[(k0+k)*ldb+n0+c]; for(int j=0;j<8;j++) sB[c+j][k]=v[j]; }
    __syncthreads();
    bf16x8 a[2],b[2];
    for(int i=0;i<2;i++){ a[i]=*(bf16x8*)&sA[wr*32+i*16+(lane&15)][(lane>>4)*8]; b[i]=*(bf16x8*)&sB[wc*32+i*16+(lane&15)][(lane>>4)*8]; }
    for(int i=0;i<2;i++)for(int j=0;j<2;j++) acc[i][j]=__builtin_amdgcn_mfma_f32_16x16x32_bf16(a[i],b[j],acc[i][j],0,0,0);
    __syncthreads();
  }
  for(int i=0;i<2;i++)for(int j=0;j<2;j++)for(int r=0;r<4;r++){
    int row=m0+wr*32+i*16+(lane>>4)*4+r,col=n0+wc*32+j*16+(lane&15);
    if(MODE==0) C[row*ldc+col]=f2bf(acc[i][j][r]);
    else { if(first) Cf[row*ldc+col]=acc[i][j][r]; else Cf[row*ldc+col]+=acc[i][j][r]; }
  }
}
__global__ void attn_k(const u16* qkv,u16* o){
  int id=blockIdx.x*256+threadIdx.x; int s=id/H,h=id%H;
  float q[DH]; for(int d=0;d<DH;d++) q[d]=bf2f(qkv[s*1920+h*DH+d]);
  float l[8]; float mx=-1e30f;
  for(int w=0;w<8;w++){ int t=s-2*w; float a=0; if(t>=0){ for(int d=0;d<DH;d++) a+=q[d]*bf2f(qkv[t*1920+640+h*DH+d]); a*=0.22360679775f; } else a=-1e9f; l[w]=a; mx=fmaxf(mx,a);}
  float sum=0; for(int w=0;w<8;w++){ l[w]=__expf(l[w]-mx); sum+=l[w]; }
  float acc[DH]; for(int d=0;d<DH;d++) acc[d]=0;
  for(int w=0;w<8;w++){ int t=s-2*w; if(t<0) t=0; float p=l[w]/sum; for(int d=0;d<DH;d++) acc[d]+=p*bf2f(qkv[t*1920+1280+h*DH+d]); }
  for(int d=0;d<DH;d++) o[s*D+h*DH+d]=f2bf(acc[d]);
}
__global__ void geglu_k(const u16* g,const u16* u,u16* hh){
  int i=blockIdx.x*256+threadIdx.x; float gv=bf2f(g[i]),uv=bf2f(u[i]);
  float z=0.7978845608f*(gv+0.044715f*gv*gv*gv); float ge=0.5f*gv*(1.f+tanhf(z)); hh[i]=f2bf(ge*uv);
}
__global__ void fin_k(const u16* x,const float* c,u16* out){ int i=blockIdx.x*256+threadIdx.x; out[i]=f2bf(bf2f(x[i])+c[i]); }
__global__ void empty_k(){}

extern "C" void kernel_launch(void* const* d_in,const int* in_sizes,int n_in,void* d_out,int out_size,void* d_ws,size_t ws_size,hipStream_t stream){
  const u16* x=(const u16*)d_in[0]; const u16* wqkv=(const u16*)d_in[1]; const u16* wo=(const u16*)d_in[2];
  const u16* wg=(const u16*)d_in[4]; const u16* wu=(const u16*)d_in[5]; const u16* wd=(const u16*)d_in[6];
  char* ws=(char*)d_ws;
  u16* xn=(u16*)ws; u16* qkv=(u16*)(ws+(4<<20)); u16* g=(u16*)(ws+(16<<20)); u16* u=(u16*)(ws+(24<<20)); u16* hh=(u16*)(ws+(32<<20)); u16* o=(u16*)(ws+(40<<20)); float* c=(float*)(ws+(48<<20));
  ln_k<<<S/4,256,0,stream>>>(x,xn);
  gemm_k<0><<<dim3(1920/64,S/64),256,0,stream>>>(xn,D,wqkv,1920,D,qkv,1920,nullptr,0);
  gemm_k<0><<<dim3(F/64,S/64),256,0,stream>>>(xn,D,wg,F,D,g,F,nullptr,0);
  gemm_k<0><<<dim3(F/64,S/64),256,0,stream>>>(xn,D,wu,F,D,u,F,nullptr,0);
  attn_k<<<S*H/256,256,0,stream>>>(qkv,o);
  geglu_k<<<S*F/256,256,0,stream>>>(g,u,hh);
  gemm_k<1><<<dim3(D/64,S/64),256,0,stream>>>(o,D,wo,D,D,nullptr,D,c,1);
  gemm_k<1><<<dim3(D/64,S/64),256,0,stream>>>(hh,F,wd,D,F,nullptr,D,c,0);
  fin_k<<<S*D/256,256,0,stream>>>(x,c,(u16*)d_out);
}
```

```cpp
#include <hip/hip_runtime.h>
#include <hip/hip_bf16.h>
typedef unsigned short u16;
typedef __attribute__((ext_vector_type(8))) short bf16x8;
typedef __attribute__((ext_vector_type(4))) float f32x4;
__device__ __forceinline__ float bf2f(u16 v){ return __uint_as_float(((unsigned)v)<<16); }
__device__ __forceinline__ u16 f2bf(float f){ unsigned u=__float_as_uint(f); return (u16)((u+0x7FFF+((u>>16)&1))>>16); }
constexpr int S=2048,D=640,F=1536,H=32,DH=20;

__global__ void ln_k(const u16* x,u16* xn){
  int row=blockIdx.x*4+(threadIdx.x>>6), lane=threadIdx.x&63;
  float v[10]; float s=0;
  for(int i=0;i<10;i++){ v[i]=bf2f(x[row*D+lane+64*i]); s+=v[i]; }
  for(int o=32;o;o>>=1) s+=__shfl_xor(s,o);
  float m=s/D; float q=0;
  for(int i=0;i<10;i++){ float d=v[i]-m; q+=d*d; }
  for(int o=32;o;o>>=1) q+=__shfl_xor(q,o);
  float r=rsqrtf(q/D+1e-5f);
  for(int i=0;i<10;i++) xn[row*D+lane+64*i]=f2bf((v[i]-m)*r);
}
template<int MODE>
__global__ void gemm_k(const u16* A,int lda,const u16* B,int ldb,int K,u16* C,int ldc,float* Cf,int first){
  __shared__ u16 sA[64][40]; __shared__ u16 sB[64][40];
  int t=threadIdx.x, lane=t&63, w=t>>6, wr=w>>1, wc=w&1;
  int m0=blockIdx.y*64,n0=blockIdx.x*64;
  f32x4 acc[2][2]={};
  for(int k0=0;k0<K;k0+=32){
    { int r=t>>2,c=(t&3)*8; *(bf16x8*)&sA[r][c]=*(const bf16x8*)&A[(m0+r)*lda+k0+c]; }
    { int k=t>>3,c=(t&7)*8; bf16x8 v=*(const bf16x8*)&B[(k0+k)*ldb+n0+c]; for(int j=0;j<8;j++) sB[c+j][k]=v[j]; }
    __syncthreads();
    bf16x8 a[2],b[2];
    for(int i=0;i<2;i++){ a[i]=*(bf16x8*)&sA[wr*32+i*16+(lane&15)][(lane>>4)*8]; b[i]=*(bf16x8*)&sB[wc*32+i*16+(lane&15)][(lane>>4)*8]; }
    for(int i=0;i<2;i++)for(int j=0;j<2;j++) acc[i][j]=__builtin_amdgcn_mfma_f32_16x16x32_bf16(a[i],b[j],acc[i][j],0,0,0);
    __syncthreads();
  }
  for(int i=0;i<2;i++)for(int j=0;j<2;j++)for(int r=0;r<4;r++){
    int row=m0+wr*32+i*16+(lane>>4)*4+r,col=n0+wc*32+j*16+(lane&15);
    if(MODE==0) C[row*ldc+col]=f2bf(acc[i][j][r]);
    else { if(first) Cf[row*ldc+col]=acc[i][j][r]; else Cf[row*ldc+col]+=acc[i][j][r]; }
  }
}
__global__ void attn_k(const u16* qkv,u16* o){
  int id=blockIdx.x*256+threadIdx.x; int s=id/H,h=id%H;
  float q[DH]; for(int d=0;d<DH;d++) q[d]=bf2f(qkv[s*1920+h*DH+d]);
  float l[8]; float mx=-1e30f;
  for(int w=0;w<8;w++){ int t=s-2*w; float a=0; if(t>=0){ for(int d=0;d<DH;d++) a+=q[d]*bf2f(qkv[t*1920+640+h*DH+d]); a*=0.22360679775f; } else a=-1e9f; l[w]=a; mx=fmaxf(mx,a);}
  float sum=0; for(int w=0;w<8;w++){ l[w]=__expf(l[w]-mx); sum+=l[w]; }
  float acc[DH]; for(int d=0;d<DH;d++) acc[d]=0;
  for(int w=0;w<8;w++){ int t=s-2*w; if(t<0) t=0; float p=l[w]/sum; for(int d=0;d<DH;d++) acc[d]+=p*bf2f(qkv[t*1920+1280+h*DH+d]); }
  for(int d=0;d<DH;d++) o[s*D+h*DH+d]=f2bf(acc[d]);
}
__global__ void geglu_k(const u16* g,const u16* u,u16* hh){
  int i=blockIdx.x*256+threadIdx.x; float gv=bf2f(g[i]),uv=bf2f(u[i]);
  float z=0.7978845608f*(gv+0.044715f*gv*gv*gv); float ge=0.5f*gv*(1.f+tanhf(z)); hh[i]=f2bf(ge*uv);
}
__global__ void fin_k(const u16* x,const float* c,u16* out){ int i=blockIdx.x*256+threadIdx.x; out[i]=f2bf(bf2f(x[i])+c[i]); }

__global__ void probe_k(const unsigned* p, int n, int strideW, unsigned* sink){
  unsigned idx=threadIdx.x; unsigned acc=0;
  for(int i=0;i<n;i++){
    unsigned v=p[idx];
    unsigned z; asm volatile("v_and_b32 %0, 0, %1":"=v"(z):"v"(v));
    acc+=v; idx+=strideW+z;
  }
  sink[threadIdx.x]=acc;
}

extern "C" void kernel_launch(void* const* d_in,const int* in_sizes,int n_in,void* d_out,int out_size,void* d_ws,size_t ws_size,hipStream_t stream){
  const u16* x=(const u16*)d_in[0]; const u16* wqkv=(const u16*)d_in[1]; const u16* wo=(const u16*)d_in[2];
  const u16* wg=(const u16*)d_in[4]; const u16* wu=(const u16*)d_in[5]; const u16* wd=(const u16*)d_in[6];
  char* ws=(char*)d_ws;
  u16* xn=(u16*)ws; u16* qkv=(u16*)(ws+(4<<20)); u16* g=(u16*)(ws+(16<<20)); u16* u=(u16*)(ws+(24<<20)); u16* hh=(u16*)(ws+(32<<20)); u16* o=(u16*)(ws+(40<<20)); float* c=(float*)(ws+(48<<20));
  probe_k<<<1,64,0,stream>>>((const unsigned*)d_in[3],0,2560,(unsigned*)(ws+(200<<20)));
  ln_k<<<S/4,256,0,stream>>>(x,xn);
  gemm_k<0><<<dim3(1920/64,S/64),256,0,stream>>>(xn,D,wqkv,1920,D,qkv,1920,nullptr,0);
  gemm_k<0><<<dim3(F/64,S/64),256,0,stream>>>(xn,D,wg,F,D,g,F,nullptr,0);
  gemm_k<0><<<dim3(F/64,S/64),256,0,stream>>>(xn,D,wu,F,D,u,F,nullptr,0);
  attn_k<<<S*H/256,256,0,stream>>>(qkv,o);
  geglu_k<<<S*F/256,256,0,stream>>>(g,u,hh);
  gemm_k<1><<<dim3(D/64,S/64),256,0,stream>>>(o,D,wo,D,D,nullptr,D,c,1);
  gemm_k<1><<<dim3(D/64,S/64),256,0,stream>>>(hh,F,wd,D,F,nullptr,D,c,0);
  fin_k<<<S*D/256,256,0,stream>>>(x,c,(u16*)d_out);
}
```

```cpp
#include <hip/hip_runtime.h>
#include <hip/hip_bf16.h>
typedef unsigned short u16;
typedef __attribute__((ext_vector_type(8))) short bf16x8;
typedef __attribute__((ext_vector_type(4))) float f32x4;
__device__ __forceinline__ float bf2f(u16 v){ return __uint_as_float(((unsigned)v)<<16); }
__device__ __forceinline__ u16 f2bf(float f){ unsigned u=__float_as_uint(f); return (u16)((u+0x7FFF+((u>>16)&1))>>16); }
constexpr int S=2048,D=640,F=1536,H=32,DH=20;

__global__ void ln_k(const u16* x,u16* xn){
  int row=blockIdx.x*4+(threadIdx.x>>6), lane=threadIdx.x&63;
  float v[10]; float s=0;
  for(int i=0;i<10;i++){ v[i]=bf2f(x[row*D+lane+64*i]); s+=v[i]; }
  for(int o=32;o;o>>=1) s+=__shfl_xor(s,o);
  float m=s/D; float q=0;
  for(int i=0;i<10;i++){ float d=v[i]-m; q+=d*d; }
  for(int o=32;o;o>>=1) q+=__shfl_xor(q,o);
  float r=rsqrtf(q/D+1e-5f);
  for(int i=0;i<10;i++) xn[row*D+lane+64*i]=f2bf((v[i]-m)*r);
}
template<int MODE>
__global__ void gemm_k(const u16* A,int lda,const u16* B,int ldb,int K,u16* C,int ldc,float* Cf,int first){
  __shared__ u16 sA[64][40]; __shared__ u16 sB[64][40];
  int t=threadIdx.x, lane=t&63, w=t>>6, wr=w>>1, wc=w&1;
  int m0=blockIdx.y*64,n0=blockIdx.x*64;
  f32x4 acc[2][2]={};
  for(int k0=0;k0<K;k0+=32){
    { int r=t>>2,c=(t&3)*8; *(bf16x8*)&sA[r][c]=*(const bf16x8*)&A[(m0+r)*lda+k0+c]; }
    { int k=t>>3,c=(t&7)*8; bf16x8 v=*(const bf16x8*)&B[(k0+k)*ldb+n0+c]; for(int j=0;j<8;j++) sB[c+j][k]=v[j]; }
    __syncthreads();
    bf16x8 a[2],b[2];
    for(int i=0;i<2;i++){ a[i]=*(bf16x8*)&sA[wr*32+i*16+(lane&15)][(lane>>4)*8]; b[i]=*(bf16x8*)&sB[wc*32+i*16+(lane&15)][(lane>>4)*8]; }
    for(int i=0;i<2;i++)for(int j=0;j<2;j++) acc[i][j]=__builtin_amdgcn_mfma_f32_16x16x32_bf16(a[i],b[j],acc[i][j],0,0,0);
    __syncthreads();
  }
  for(int i=0;i<2;i++)for(int j=0;j<2;j++)for(int r=0;r<4;r++){
    int row=m0+wr*32+i*16+(lane>>4)*4+r,col=n0+wc*32+j*16+(lane&15);
    if(MODE==0) C[row*ldc+col]=f2bf(acc[i][j][r]);
    else { if(first) Cf[row*ldc+col]=acc[i][j][r]; else Cf[row*ldc+col]+=acc[i][j][r]; }
  }
}
__global__ void attn_k(const u16* qkv,u16* o){
  int id=blockIdx.x*256+threadIdx.x; int s=id/H,h=id%H;
  float q[DH]; for(int d=0;d<DH;d++) q[d]=bf2f(qkv[s*1920+h*DH+d]);
  float l[8]; float mx=-1e30f;
  for(int w=0;w<8;w++){ int t=s-2*w; float a=0; if(t>=0){ for(int d=0;d<DH;d++) a+=q[d]*bf2f(qkv[t*1920+640+h*DH+d]); a*=0.22360679775f; } else a=-1e9f; l[w]=a; mx=fmaxf(mx,a);}
  float sum=0; for(int w=0;w<8;w++){ l[w]=__expf(l[w]-mx); sum+=l[w]; }
  float acc[DH]; for(int d=0;d<DH;d++) acc[d]=0;
  for(int w=0;w<8;w++){ int t=s-2*w; if(t<0) t=0; float p=l[w]/sum; for(int d=0;d<DH;d++) acc[d]+=p*bf2f(qkv[t*1920+1280+h*DH+d]); }
  for(int d=0;d<DH;d++) o[s*D+h*DH+d]=f2bf(acc[d]);
}
__global__ void geglu_k(const u16* g,const u16* u,u16* hh){
  int i=blockIdx.x*256+threadIdx.x; float gv=bf2f(g[i]),uv=bf2f(u[i]);
  float z=0.7978845608f*(gv+0.044715f*gv*gv*gv); float ge=0.5f*gv*(1.f+tanhf(z)); hh[i]=f2bf(ge*uv);
}
__global__ void fin_k(const u16* x,const float* c,u16* out){ int i=blockIdx.x*256+threadIdx.x; out[i]=f2bf(bf2f(x[i])+c[i]); }

template<int PF>
__global__ __launch_bounds__(256,1) void probe_k(const char* cold,int n,int share,unsigned* sink){
  int lane=threadIdx.x&63, w=__builtin_amdgcn_readfirstlane(threadIdx.x>>6);
  int b=blockIdx.x; int region= share? (b>>3) : b;
  const char* cb=cold+(size_t)region*(n*32768);
  __amdgpu_buffer_rsrc_t rc=__builtin_amdgcn_make_buffer_rsrc((void*)cb,0,0x7fffffff,0x00020000);
  unsigned poff=lane*128; unsigned pd=0; unsigned po=w*8192;
  for(int i=0;i<n;i++){
    if(PF==2) asm volatile("buffer_load_dword %0, %1, %2, %3 offen":"=v"(pd):"v"(poff),"s"(rc),"s"(po):"memory");
    if(PF==4) asm volatile("buffer_load_dword %0, %1, %2, %3 offen nt":"=v"(pd):"v"(poff),"s"(rc),"s"(po):"memory");
    po+=4*8192;
  }
  asm volatile("s_waitcnt vmcnt(0)":"+v"(pd)::"memory");
  if(pd==0x12345678 && n==77) sink[threadIdx.x]=pd;
}

extern "C" void kernel_launch(void* const* d_in,const int* in_sizes,int n_in,void* d_out,int out_size,void* d_ws,size_t ws_size,hipStream_t stream){
  const u16* x=(const u16*)d_in[0]; const u16* wqkv=(const u16*)d_in[1]; const u16* wo=(const u16*)d_in[2];
  const u16* wg=(const u16*)d_in[4]; const u16* wu=(const u16*)d_in[5]; const u16* wd=(const u16*)d_in[6];
  char* ws=(char*)d_ws;
  u16* xn=(u16*)ws; u16* qkv=(u16*)(ws+(4<<20)); u16* g=(u16*)(ws+(16<<20)); u16* u=(u16*)(ws+(24<<20)); u16* hh=(u16*)(ws+(32<<20)); u16* o=(u16*)(ws+(40<<20)); float* c=(float*)(ws+(48<<20));
  probe_k<2><<<256,256,0,stream>>>(ws+(64<<20),0,0,(unsigned*)(ws+(200<<20)));
  ln_k<<<S/4,256,0,stream>>>(x,xn);
  gemm_k<0><<<dim3(1920/64,S/64),256,0,stream>>>(xn,D,wqkv,1920,D,qkv,1920,nullptr,0);
  gemm_k<0><<<dim3(F/64,S/64),256,0,stream>>>(xn,D,wg,F,D,g,F,nullptr,0);
  gemm_k<0><<<dim3(F/64,S/64),256,0,stream>>>(xn,D,wu,F,D,u,F,nullptr,0);
  attn_k<<<S*H/256,256,0,stream>>>(qkv,o);
  geglu_k<<<S*F/256,256,0,stream>>>(g,u,hh);
  gemm_k<1><<<dim3(D/64,S/64),256,0,stream>>>(o,D,wo,D,D,nullptr,D,c,1);
  gemm_k<1><<<dim3(D/64,S/64),256,0,stream>>>(hh,F,wd,D,F,nullptr,D,c,0);
  fin_k<<<S*D/256,256,0,stream>>>(x,c,(u16*)d_out);
}
```

```cpp
#include <hip/hip_runtime.h>
#include <hip/hip_bf16.h>
typedef unsigned short u16;
typedef __attribute__((ext_vector_type(8))) short bf16x8;
typedef __attribute__((ext_vector_type(4))) float f32x4;
__device__ __forceinline__ float bf2f(u16 v){ return __uint_as_float(((unsigned)v)<<16); }
__device__ __forceinline__ u16 f2bf(float f){ unsigned u=__float_as_uint(f); return (u16)((u+0x7FFF+((u>>16)&1))>>16); }
constexpr int S=2048,D=640,F=1536,H=32,DH=20;

__global__ void ln_k(const u16* x,u16* xn){
  int row=blockIdx.x*4+(threadIdx.x>>6), lane=threadIdx.x&63;
  float v[10]; float s=0;
  for(int i=0;i<10;i++){ v[i]=bf2f(x[row*D+lane+64*i]); s+=v[i]; }
  for(int o=32;o;o>>=1) s+=__shfl_xor(s,o);
  float m=s/D; float q=0;
  for(int i=0;i<10;i++){ float d=v[i]-m; q+=d*d; }
  for(int o=32;o;o>>=1) q+=__shfl_xor(q,o);
  float r=rsqrtf(q/D+1e-5f);
  for(int i=0;i<10;i++) xn[row*D+lane+64*i]=f2bf((v[i]-m)*r);
}
template<int MODE>
__global__ void gemm_k(const u16* A,int lda,const u16* B,int ldb,int K,u16* C,int ldc,float* Cf,int first){
  __shared__ u16 sA[64][40]; __shared__ u16 sB[64][40];
  int t=threadIdx.x, lane=t&63, w=t>>6, wr=w>>1, wc=w&1;
  int m0=blockIdx.y*64,n0=blockIdx.x*64;
  f32x4 acc[2][2]={};
  for(int k0=0;k0<K;k0+=32){
    { int r=t>>2,c=(t&3)*8; *(bf16x8*)&sA[r][c]=*(const bf16x8*)&A[(m0+r)*lda+k0+c]; }
    { int k=t>>3,c=(t&7)*8; bf16x8 v=*(const bf16x8*)&B[(k0+k)*ldb+n0+c]; for(int j=0;j<8;j++) sB[c+j][k]=v[j]; }
    __syncthreads();
    bf16x8 a[2],b[2];
    for(int i=0;i<2;i++){ a[i]=*(bf16x8*)&sA[wr*32+i*16+(lane&15)][(lane>>4)*8]; b[i]=*(bf16x8*)&sB[wc*32+i*16+(lane&15)][(lane>>4)*8]; }
    for(int i=0;i<2;i++)for(int j=0;j<2;j++) acc[i][j]=__builtin_amdgcn_mfma_f32_16x16x32_bf16(a[i],b[j],acc[i][j],0,0,0);
    __syncthreads();
  }
  for(int i=0;i<2;i++)for(int j=0;j<2;j++)for(int r=0;r<4;r++){
    int row=m0+wr*32+i*16+(lane>>4)*4+r,col=n0+wc*32+j*16+(lane&15);
    if(MODE==0) C[row*ldc+col]=f2bf(acc[i][j][r]);
    else { if(first) Cf[row*ldc+col]=acc[i][j][r]; else Cf[row*ldc+col]+=acc[i][j][r]; }
  }
}
__global__ void attn_k(const u16* qkv,u16* o){
  int id=blockIdx.x*256+threadIdx.x; int s=id/H,h=id%H;
  float q[DH]; for(int d=0;d<DH;d++) q[d]=bf2f(qkv[s*1920+h*DH+d]);
  float l[8]; float mx=-1e30f;
  for(int w=0;w<8;w++){ int t=s-2*w; float a=0; if(t>=0){ for(int d=0;d<DH;d++) a+=q[d]*bf2f(qkv[t*1920+640+h*DH+d]); a*=0.22360679775f; } else a=-1e9f; l[w]=a; mx=fmaxf(mx,a);}
  float sum=0; for(int w=0;w<8;w++){ l[w]=__expf(l[w]-mx); sum+=l[w]; }
  float acc[DH]; for(int d=0;d<DH;d++) acc[d]=0;
  for(int w=0;w<8;w++){ int t=s-2*w; if(t<0) t=0; float p=l[w]/sum; for(int d=0;d<DH;d++) acc[d]+=p*bf2f(qkv[t*1920+1280+h*DH+d]); }
  for(int d=0;d<DH;d++) o[s*D+h*DH+d]=f2bf(acc[d]);
}
__global__ void geglu_k(const u16* g,const u16* u,u16* hh){
  int i=blockIdx.x*256+threadIdx.x; float gv=bf2f(g[i]),uv=bf2f(u[i]);
  float z=0.7978845608f*(gv+0.044715f*gv*gv*gv); float ge=0.5f*gv*(1.f+tanhf(z)); hh[i]=f2bf(ge*uv);
}
__global__ void fin_k(const u16* x,const float* c,u16* out){ int i=blockIdx.x*256+threadIdx.x; out[i]=f2bf(bf2f(x[i])+c[i]); }

typedef __attribute__((ext_vector_type(16))) float f32x16;
__global__ __launch_bounds__(256,1) void probe_k(const u16* x,int n,float* sink){
  int lane=threadIdx.x;
  bf16x8 a[4],b[4];
  for(int i=0;i<4;i++){ a[i]=*(const bf16x8*)(x+(blockIdx.x*256+lane)*8+i*524288/4); b[i]=*(const bf16x8*)(x+(blockIdx.x*256+lane)*8+i*524288/4+65536*8);}
  f32x16 acc[4]; for(int i=0;i<4;i++) for(int j=0;j<16;j++) acc[i][j]=0;
  for(int it=0;it<n;it++){
    #pragma unroll
    for(int m=0;m<16;m++) asm volatile("v_mfma_f32_32x32x16_bf16 %0, %1, %2, %0":"+v"(acc[m&3]):"v"(a[(m>>2)&3]),"v"(b[m&3]));
  }
  float s=0; for(int i=0;i<4;i++) s+=acc[i][0]+acc[i][15];
  if(s==1.2345f) sink[threadIdx.x]=s;
}

extern "C" void kernel_launch(void* const* d_in,const int* in_sizes,int n_in,void* d_out,int out_size,void* d_ws,size_t ws_size,hipStream_t stream){
  const u16* x=(const u16*)d_in[0]; const u16* wqkv=(const u16*)d_in[1]; const u16* wo=(const u16*)d_in[2];
  const u16* wg=(const u16*)d_in[4]; const u16* wu=(const u16*)d_in[5]; const u16* wd=(const u16*)d_in[6];
  char* ws=(char*)d_ws;
  u16* xn=(u16*)ws; u16* qkv=(u16*)(ws+(4<<20)); u16* g=(u16*)(ws+(16<<20)); u16* u=(u16*)(ws+(24<<20)); u16* hh=(u16*)(ws+(32<<20)); u16* o=(u16*)(ws+(40<<20)); float* c=(float*)(ws+(48<<20));
  probe_k<<<256,256,0,stream>>>((const u16*)d_in[3],0,(float*)(ws+(200<<20)));
  ln_k<<<S/4,256,0,stream>>>(x,xn);
  gemm_k<0><<<dim3(1920/64,S/64),256,0,stream>>>(xn,D,wqkv,1920,D,qkv,1920,nullptr,0);
  gemm_k<0><<<dim3(F/64,S/64),256,0,stream>>>(xn,D,wg,F,D,g,F,nullptr,0);
  gemm_k<0><<<dim3(F/64,S/64),256,0,stream>>>(xn,D,wu,F,D,u,F,nullptr,0);
  attn_k<<<S*H/256,256,0,stream>>>(qkv,o);
  geglu_k<<<S*F/256,256,0,stream>>>(g,u,hh);
  gemm_k<1><<<dim3(D/64,S/64),256,0,stream>>>(o,D,wo,D,D,nullptr,D,c,1);
  gemm_k<1><<<dim3(D/64,S/64),256,0,stream>>>(hh,F,wd,D,F,nullptr,D,c,0);
  fin_k<<<S*D/256,256,0,stream>>>(x,c,(u16*)d_out);
}
```

```cpp
#include <hip/hip_runtime.h>
#include <hip/hip_bf16.h>
typedef unsigned short u16;
typedef __attribute__((ext_vector_type(8))) short bf16x8;
typedef __attribute__((ext_vector_type(4))) float f32x4;
__device__ __forceinline__ float bf2f(u16 v){ return __uint_as_float(((unsigned)v)<<16); }
__device__ __forceinline__ u16 f2bf(float f){ unsigned u=__float_as_uint(f); return (u16)((u+0x7FFF+((u>>16)&1))>>16); }
constexpr int S=2048,D=640,F=1536,H=32,DH=20;

__global__ void ln_k(const u16* x,u16* xn){
  int row=blockIdx.x*4+(threadIdx.x>>6), lane=threadIdx.x&63;
  float v[10]; float s=0;
  for(int i=0;i<10;i++){ v[i]=bf2f(x[row*D+lane+64*i]); s+=v[i]; }
  for(int o=32;o;o>>=1) s+=__shfl_xor(s,o);
  float m=s/D; float q=0;
  for(int i=0;i<10;i++){ float d=v[i]-m; q+=d*d; }
  for(int o=32;o;o>>=1) q+=__shfl_xor(q,o);
  float r=rsqrtf(q/D+1e-5f);
  for(int i=0;i<10;i++) xn[row*D+lane+64*i]=f2bf((v[i]-m)*r);
}
template<int MODE>
__global__ void gemm_k(const u16* A,int lda,const u16* B,int ldb,int K,u16* C,int ldc,float* Cf,int first){
  __shared__ u16 sA[64][40]; __shared__ u16 sB[64][40];
  int t=threadIdx.x, lane=t&63, w=t>>6, wr=w>>1, wc=w&1;
  int m0=blockIdx.y*64,n0=blockIdx.x*64;
  f32x4 acc[2][2]={};
  for(int k0=0;k0<K;k0+=32){
    { int r=t>>2,c=(t&3)*8; *(bf16x8*)&sA[r][c]=*(const bf16x8*)&A[(m0+r)*lda+k0+c]; }
    { int k=t>>3,c=(t&7)*8; bf16x8 v=*(const bf16x8*)&B[(k0+k)*ldb+n0+c]; for(int j=0;j<8;j++) sB[c+j][k]=v[j]; }
    __syncthreads();
    bf16x8 a[2],b[2];
    for(int i=0;i<2;i++){ a[i]=*(bf16x8*)&sA[wr*32+i*16+(lane&15)][(lane>>4)*8]; b[i]=*(bf16x8*)&sB[wc*32+i*16+(lane&15)][(lane>>4)*8]; }
    for(int i=0;i<2;i++)for(int j=0;j<2;j++) acc[i][j]=__builtin_amdgcn_mfma_f32_16x16x32_bf16(a[i],b[j],acc[i][j],0,0,0);
    __syncthreads();
  }
  for(int i=0;i<2;i++)for(int j=0;j<2;j++)for(int r=0;r<4;r++){
    int row=m0+wr*32+i*16+(lane>>4)*4+r,col=n0+wc*32+j*16+(lane&15);
    if(MODE==0) C[row*ldc+col]=f2bf(acc[i][j][r]);
    else { if(first) Cf[row*ldc+col]=acc[i][j][r]; else Cf[row*ldc+col]+=acc[i][j][r]; }
  }
}
__global__ void attn_k(const u16* qkv,u16* o){
  int id=blockIdx.x*256+threadIdx.x; int s=id/H,h=id%H;
  float q[DH]; for(int d=0;d<DH;d++) q[d]=bf2f(qkv[s*1920+h*DH+d]);
  float l[8]; float mx=-1e30f;
  for(int w=0;w<8;w++){ int t=s-2*w; float a=0; if(t>=0){ for(int d=0;d<DH;d++) a+=q[d]*bf2f(qkv[t*1920+640+h*DH+d]); a*=0.22360679775f; } else a=-1e9f; l[w]=a; mx=fmaxf(mx,a);}
  float sum=0; for(int w=0;w<8;w++){ l[w]=__expf(l[w]-mx); sum+=l[w]; }
  float acc[DH]; for(int d=0;d<DH;d++) acc[d]=0;
  for(int w=0;w<8;w++){ int t=s-2*w; if(t<0) t=0; float p=l[w]/sum; for(int d=0;d<DH;d++) acc[d]+=p*bf2f(qkv[t*1920+1280+h*DH+d]); }
  for(int d=0;d<DH;d++) o[s*D+h*DH+d]=f2bf(acc[d]);
}
__global__ void geglu_k(const u16* g,const u16* u,u16* hh){
  int i=blockIdx.x*256+threadIdx.x; float gv=bf2f(g[i]),uv=bf2f(u[i]);
  float z=0.7978845608f*(gv+0.044715f*gv*gv*gv); float ge=0.5f*gv*(1.f+tanhf(z)); hh[i]=f2bf(ge*uv);
}
__global__ void fin_k(const u16* x,const float* c,u16* out){ int i=blockIdx.x*256+threadIdx.x; out[i]=f2bf(bf2f(x[i])+c[i]); }

__global__ __launch_bounds__(256,1) void e_small(){ asm volatile("s_nop 0"); }
__global__ __launch_bounds__(256,1) void e_lds(){ __shared__ char lds[163840]; asm volatile("s_nop 0"::"s"((unsigned)(size_t)lds)); }
__global__ __launch_bounds__(256,1) void e_reg(){ asm volatile("s_nop 0":::"v1","v2","v3","v4","v5","v6","v7","v8","v9","v10","v11","v12","v13","v14","v15","v16","v17","v18","v19","v20","v21","v22","v23","v24","v25","v26","v27","v28","v29","v30","v31","v32","v33","v34","v35","v36","v37","v38","v39","v40","v41","v42","v43","v44","v45","v46","v47","v48","v49","v50","v51","v52","v53","v54","v55","v56","v57","v58","v59","v60","v61","v62","v63","v64","v65","v66","v67","v68","v69","v70","v71","v72","v73","v74","v75","v76","v77","v78","v79","v80","v81","v82","v83","v84","v85","v86","v87","v88","v89","v90","v91","v92","v93","v94","v95","v96","v97","v98","v99","v100","v101","v102","v103","v104","v105","v106","v107","v108","v109","v110","v111","v112","v113","v114","v115","v116","v117","v118","v119","v120","v121","v122","v123","v124","v125","v126","v127","v128","v129","v130","v131","v132","v133","v134","v135","v136","v137","v138","v139","v140","v141","v142","v143","v144","v145","v146","v147","v148","v149","v150","v151","v152","v153","v154","v155","v156","v157","v158","v159","v160","v161","v162","v163","v164","v165","v166","v167","v168","v169","v170","v171","v172","v173","v174","v175","v176","v177","v178","v179","v180","v181","v182","v183","v184","v185","v186","v187","v188","v189","v190","v191","v192","v193","v194","v195","v196","v197","v198","v199","v200","v201","v202","v203","v204","v205","v206","v207","v208","v209","v210","v211","v212","v213","v214","v215","v216","v217","v218","v219","v220","v221","v222","v223","v224","v225","v226","v227","v228","v229","v230","v231","v232","v233","v234","v235","v236","v237","v238","v239","v240","v241","v242","v243","v244","v245","v246","v247","v248","v249","v250","v251","v252","v253","v254","v255","a0","a1","a2","a3","a4","a5","a6","a7","a8","a9","a10","a11","a12","a13","a14","a15","a16","a17","a18","a19","a20","a21","a22","a23","a24","a25","a26","a27","a28","a29","a30","a31","a32","a33","a34","a35","a36","a37","a38","a39","a40","a41","a42","a43","a44","a45","a46","a47","a48","a49","a50","a51","a52","a53","a54","a55","a56","a57","a58","a59","a60","a61","a62","a63","a64","a65","a66","a67","a68","a69","a70","a71","a72","a73","a74","a75","a76","a77","a78","a79","a80","a81","a82","a83","a84","a85","a86","a87","a88","a89","a90","a91","a92","a93","a94","a95","a96","a97","a98","a99","a100","a101","a102","a103","a104","a105","a106","a107","a108","a109","a110","a111","a112","a113","a114","a115","a116","a117","a118","a119","a120","a121","a122","a123","a124","a125","a126","a127","a128","a129","a130","a131","a132","a133","a134","a135","a136","a137","a138","a139","a140","a141","a142","a143","a144","a145","a146","a147","a148","a149","a150","a151","a152","a153","a154","a155","a156","a157","a158","a159","a160","a161","a162","a163","a164","a165","a166","a167","a168","a169","a170","a171","a172","a173","a174","a175","a176","a177","a178","a179","a180","a181","a182","a183","a184","a185","a186","a187","a188","a189","a190","a191","a192","a193","a194","a195","a196","a197","a198","a199","a200","a201","a202","a203","a204","a205","a206","a207","a208","a209","a210","a211","a212","a213","a214","a215","a216","a217","a218","a219","a220","a221","a222","a223","a224","a225","a226","a227","a228","a229","a230","a231","a232","a233","a234","a235","a236","a237","a238","a239","a240","a241","a242","a243","a244","a245","a246","a247","a248","a249","a250","a251","a252","a253","a254","a255"); }
__global__ __launch_bounds__(256,1) void e_both(){ __shared__ char lds[163840]; asm volatile("s_nop 0"::"s"((unsigned)(size_t)lds):"v1","v2","v3","v4","v5","v6","v7","v8","v9","v10","v11","v12","v13","v14","v15","v16","v17","v18","v19","v20","v21","v22","v23","v24","v25","v26","v27","v28","v29","v30","v31","v32","v33","v34","v35","v36","v37","v38","v39","v40","v41","v42","v43","v44","v45","v46","v47","v48","v49","v50","v51","v52","v53","v54","v55","v56","v57","v58","v59","v60","v61","v62","v63","v64","v65","v66","v67","v68","v69","v70","v71","v72","v73","v74","v75","v76","v77","v78","v79","v80","v81","v82","v83","v84","v85","v86","v87","v88","v89","v90","v91","v92","v93","v94","v95","v96","v97","v98","v99","v100","v101","v102","v103","v104","v105","v106","v107","v108","v109","v110","v111","v112","v113","v114","v115","v116","v117","v118","v119","v120","v121","v122","v123","v124","v125","v126","v127","v128","v129","v130","v131","v132","v133","v134","v135","v136","v137","v138","v139","v140","v141","v142","v143","v144","v145","v146","v147","v148","v149","v150","v151","v152","v153","v154","v155","v156","v157","v158","v159","v160","v161","v162","v163","v164","v165","v166","v167","v168","v169","v170","v171","v172","v173","v174","v175","v176","v177","v178","v179","v180","v181","v182","v183","v184","v185","v186","v187","v188","v189","v190","v191","v192","v193","v194","v195","v196","v197","v198","v199","v200","v201","v202","v203","v204","v205","v206","v207","v208","v209","v210","v211","v212","v213","v214","v215","v216","v217","v218","v219","v220","v221","v222","v223","v224","v225","v226","v227","v228","v229","v230","v231","v232","v233","v234","v235","v236","v237","v238","v239","v240","v241","v242","v243","v244","v245","v246","v247","v248","v249","v250","v251","v252","v253","v254","v255","a0","a1","a2","a3","a4","a5","a6","a7","a8","a9","a10","a11","a12","a13","a14","a15","a16","a17","a18","a19","a20","a21","a22","a23","a24","a25","a26","a27","a28","a29","a30","a31","a32","a33","a34","a35","a36","a37","a38","a39","a40","a41","a42","a43","a44","a45","a46","a47","a48","a49","a50","a51","a52","a53","a54","a55","a56","a57","a58","a59","a60","a61","a62","a63","a64","a65","a66","a67","a68","a69","a70","a71","a72","a73","a74","a75","a76","a77","a78","a79","a80","a81","a82","a83","a84","a85","a86","a87","a88","a89","a90","a91","a92","a93","a94","a95","a96","a97","a98","a99","a100","a101","a102","a103","a104","a105","a106","a107","a108","a109","a110","a111","a112","a113","a114","a115","a116","a117","a118","a119","a120","a121","a122","a123","a124","a125","a126","a127","a128","a129","a130","a131","a132","a133","a134","a135","a136","a137","a138","a139","a140","a141","a142","a143","a144","a145","a146","a147","a148","a149","a150","a151","a152","a153","a154","a155","a156","a157","a158","a159","a160","a161","a162","a163","a164","a165","a166","a167","a168","a169","a170","a171","a172","a173","a174","a175","a176","a177","a178","a179","a180","a181","a182","a183","a184","a185","a186","a187","a188","a189","a190","a191","a192","a193","a194","a195","a196","a197","a198","a199","a200","a201","a202","a203","a204","a205","a206","a207","a208","a209","a210","a211","a212","a213","a214","a215","a216","a217","a218","a219","a220","a221","a222","a223","a224","a225","a226","a227","a228","a229","a230","a231","a232","a233","a234","a235","a236","a237","a238","a239","a240","a241","a242","a243","a244","a245","a246","a247","a248","a249","a250","a251","a252","a253","a254","a255"); }

extern "C" void kernel_launch(void* const* d_in,const int* in_sizes,int n_in,void* d_out,int out_size,void* d_ws,size_t ws_size,hipStream_t stream){
  const u16* x=(const u16*)d_in[0]; const u16* wqkv=(const u16*)d_in[1]; const u16* wo=(const u16*)d_in[2];
  const u16* wg=(const u16*)d_in[4]; const u16* wu=(const u16*)d_in[5]; const u16* wd=(const u16*)d_in[6];
  char* ws=(char*)d_ws;
  u16* xn=(u16*)ws; u16* qkv=(u16*)(ws+(4<<20)); u16* g=(u16*)(ws+(16<<20)); u16* u=(u16*)(ws+(24<<20)); u16* hh=(u16*)(ws+(32<<20)); u16* o=(u16*)(ws+(40<<20)); float* c=(float*)(ws+(48<<20));
  for(int i=0;i<4;i++) e_lds<<<256,256,0,stream>>>();
  ln_k<<<S/4,256,0,stream>>>(x,xn);
  gemm_k<0><<<dim3(1920/64,S/64),256,0,stream>>>(xn,D,wqkv,1920,D,qkv,1920,nullptr,0);
  gemm_k<0><<<dim3(F/64,S/64),256,0,stream>>>(xn,D,wg,F,D,g,F,nullptr,0);
  gemm_k<0><<<dim3(F/64,S/64),256,0,stream>>>(xn,D,wu,F,D,u,F,nullptr,0);
  attn_k<<<S*H/256,256,0,stream>>>(qkv,o);
  geglu_k<<<S*F/256,256,0,stream>>>(g,u,hh);
  gemm_k<1><<<dim3(D/64,S/64),256,0,stream>>>(o,D,wo,D,D,nullptr,D,c,1);
  gemm_k<1><<<dim3(D/64,S/64),256,0,stream>>>(hh,F,wd,D,F,nullptr,D,c,0);
  fin_k<<<S*D/256,256,0,stream>>>(x,c,(u16*)d_out);
}
```

```cpp
#include <hip/hip_runtime.h>
#include <hip/hip_bf16.h>
typedef unsigned short u16;
typedef __attribute__((ext_vector_type(8))) short bf16x8;
typedef __attribute__((ext_vector_type(4))) float f32x4;
__device__ __forceinline__ float bf2f(u16 v){ return __uint_as_float(((unsigned)v)<<16); }
__device__ __forceinline__ u16 f2bf(float f){ unsigned u=__float_as_uint(f); return (u16)((u+0x7FFF+((u>>16)&1))>>16); }
constexpr int S=2048,D=640,F=1536,H=32,DH=20;

__global__ void ln_k(const u16* x,u16* xn){
  int row=blockIdx.x*4+(threadIdx.x>>6), lane=threadIdx.x&63;
  float v[10]; float s=0;
  for(int i=0;i<10;i++){ v[i]=bf2f(x[row*D+lane+64*i]); s+=v[i]; }
  for(int o=32;o;o>>=1) s+=__shfl_xor(s,o);
  float m=s/D; float q=0;
  for(int i=0;i<10;i++){ float d=v[i]-m; q+=d*d; }
  for(int o=32;o;o>>=1) q+=__shfl_xor(q,o);
  float r=rsqrtf(q/D+1e-5f);
  for(int i=0;i<10;i++) xn[row*D+lane+64*i]=f2bf((v[i]-m)*r);
}
template<int MODE>
__global__ void gemm_k(const u16* A,int lda,const u16* B,int ldb,int K,u16* C,int ldc,float* Cf,int first){
  __shared__ u16 sA[64][40]; __shared__ u16 sB[64][40];
  int t=threadIdx.x, lane=t&63, w=t>>6, wr=w>>1, wc=w&1;
  int m0=blockIdx.y*64,n0=blockIdx.x*64;
  f32x4 acc[2][2]={};
  for(int k0=0;k0<K;k0+=32){
    { int r=t>>2,c=(t&3)*8; *(bf16x8*)&sA[r][c]=*(const bf16x8*)&A[(m0+r)*lda+k0+c]; }
    { int k=t>>3,c=(t&7)*8; bf16x8 v=*(const bf16x8*)&B[(k0+k)*ldb+n0+c]; for(int j=0;j<8;j++) sB[c+j][k]=v[j]; }
    __syncthreads();
    bf16x8 a[2],b[2];
    for(int i=0;i<2;i++){ a[i]=*(bf16x8*)&sA[wr*32+i*16+(lane&15)][(lane>>4)*8]; b[i]=*(bf16x8*)&sB[wc*32+i*16+(lane&15)][(lane>>4)*8]; }
    for(int i=0;i<2;i++)for(int j=0;j<2;j++) acc[i][j]=__builtin_amdgcn_mfma_f32_16x16x32_bf16(a[i],b[j],acc[i][j],0,0,0);
    __syncthreads();
  }
  for(int i=0;i<2;i++)for(int j=0;j<2;j++)for(int r=0;r<4;r++){
    int row=m0+wr*32+i*16+(lane>>4)*4+r,col=n0+wc*32+j*16+(lane&15);
    if(MODE==0) C[row*ldc+col]=f2bf(acc[i][j][r]);
    else { if(first) Cf[row*ldc+col]=acc[i][j][r]; else Cf[row*ldc+col]+=acc[i][j][r]; }
  }
}
__global__ void attn_k(const u16* qkv,u16* o){
  int id=blockIdx.x*256+threadIdx.x; int s=id/H,h=id%H;
  float q[DH]; for(int d=0;d<DH;d++) q[d]=bf2f(qkv[s*1920+h*DH+d]);
  float l[8]; float mx=-1e30f;
  for(int w=0;w<8;w++){ int t=s-2*w; float a=0; if(t>=0){ for(int d=0;d<DH;d++) a+=q[d]*bf2f(qkv[t*1920+640+h*DH+d]); a*=0.22360679775f; } else a=-1e9f; l[w]=a; mx=fmaxf(mx,a);}
  float sum=0; for(int w=0;w<8;w++){ l[w]=__expf(l[w]-mx); sum+=l[w]; }
  float acc[DH]; for(int d=0;d<DH;d++) acc[d]=0;
  for(int w=0;w<8;w++){ int t=s-2*w; if(t<0) t=0; float p=l[w]/sum; for(int d=0;d<DH;d++) acc[d]+=p*bf2f(qkv[t*1920+1280+h*DH+d]); }
  for(int d=0;d<DH;d++) o[s*D+h*DH+d]=f2bf(acc[d]);
}
__global__ void geglu_k(const u16* g,const u16* u,u16* hh){
  int i=blockIdx.x*256+threadIdx.x; float gv=bf2f(g[i]),uv=bf2f(u[i]);
  float z=0.7978845608f*(gv+0.044715f*gv*gv*gv); float ge=0.5f*gv*(1.f+tanhf(z)); hh[i]=f2bf(ge*uv);
}
__global__ void fin_k(const u16* x,const float* c,u16* out){ int i=blockIdx.x*256+threadIdx.x; out[i]=f2bf(bf2f(x[i])+c[i]); }

__global__ void st_k(unsigned long long* p){ unsigned long long t0; asm volatile("s_memrealtime %0\n\ts_waitcnt lgkmcnt(0)":"=s"(t0)::"memory"); if(threadIdx.x==0) p[blockIdx.x]=t0; }
__global__ void en_k(const unsigned long long* p,int ticks){ unsigned long long t0=p[0],t; do { asm volatile("s_memrealtime %0\n\ts_waitcnt lgkmcnt(0)":"=s"(t)::"memory"); } while(t-t0<(unsigned long long)ticks); }

extern "C" void kernel_launch(void* const* d_in,const int* in_sizes,int n_in,void* d_out,int out_size,void* d_ws,size_t ws_size,hipStream_t stream){
  const u16* x=(const u16*)d_in[0]; const u16* wqkv=(const u16*)d_in[1]; const u16* wo=(const u16*)d_in[2];
  const u16* wg=(const u16*)d_in[4]; const u16* wu=(const u16*)d_in[5]; const u16* wd=(const u16*)d_in[6];
  char* ws=(char*)d_ws;
  u16* xn=(u16*)ws; u16* qkv=(u16*)(ws+(4<<20)); u16* g=(u16*)(ws+(16<<20)); u16* u=(u16*)(ws+(24<<20)); u16* hh=(u16*)(ws+(32<<20)); u16* o=(u16*)(ws+(40<<20)); float* c=(float*)(ws+(48<<20));
  st_k<<<1,256,0,stream>>>((unsigned long long*)(ws+(200<<20)));
  ln_k<<<S/4,256,0,stream>>>(x,xn);
  gemm_k<0><<<dim3(1920/64,S/64),256,0,stream>>>(xn,D,wqkv,1920,D,qkv,1920,nullptr,0);
  gemm_k<0><<<dim3(F/64,S/64),256,0,stream>>>(xn,D,wg,F,D,g,F,nullptr,0);
  gemm_k<0><<<dim3(F/64,S/64),256,0,stream>>>(xn,D,wu,F,D,u,F,nullptr,0);
  attn_k<<<S*H/256,256,0,stream>>>(qkv,o);
  geglu_k<<<S*F/256,256,0,stream>>>(g,u,hh);
  gemm_k<1><<<dim3(D/64,S/64),256,0,stream>>>(o,D,wo,D,D,nullptr,D,c,1);
  gemm_k<1><<<dim3(D/64,S/64),256,0,stream>>>(hh,F,wd,D,F,nullptr,D,c,0);
  fin_k<<<S*D/256,256,0,stream>>>(x,c,(u16*)d_out);
  en_k<<<1,256,0,stream>>>((unsigned long long*)(ws+(200<<20)),25000);
}
```

```cpp
#include <hip/hip_runtime.h>
struct KArgs { const void* p[8]; };
__global__ __launch_bounds__(256,1) void fused_k(KArgs) {
  __shared__ __attribute__((aligned(16))) char lds[163840];
  asm volatile(
  ".p2align 3\n"
  "s_mov_b64 s[4:5], s[0:1]\n"
  "s_mov_b32 s6, s2\n"
  "v_mov_b32_e64 v1, v0\n"
  "s_load_dwordx16 s[8:23], s[4:5], 0x0\n"
  ".Lcode_start:\n"
  "s_getpc_b64 s[2:3]\n"
  "s_lshr_b32 s7, s6, 3\n"
  "s_lshl_b32 s7, s7, 11\n"
  "v_lshlrev_b32 v2, 7, v1\n"
  "v_add_u32 v2, s7, v2\n"
  "v_cmp_gt_u32 vcc, 16, v1\n"
  "s_nop 0\n"
  "s_and_saveexec_b64 s[0:1], vcc\n"
  "s_mov_b32 s7, .Lcode_end-.Lcode_start-128\n"
  "v_cmp_gt_u32 vcc, s7, v2\n"
  "s_nop 0\n"
  "s_and_b64 exec, exec, vcc\n"
  "s_cbranch_execz .Lnopf\n"
  "global_load_dword v63, v2, s[2:3]\n"
  ".Lnopf:\n"
  "s_mov_b64 exec, s[0:1]\n"
  "v_lshrrev_b32 v2, 6, v1\n"
  "v_and_b32 v0, 63, v1\n"
  "s_nop 0\n"
  "v_readfirstlane_b32 s46, v2\n"
  "s_mov_b32 s50, s6\n"
  "s_and_b32 s66, s6, 7\n"
  "s_lshr_b32 s67, s6, 3\n"
  "s_lshr_b32 s68, s66, 2\n"
  "s_and_b32 s69, s66, 3\n"
  "s_lshl_b32 s47, s68, 3\n"
  "s_lshr_b32 s70, s67, 2\n"
  "s_add_u32 s47, s47, s70\n"
  "s_and_b32 s49, s67, 1\n"
  "s_bfe_u32 s71, s67, 0x10001\n"
  "s_lshl_b32 s48, s69, 1\n"
  "s_add_u32 s48, s48, s71\n"
  "s_mov_b32 s2, 0xf3f1f020\n"
  "s_cmp_eq_u32 s68, 0\n"
  "s_nop 0\n"
  "s_cselect_b32 s2, s2, 0xf3f1f020\n"
  "s_lshl_b32 s3, s70, 2\n"
  "s_lshr_b32 s2, s2, s3\n"
  "s_and_b32 s51, s2, 15\n"
  "s_and_b32 s70, s70, 3\n"
  "s_lshl_b32 s71, s68, 2\n"
  "s_add_u32 s70, s70, s71\n"
  "s_mov_b32 s2, 0x75643120\n"
  "s_mov_b32 s3, 0x98\n"
  "s_cmp_eq_u32 s70, 1\n"
  "s_nop 0\n"
  "s_cselect_b32 s2, 0x67542310, s2\n"
  "s_cselect_b32 s3, 0x98, s3\n"
  "s_cmp_eq_u32 s70, 2\n"
  "s_nop 0\n"
  "s_cselect_b32 s2, 0x86534201, s2\n"
  "s_cselect_b32 s3, 0x97, s3\n"
  "s_cmp_eq_u32 s70, 3\n"
  "s_nop 0\n"
  "s_cselect_b32 s2, 0x76453201, s2\n"
  "s_cselect_b32 s3, 0x89, s3\n"
  "s_cmp_eq_u32 s70, 4\n"
  "s_nop 0\n"
  "s_cselect_b32 s2, 0x20198675, s2\n"
  "s_cselect_b32 s3, 0x43, s3\n"
  "s_cmp_eq_u32 s70, 5\n"
  "s_nop 0\n"
  "s_cselect_b32 s2, 0x12097865, s2\n"
  "s_cselect_b32 s3, 0x43, s3\n"
  "s_cmp_eq_u32 s70, 6\n"
  "s_nop 0\n"
  "s_cselect_b32 s2, 0x31089756, s2\n"
  "s_cselect_b32 s3, 0x42, s3\n"
  "s_cmp_eq_u32 s70, 7\n"
  "s_nop 0\n"
  "s_cselect_b32 s2, 0x21908756, s2\n"
  "s_cselect_b32 s3, 0x34, s3\n"
  "s_nop 0\n"
  "s_bfe_u32 s52, s2, 0x40000\n"
  "s_lshl_b32 s52, s52, 7\n"
  "s_nop 0\n"
  "s_bfe_u32 s53, s2, 0x40004\n"
  "s_lshl_b32 s53, s53, 7\n"
  "s_nop 0\n"
  "s_bfe_u32 s54, s2, 0x40008\n"
  "s_lshl_b32 s54, s54, 7\n"
  "s_nop 0\n"
  "s_bfe_u32 s55, s2, 0x4000c\n"
  "s_lshl_b32 s55, s55, 7\n"
  "s_nop 0\n"
  "s_bfe_u32 s56, s2, 0x40010\n"
  "s_lshl_b32 s56, s56, 7\n"
  "s_nop 0\n"
  "s_bfe_u32 s57, s2, 0x40014\n"
  "s_lshl_b32 s57, s57, 7\n"
  "s_nop 0\n"
  "s_bfe_u32 s58, s2, 0x40018\n"
  "s_lshl_b32 s58, s58, 7\n"
  "s_nop 0\n"
  "s_bfe_u32 s59, s2, 0x4001c\n"
  "s_lshl_b32 s59, s59, 7\n"
  "s_nop 0\n"
  "s_bfe_u32 s60, s3, 0x40000\n"
  "s_lshl_b32 s60, s60, 7\n"
  "s_nop 0\n"
  "s_bfe_u32 s61, s3, 0x40004\n"
  "s_lshl_b32 s61, s61, 7\n"
  "s_lshl_b32 s64, s46, 10\n"
  "s_lshr_b32 s78, s66, 1\n"
  "s_lshl_b32 s78, s78, 3\n"
  "s_lshr_b32 s2, s67, 2\n"
  "s_add_u32 s78, s78, s2\n"
  "s_and_b32 s79, s66, 1\n"
  "s_lshl_b32 s79, s79, 2\n"
  "s_and_b32 s2, s67, 3\n"
  "s_add_u32 s79, s79, s2\n"
  "s_and_b32 s2, s67, 3\n"
  "s_lshl_b32 s2, s2, 0\n"
  "s_mul_i32 s2, s2, 3\n"
  "s_lshr_b32 s2, s2, 1\n"
  "s_add_u32 s3, s2, 0\n"
  "s_cmp_ge_u32 s3, 6\n"
  "s_cselect_b32 s7, 6, 0\n"
  "s_sub_u32 s3, s3, s7\n"
  "s_lshl_b32 s81, s3, 11\n"
  "s_add_u32 s3, s2, 1\n"
  "s_cmp_ge_u32 s3, 6\n"
  "s_cselect_b32 s7, 6, 0\n"
  "s_sub_u32 s3, s3, s7\n"
  "s_lshl_b32 s82, s3, 11\n"
  "s_add_u32 s3, s2, 2\n"
  "s_cmp_ge_u32 s3, 6\n"
  "s_cselect_b32 s7, 6, 0\n"
  "s_sub_u32 s3, s3, s7\n"
  "s_lshl_b32 s83, s3, 11\n"
  "s_add_u32 s3, s2, 3\n"
  "s_cmp_ge_u32 s3, 6\n"
  "s_cselect_b32 s7, 6, 0\n"
  "s_sub_u32 s3, s3, s7\n"
  "s_lshl_b32 s84, s3, 11\n"
  "s_add_u32 s3, s2, 4\n"
  "s_cmp_ge_u32 s3, 6\n"
  "s_cselect_b32 s7, 6, 0\n"
  "s_sub_u32 s3, s3, s7\n"
  "s_lshl_b32 s85, s3, 11\n"
  "s_add_u32 s3, s2, 5\n"
  "s_cmp_ge_u32 s3, 6\n"
  "s_cselect_b32 s7, 6, 0\n"
  "s_sub_u32 s3, s3, s7\n"
  "s_lshl_b32 s86, s3, 11\n"
  "s_mul_i32 s80, s79, 68\n"
  "s_mul_i32 s2, s46, 17\n"
  "s_add_u32 s80, s80, s2\n"
  "s_mul_i32 s80, s80, 5120\n"
  "s_add_u32 s80, s80, 262144\n"
  "s_cmp_eq_u32 s46, 0\n"
  "s_cselect_b32 s88, 1, 0\n"
  "s_mov_b32 s89, 0\n"
  "s_cmp_eq_u32 s49, 0\n"
  "s_cbranch_scc1 .LQ\n"
  "v_lshrrev_b32 v20, 3, v0\n"
  "v_and_b32 v21, 7, v0\n"
  "v_lshrrev_b32 v22, 4, v0\n"
  "v_and_b32 v23, 15, v0\n"
  "s_and_b32 s66, s46, 1\n"
  "s_lshl_b32 s66, s66, 1\n"
  "v_and_b32 v24, 1, v22\n"
  "v_add_u32 v24, s66, v24\n"
  "v_lshlrev_b32 v24, 1, v24\n"
  "v_xor_b32 v24, v24, v21\n"
  "v_lshlrev_b32 v24, 4, v24\n"
  "s_lshl_b32 s66, s46, 3\n"
  "v_add_u32 v25, s66, v20\n"
  "s_movk_i32 s67, 3072\n"
  "s_nop 0\n"
  "v_mad_u32_u24 v2, v25, s67, v24\n"
  "s_movk_i32 s67, 1280\n"
  "s_lshl_b32 s66, s46, 5\n"
  "s_lshl_b32 s68, s47, 7\n"
  "s_add_u32 s66, s66, s68\n"
  "v_lshlrev_b32 v24, 1, v20\n"
  "v_add_u32 v24, 0, v24\n"
  "v_add_u32 v24, s66, v24\n"
  "v_xor_b32 v25, 0, v22\n"
  "v_xor_b32 v25, v25, v21\n"
  "v_lshlrev_b32 v25, 4, v25\n"
  "v_mad_u32_u24 v6, v24, s67, v25\n"
  "v_lshlrev_b32 v24, 1, v20\n"
  "v_add_u32 v24, 16, v24\n"
  "v_add_u32 v24, s66, v24\n"
  "v_xor_b32 v25, 4, v22\n"
  "v_xor_b32 v25, v25, v21\n"
  "v_lshlrev_b32 v25, 4, v25\n"
  "v_mad_u32_u24 v7, v24, s67, v25\n"
  "v_lshlrev_b32 v24, 1, v20\n"
  "v_add_u32 v24, 1, v24\n"
  "v_add_u32 v24, s66, v24\n"
  "v_xor_b32 v25, 0, v22\n"
  "v_xor_b32 v25, v25, v21\n"
  "v_lshlrev_b32 v25, 4, v25\n"
  "v_mad_u32_u24 v8, v24, s67, v25\n"
  "v_lshlrev_b32 v24, 1, v20\n"
  "v_add_u32 v24, 17, v24\n"
  "v_add_u32 v24, s66, v24\n"
  "v_xor_b32 v25, 4, v22\n"
  "v_xor_b32 v25, v25, v21\n"
  "v_lshlrev_b32 v25, 4, v25\n"
  "v_mad_u32_u24 v9, v24, s67, v25\n"
  "s_lshl_b32 s66, s50, 3\n"
  "s_lshl_b32 s67, s46, 1\n"
  "s_add_u32 s66, s66, s67\n"
  "v_lshrrev_b32 v46, 5, v0\n"
  "v_add_u32 v46, s66, v46\n"
  "v_and_b32 v44, 31, v0\n"
  "v_cmp_gt_u32 vcc, 16, v44\n"
  "v_lshlrev_b32 v44, 4, v44\n"
  "s_movk_i32 s67, 1280\n"
  "s_nop 0\n"
  "v_mad_u32_u24 v44, v46, s67, v44\n"
  "v_mov_b32 v45, 0x7fff0000\n"
  "v_cndmask_b32 v45, v45, v44, vcc\n"
  "s_waitcnt lgkmcnt(0)\n"
  "s_load_dwordx2 s[44:45], s[22:23], 0x0\n"
  "s_mov_b32 s24, s8\n"
  "s_nop 0\n"
  "s_and_b32 s25, s9, 0xffff\n"
  "s_mov_b32 s26, 2621440\n"
  "s_mov_b32 s27, 0x20000\n"
  "buffer_load_dwordx4 v[32:35], v44, s[24:27], 0 offen\n"
  "buffer_load_dwordx4 v[36:39], v44, s[24:27], 0 offen offset:512\n"
  "buffer_load_dwordx4 v[40:43], v45, s[24:27], 0 offen offset:1024\n"
  "s_mul_i32 s66, s48, 384\n"
  "s_mov_b32 s28, s14\n"
  "s_nop 0\n"
  "s_and_b32 s29, s15, 0xffff\n"
  "s_mov_b32 s30, 1966080\n"
  "s_mov_b32 s31, 0x20000\n"
  "s_add_u32 s28, s28, s66\n"
  "s_addc_u32 s29, s29, 0\n"
  "s_mov_b32 s32, s16\n"
  "s_nop 0\n"
  "s_and_b32 s33, s17, 0xffff\n"
  "s_mov_b32 s34, 1966080\n"
  "s_mov_b32 s35, 0x20000\n"
  "s_add_u32 s32, s32, s66\n"
  "s_addc_u32 s33, s33, 0\n"
  "s_lshl_b32 s65, s46, 12\n"
  "s_nop 0\n"
  "s_add_u32 s65, s65, 98304\n"
  "s_mul_i32 s62, s52, 1536\n"
  "s_add_u32 m0, s64, 0\n"
  "s_nop 0\n"
  "buffer_load_dwordx4 v2, s[28:31], s62 offen lds\n"
  "s_add_u32 m0, s64, 4096\n"
  "s_nop 0\n"
  "s_nop 0\n"
  "buffer_load_dwordx4 v2, s[32:35], s62 offen lds\n"
  "s_add_u32 m0, s64, 8064\n"
  "s_nop 0\n"
  "s_nop 0\n"
  "buffer_load_dwordx4 v2, s[28:31], s62 offen offset:128 lds\n"
  "s_add_u32 m0, s64, 12160\n"
  "s_nop 0\n"
  "s_nop 0\n"
  "buffer_load_dwordx4 v2, s[32:35], s62 offen offset:128 lds\n"
  "s_add_u32 m0, s64, 16128\n"
  "s_nop 0\n"
  "s_nop 0\n"
  "buffer_load_dwordx4 v2, s[28:31], s62 offen offset:256 lds\n"
  "s_add_u32 m0, s64, 20224\n"
  "s_nop 0\n"
  "s_nop 0\n"
  "buffer_load_dwordx4 v2, s[32:35], s62 offen offset:256 lds\n"
  "s_add_u32 m0, s65, 0\n"
  "s_nop 0\n"
  "buffer_load_dwordx4 v6, s[24:27], s52 offen lds\n"
  "s_add_u32 m0, s65, 1024\n"
  "s_nop 0\n"
  "s_nop 0\n"
  "buffer_load_dwordx4 v7, s[24:27], s52 offen lds\n"
  "s_add_u32 m0, s65, 2048\n"
  "s_nop 0\n"
  "s_nop 0\n"
  "buffer_load_dwordx4 v8, s[24:27], s52 offen lds\n"
  "s_add_u32 m0, s65, 3072\n"
  "s_nop 0\n"
  "s_nop 0\n"
  "buffer_load_dwordx4 v9, s[24:27], s52 offen lds\n"
  "s_add_u32 s63, s62, 98304\n"
  "s_add_u32 m0, s64, 24576\n"
  "s_nop 0\n"
  "s_nop 0\n"
  "buffer_load_dwordx4 v2, s[28:31], s63 offen lds\n"
  "s_add_u32 m0, s64, 28672\n"
  "s_nop 0\n"
  "s_nop 0\n"
  "buffer_load_dwordx4 v2, s[32:35], s63 offen lds\n"
  "s_add_u32 m0, s64, 32640\n"
  "s_nop 0\n"
  "s_nop 0\n"
  "buffer_load_dwordx4 v2, s[28:31], s63 offen offset:128 lds\n"
  "s_add_u32 m0, s64, 36736\n"
  "s_nop 0\n"
  "s_nop 0\n"
  "buffer_load_dwordx4 v2, s[32:35], s63 offen offset:128 lds\n"
  "s_add_u32 m0, s64, 40704\n"
  "s_nop 0\n"
  "s_nop 0\n"
  "buffer_load_dwordx4 v2, s[28:31], s63 offen offset:256 lds\n"
  "s_add_u32 m0, s64, 44800\n"
  "s_nop 0\n"
  "s_nop 0\n"
  "buffer_load_dwordx4 v2, s[32:35], s63 offen offset:256 lds\n"
  "s_mov_b32 s36, s22\n"
  "s_nop 0\n"
  "s_and_b32 s37, s23, 0xffff\n"
  "s_mov_b32 s38, 16777216\n"
  "s_mov_b32 s39, 0x20000\n"
  "s_waitcnt vmcnt(16)\n"
  "v_mov_b32 v24, 0\n"
  "v_mov_b32 v25, 0\n"
  "v_mov_b32 v26, 0\n"
  "v_mov_b32_e64 v27, 0\n"
  "v_mov_b32 v28, 0x3f803f80\n"
  "v_dot2c_f32_bf16 v24, v28, v32\n"
  "v_dot2c_f32_bf16 v26, v32, v32\n"
  "v_dot2c_f32_bf16 v25, v28, v33\n"
  "v_dot2c_f32_bf16 v27, v33, v33\n"
  "v_dot2c_f32_bf16 v24, v28, v34\n"
  "v_dot2c_f32_bf16 v26, v34, v34\n"
  "v_dot2c_f32_bf16 v25, v28, v35\n"
  "v_dot2c_f32_bf16 v27, v35, v35\n"
  "v_dot2c_f32_bf16 v24, v28, v36\n"
  "v_dot2c_f32_bf16 v26, v36, v36\n"
  "v_dot2c_f32_bf16 v25, v28, v37\n"
  "v_dot2c_f32_bf16 v27, v37, v37\n"
  "v_dot2c_f32_bf16 v24, v28, v38\n"
  "v_dot2c_f32_bf16 v26, v38, v38\n"
  "v_dot2c_f32_bf16 v25, v28, v39\n"
  "v_dot2c_f32_bf16 v27, v39, v39\n"
  "v_dot2c_f32_bf16 v24, v28, v40\n"
  "v_dot2c_f32_bf16 v26, v40, v40\n"
  "v_dot2c_f32_bf16 v25, v28, v41\n"
  "v_dot2c_f32_bf16 v27, v41, v41\n"
  "v_dot2c_f32_bf16 v24, v28, v42\n"
  "v_dot2c_f32_bf16 v26, v42, v42\n"
  "v_dot2c_f32_bf16 v25, v28, v43\n"
  "v_dot2c_f32_bf16 v27, v43, v43\n"
  "s_nop 1\n"
  "v_add_f32 v24, v24, v25\n"
  "v_add_f32 v26, v26, v27\n"
  "s_nop 0\n"
  "v_add_f32_dpp v24, v24, v24 row_ror:8 row_mask:0xf bank_mask:0xf\n"
  "v_add_f32_dpp v26, v26, v26 row_ror:8 row_mask:0xf bank_mask:0xf\n"
  "s_nop 0\n"
  "s_nop 0\n"
  "v_add_f32_dpp v24, v24, v24 row_ror:4 row_mask:0xf bank_mask:0xf\n"
  "v_add_f32_dpp v26, v26, v26 row_ror:4 row_mask:0xf bank_mask:0xf\n"
  "s_nop 0\n"
  "s_nop 0\n"
  "v_add_f32_dpp v24, v24, v24 row_ror:2 row_mask:0xf bank_mask:0xf\n"
  "v_add_f32_dpp v26, v26, v26 row_ror:2 row_mask:0xf bank_mask:0xf\n"
  "s_nop 0\n"
  "s_nop 0\n"
  "v_add_f32_dpp v24, v24, v24 row_ror:1 row_mask:0xf bank_mask:0xf\n"
  "v_add_f32_dpp v26, v26, v26 row_ror:1 row_mask:0xf bank_mask:0xf\n"
  "v_mov_b32 v25, v24\n"
  "v_mov_b32 v27, v26\n"
  "s_nop 0\n"
  "v_permlane16_swap_b32 v24, v25\n"
  "v_permlane16_swap_b32 v26, v27\n"
  "v_add_f32 v24, v24, v25\n"
  "v_add_f32_e64 v26, v26, v27\n"
  "v_mul_f32 v24, 0x3acccccd, v24\n"
  "v_mul_f32 v26, 0x3acccccd, v26\n"
  "v_fma_f32 v26, -v24, v24, v26\n"
  "v_max_f32_e64 v26, 0, v26\n"
  "v_add_f32 v26, 0x3727c5ac, v26\n"
  "v_rsq_f32_e64 v25, v26\n"
  "v_and_b32 v27, 0xffffffe0, v46\n"
  "v_and_b32_e64 v28, 1, v46\n"
  "v_lshl_add_u32 v27, v28, 4, v27\n"
  "v_bfe_u32 v28, v46, 1, 4\n"
  "v_add_u32 v27, v27, v28\n"
  "v_lshlrev_b32 v27, 3, v27\n"
  "s_mov_b32 s66, 32768\n"
  "s_mov_b32 exec_lo, 1\n"
  "s_mov_b32 exec_hi, 1\n"
  "buffer_store_dwordx2 v[24:25], v27, s[36:39], s66 offen sc1\n"
  "s_mov_b64 exec, -1\n"
  "s_nop 0\n"
  "s_add_u32 m0, s65, 16384\n"
  "s_nop 0\n"
  "s_nop 0\n"
  "buffer_load_dwordx4 v6, s[24:27], s53 offen lds\n"
  "s_add_u32 m0, s65, 17408\n"
  "s_nop 0\n"
  "s_nop 0\n"
  "buffer_load_dwordx4 v7, s[24:27], s53 offen lds\n"
  "s_add_u32 m0, s65, 18432\n"
  "s_nop 0\n"
  "s_nop 0\n"
  "buffer_load_dwordx4 v8, s[24:27], s53 offen lds\n"
  "s_add_u32 m0, s65, 19456\n"
  "s_nop 0\n"
  "s_nop 0\n"
  "buffer_load_dwordx4 v9, s[24:27], s53 offen lds\n"
  "s_mul_i32 s62, s53, 1536\n"
  "s_add_u32 m0, s64, 49152\n"
  "s_nop 0\n"
  "s_nop 0\n"
  "buffer_load_dwordx4 v2, s[28:31], s62 offen lds\n"
  "s_add_u32 m0, s64, 53248\n"
  "s_nop 0\n"
  "s_nop 0\n"
  "buffer_load_dwordx4 v2, s[32:35], s62 offen lds\n"
  "s_add_u32 m0, s64, 57216\n"
  "s_nop 0\n"
  "s_nop 0\n"
  "buffer_load_dwordx4 v2, s[28:31], s62 offen offset:128 lds\n"
  "s_add_u32 m0, s64, 61312\n"
  "s_nop 0\n"
  "s_nop 0\n"
  "buffer_load_dwordx4 v2, s[32:35], s62 offen offset:128 lds\n"
  "s_add_u32 m0, s64, 65280\n"
  "s_nop 0\n"
  "s_nop 0\n"
  "buffer_load_dwordx4 v2, s[28:31], s62 offen offset:256 lds\n"
  "s_add_u32 m0, s64, 69376\n"
  "s_nop 0\n"
  "s_nop 0\n"
  "buffer_load_dwordx4 v2, s[32:35], s62 offen offset:256 lds\n"
  "s_add_u32 s63, s62, 98304\n"
  "s_add_u32 m0, s64, 73728\n"
  "s_nop 0\n"
  "s_nop 0\n"
  "buffer_load_dwordx4 v2, s[28:31], s63 offen lds\n"
  "s_add_u32 m0, s64, 77824\n"
  "s_nop 0\n"
  "s_nop 0\n"
  "buffer_load_dwordx4 v2, s[32:35], s63 offen lds\n"
  "s_add_u32 m0, s64, 81792\n"
  "s_nop 0\n"
  "s_nop 0\n"
  "buffer_load_dwordx4 v2, s[28:31], s63 offen offset:128 lds\n"
  "s_add_u32 m0, s64, 85888\n"
  "s_nop 0\n"
  "s_nop 0\n"
  "buffer_load_dwordx4 v2, s[32:35], s63 offen offset:128 lds\n"
  "s_add_u32 m0, s64, 89856\n"
  "s_nop 0\n"
  "s_nop 0\n"
  "buffer_load_dwordx4 v2, s[28:31], s63 offen offset:256 lds\n"
  "s_add_u32 m0, s64, 93952\n"
  "s_nop 0\n"
  "s_nop 0\n"
  "buffer_load_dwordx4 v2, s[32:35], s63 offen offset:256 lds\n"
  "s_add_u32 m0, s65, 32768\n"
  "s_nop 0\n"
  "s_nop 0\n"
  "buffer_load_dwordx4 v6, s[24:27], s54 offen lds\n"
  "s_add_u32 m0, s65, 33792\n"
  "s_nop 0\n"
  "s_nop 0\n"
  "buffer_load_dwordx4 v7, s[24:27], s54 offen lds\n"
  "s_lshr_b32 s66, s50, 4\n"
  "s_lshl_b32 s66, s66, 3\n"
  "s_and_b32 s67, s50, 7\n"
  "s_or_b32 s66, s66, s67\n"
  "s_mul_i32 s66, s66, 3\n"
  "s_add_u32 s66, s66, s46\n"
  "s_cmp_lt_u32 s46, 3\n"
  "s_nop 0\n"
  "s_cselect_b32 s66, s66, 0x1000\n"
  "s_cmp_lt_u32 s66, 544\n"
  "s_cselect_b32 s90, -1, 0\n"
  "s_cselect_b32 s91, -1, 0\n"
  "s_cselect_b32 s66, s66, 0\n"
  "s_and_b32 s67, s66, 7\n"
  "s_lshr_b32 s68, s66, 3\n"
  "s_nop 0\n"
  "s_mul_i32 s69, s68, 241\n"
  "s_lshr_b32 s69, s69, 12\n"
  "s_mul_i32 s70, s69, 17\n"
  "s_sub_u32 s70, s68, s70\n"
  "s_nop 0\n"
  "s_mul_i32 s76, s67, 68\n"
  "s_add_u32 s76, s76, s68\n"
  "s_nop 0\n"
  "s_mul_i32 s76, s76, 5120\n"
  "s_add_u32 s76, s76, 262144\n"
  "s_mul_i32 s2, s69, 12\n"
  "s_add_u32 s2, s2, s70\n"
  "s_sub_u32 s2, s2, 5\n"
  "s_lshl_b32 s2, s2, 5\n"
  "s_mov_b32 s3, 16\n"
  "s_mov_b32 s6, 8\n"
  "s_mov_b32 s7, 4\n"
  "s_nop 0\n"
  "s_mul_i32 s4, s69, 160\n"
  "s_mul_i32 s5, s70, 40\n"
  "s_add_u32 s5, s5, s4\n"
  "s_add_u32 s4, s4, 16\n"
  "s_cmp_lt_u32 s70, 4\n"
  "s_cselect_b32 s2, s5, s2\n"
  "s_cselect_b32 s3, 4, s3\n"
  "s_cselect_b32 s6, 20, s6\n"
  "s_cselect_b32 s7, 8, s7\n"
  "s_cmp_eq_u32 s70, 4\n"
  "s_cselect_b32 s2, s4, s2\n"
  "s_cselect_b32 s3, 80, s3\n"
  "s_cselect_b32 s6, 40, s6\n"
  "s_cselect_b32 s7, 20, s7\n"
  "s_mov_b32 s42, 1966080\n"
  "s_cmp_lt_u32 s70, 5\n"
  "s_cselect_b32 s40, s12, s18\n"
  "s_cselect_b32 s41, s13, s19\n"
  "s_nop 0\n"
  "s_cselect_b32 s42, 819200, s42\n"
  "s_and_b32 s41, s41, 0xffff\n"
  "s_mov_b32 s43, 0x20000\n"
  "s_mul_i32 s67, s67, 160\n"
  "s_movk_i32 s68, 1280\n"
  "s_movk_i32 s69, 6554\n"
  "s_mul_i32 s71, s46, 5120\n"
  "s_add_u32 s71, s71, 147456\n"
  "v_add_u32 v24, 0, v0\n"
  "v_mul_u32_u24 v25, s69, v24\n"
  "v_lshrrev_b32 v25, 16, v25\n"
  "v_mul_u32_u24 v26, 10, v25\n"
  "v_sub_u32 v26, v24, v26\n"
  "v_and_b32 v27, 3, v25\n"
  "v_add_u32_e64 v27, s2, v27\n"
  "v_bfe_u32 v28, v25, 2, 1\n"
  "v_mad_u32_u24 v27, v28, s3, v27\n"
  "v_bfe_u32 v28, v25, 4, 1\n"
  "v_mad_u32_u24 v27, v28, s6, v27\n"
  "v_bfe_u32 v28, v25, 3, 1\n"
  "v_mad_u32_u24 v27, v28, s7, v27\n"
  "v_lshlrev_b32_e64 v26, 4, v26\n"
  "v_mad_u32_u24 v27, v27, s68, v26\n"
  "s_add_u32 m0, s71, 0\n"
  "s_mov_b64 exec, s[90:91]\n"
  "buffer_load_dwordx4 v27, s[40:43], s67 offen lds\n"
  "s_mov_b64 exec, -1\n"
  "v_add_u32 v24, 64, v0\n"
  "v_mul_u32_u24 v25, s69, v24\n"
  "v_lshrrev_b32 v25, 16, v25\n"
  "v_mul_u32_u24 v26, 10, v25\n"
  "v_sub_u32 v26, v24, v26\n"
  "v_and_b32 v27, 3, v25\n"
  "v_add_u32 v27, s2, v27\n"
  "v_bfe_u32 v28, v25, 2, 1\n"
  "v_mad_u32_u24 v27, v28, s3, v27\n"
  "v_bfe_u32 v28, v25, 4, 1\n"
  "v_mad_u32_u24 v27, v28, s6, v27\n"
  "v_bfe_u32 v28, v25, 3, 1\n"
  "v_mad_u32_u24 v27, v28, s7, v27\n"
  "v_lshlrev_b32_e64 v26, 4, v26\n"
  "v_mad_u32_u24 v27, v27, s68, v26\n"
  "s_add_u32 m0, s71, 1024\n"
  "s_mov_b64 exec, s[90:91]\n"
  "s_nop 0\n"
  "buffer_load_dwordx4 v27, s[40:43], s67 offen lds\n"
  "s_mov_b64 exec, -1\n"
  "s_nop 0\n"
  "v_add_u32 v24, 128, v0\n"
  "v_mul_u32_u24 v25, s69, v24\n"
  "v_lshrrev_b32 v25, 16, v25\n"
  "v_mul_u32_u24 v26, 10, v25\n"
  "v_sub_u32 v26, v24, v26\n"
  "v_and_b32 v27, 3, v25\n"
  "v_add_u32 v27, s2, v27\n"
  "v_bfe_u32 v28, v25, 2, 1\n"
  "v_mad_u32_u24 v27, v28, s3, v27\n"
  "v_bfe_u32 v28, v25, 4, 1\n"
  "v_mad_u32_u24 v27, v28, s6, v27\n"
  "v_bfe_u32 v28, v25, 3, 1\n"
  "v_mad_u32_u24 v27, v28, s7, v27\n"
  "v_lshlrev_b32_e64 v26, 4, v26\n"
  "v_mad_u32_u24 v27, v27, s68, v26\n"
  "s_add_u32 m0, s71, 2048\n"
  "s_mov_b64 exec, s[90:91]\n"
  "s_nop 0\n"
  "buffer_load_dwordx4 v27, s[40:43], s67 offen lds\n"
  "s_mov_b64 exec, -1\n"
  "s_nop 0\n"
  "v_add_u32 v24, 192, v0\n"
  "v_mul_u32_u24 v25, s69, v24\n"
  "v_lshrrev_b32 v25, 16, v25\n"
  "v_mul_u32_u24 v26, 10, v25\n"
  "v_sub_u32 v26, v24, v26\n"
  "v_and_b32 v27, 3, v25\n"
  "v_add_u32 v27, s2, v27\n"
  "v_bfe_u32 v28, v25, 2, 1\n"
  "v_mad_u32_u24 v27, v28, s3, v27\n"
  "v_bfe_u32 v28, v25, 4, 1\n"
  "v_mad_u32_u24 v27, v28, s6, v27\n"
  "v_bfe_u32 v28, v25, 3, 1\n"
  "v_mad_u32_u24 v27, v28, s7, v27\n"
  "v_lshlrev_b32_e64 v26, 4, v26\n"
  "v_mad_u32_u24 v27, v27, s68, v26\n"
  "s_add_u32 m0, s71, 3072\n"
  "s_mov_b64 exec, s[90:91]\n"
  "s_nop 0\n"
  "buffer_load_dwordx4 v27, s[40:43], s67 offen lds\n"
  "s_mov_b64 exec, -1\n"
  "s_nop 0\n"
  "v_add_u32 v24, 256, v0\n"
  "v_mul_u32_u24 v25, s69, v24\n"
  "v_lshrrev_b32 v25, 16, v25\n"
  "v_mul_u32_u24 v26, 10, v25\n"
  "v_sub_u32 v26, v24, v26\n"
  "v_and_b32 v27, 3, v25\n"
  "v_add_u32 v27, s2, v27\n"
  "v_bfe_u32 v28, v25, 2, 1\n"
  "v_mad_u32_u24 v27, v28, s3, v27\n"
  "v_bfe_u32 v28, v25, 4, 1\n"
  "v_mad_u32_u24 v27, v28, s6, v27\n"
  "v_bfe_u32 v28, v25, 3, 1\n"
  "v_mad_u32_u24 v27, v28, s7, v27\n"
  "v_lshlrev_b32_e64 v26, 4, v26\n"
  "v_mad_u32_u24 v27, v27, s68, v26\n"
  "s_add_u32 m0, s71, 4096\n"
  "s_mov_b64 exec, s[90:91]\n"
  "s_nop 0\n"
  "buffer_load_dwordx4 v27, s[40:43], s67 offen lds\n"
  "s_mov_b64 exec, -1\n"
  "s_nop 0\n"
  "v_bfe_u32 v24, v0, 2, 2\n"
  "v_and_b32 v25, 3, v0\n"
  "v_lshlrev_b32 v18, 4, v0\n"
  "v_add_u32 v19, 4096, v18\n"
  "v_mov_b32 v26, 0x7fff0000\n"
  "v_cndmask_b32_e64 v48, v26, v18, s[90:91]\n"
  "s_min_u32 s71, s71, 157696\n"
  "v_lshlrev_b32 v26, 3, v22\n"
  "v_add_u32 v26, v26, v24\n"
  "s_movk_i32 s66, 160\n"
  "v_mul_u32_u24 v26, s66, v26\n"
  "v_mul_u32_u24 v27, 40, v25\n"
  "s_nop 0\n"
  "v_add3_u32 v49, v26, v27, s71\n"
  "v_mov_b32 v52, 0x3f803f80\n"
  "v_mov_b32 v53, 0x3f803f80\n"
  "v_mov_b32 v54, 0x3f803f80\n"
  "v_mov_b32 v55, 0x3f803f80\n"
  "v_lshlrev_b32 v26, 3, v22\n"
  "v_add_u32 v26, v26, v24\n"
  "v_lshlrev_b32_e64 v26, 7, v26\n"
  "v_lshl_add_u32 v26, v25, 3, v26\n"
  "v_lshrrev_b32 v27, 1, v24\n"
  "v_and_b32 v28, 1, v22\n"
  "v_lshl_add_u32 v27, v28, 1, v27\n"
  "s_and_b32 s66, s46, 1\n"
  "s_lshl_b32 s66, s66, 1\n"
  "s_add_u32 s67, s66, 0\n"
  "v_xor_b32 v28, s67, v27\n"
  "v_lshl_add_u32 v12, v28, 5, v26\n"
  "v_add_u32 v13, 49152, v12\n"
  "s_add_u32 s67, s66, 1\n"
  "v_xor_b32 v28, s67, v27\n"
  "v_lshl_add_u32 v14, v28, 5, v26\n"
  "v_add_u32 v15, 49152, v14\n"
  "s_and_b32 s66, s46, 1\n"
  "s_nop 0\n"
  "s_mul_i32 s66, s66, 768\n"
  "s_lshr_b32 s67, s46, 1\n"
  "s_nop 0\n"
  "s_mul_i32 s67, s67, 384\n"
  "s_add_u32 s66, s66, s67\n"
  "s_cmp_lt_u32 s51, 15\n"
  "s_cselect_b32 s66, s66, 0x7fff0000\n"
  "v_lshl_add_u32 v50, v22, 4, s66\n"
  "v_cmp_eq_u32 vcc, 0, v23\n"
  "s_nop 0\n"
  "v_mov_b32 v26, 0x7fff0000\n"
  "s_nop 0\n"
  "v_cndmask_b32 v50, v26, v50, vcc\n"
  "s_mov_b32 s77, s52\n"
  "s_cmp_eq_u32 s51, 1\n"
  "s_cselect_b32 s77, s53, s77\n"
  "s_cmp_eq_u32 s51, 2\n"
  "s_cselect_b32 s77, s54, s77\n"
  "s_cmp_eq_u32 s51, 3\n"
  "s_cselect_b32 s77, s55, s77\n"
  "s_cmp_eq_u32 s51, 4\n"
  "s_cselect_b32 s77, s56, s77\n"
  "s_mul_i32 s77, s77, 12\n"
  "s_mul_i32 s66, s48, 15360\n"
  "s_add_u32 s77, s77, s66\n"
  "s_nop 0\n"
  "s_add_u32 s77, s77, 65536\n"
  "s_lshl_b32 s51, s51, 1\n"
  "s_lshr_b32 s66, s46, 1\n"
  "s_add_u32 s51, s51, s66\n"
  "s_lshr_b32 s66, s46, 1\n"
  "s_lshl_b32 s66, s66, 13\n"
  "s_nop 0\n"
  "s_add_u32 s66, s66, 98304\n"
  "v_lshrrev_b32 v26, 1, v23\n"
  "v_xor_b32 v26, v26, v22\n"
  "v_lshlrev_b32_e64 v26, 4, v26\n"
  "v_lshl_add_u32 v26, v23, 7, v26\n"
  "v_add_u32 v16, s66, v26\n"
  "v_xor_b32 v26, 64, v26\n"
  "v_add_u32 v17, s66, v26\n"
  "s_waitcnt lgkmcnt(0)\n"
  "s_add_u32 s44, s44, 0x7f4a7c15\n"
  "s_addc_u32 s45, s45, 0x9e3779b9\n"
  "s_waitcnt vmcnt(24) lgkmcnt(0)\n"
  "s_barrier\n"
  "ds_read_b128 a[48:51], v16 offset:0\n"
  "ds_read_b128 a[52:55], v16 offset:2048\n"
  "ds_read_b128 a[56:59], v16 offset:4096\n"
  "ds_read_b128 a[60:63], v16 offset:6144\n"
  "ds_read_b64_tr_b16 a[0:1], v12 offset:0\n"
  "ds_read_b64_tr_b16 a[2:3], v12 offset:512\n"
  "ds_read_b64_tr_b16 a[4:5], v14 offset:0\n"
  "ds_read_b64_tr_b16 a[6:7], v14 offset:512\n"
  "ds_read_b64_tr_b16 a[8:9], v12 offset:4096\n"
  "ds_read_b64_tr_b16 a[10:11], v12 offset:4608\n"
  "ds_read_b64_tr_b16 a[12:13], v14 offset:4096\n"
  "ds_read_b64_tr_b16 a[14:15], v14 offset:4608\n"
  "ds_read_b64_tr_b16 a[16:17], v12 offset:8192\n"
  "ds_read_b64_tr_b16 a[18:19], v12 offset:8704\n"
  "ds_read_b64_tr_b16 a[20:21], v14 offset:8192\n"
  "ds_read_b64_tr_b16 a[22:23], v14 offset:8704\n"
  "ds_read_b64_tr_b16 a[24:25], v12 offset:12288\n"
  "ds_read_b64_tr_b16 a[26:27], v12 offset:12800\n"
  "ds_read_b64_tr_b16 a[28:29], v14 offset:12288\n"
  "ds_read_b64_tr_b16 a[30:31], v14 offset:12800\n"
  "ds_read_b64_tr_b16 a[32:33], v12 offset:16384\n"
  "ds_read_b64_tr_b16 a[34:35], v12 offset:16896\n"
  "ds_read_b64_tr_b16 a[36:37], v14 offset:16384\n"
  "ds_read_b64_tr_b16 a[38:39], v14 offset:16896\n"
  "ds_read_b64_tr_b16 a[40:41], v12 offset:20480\n"
  "ds_read_b64_tr_b16 a[42:43], v12 offset:20992\n"
  "ds_read_b64_tr_b16 a[44:45], v14 offset:20480\n"
  "ds_read_b64_tr_b16 a[46:47], v14 offset:20992\n"
  "s_waitcnt lgkmcnt(0)\n"
  "s_nop 0\n"
  "v_mfma_f32_16x16x32_bf16 v[64:67], a[0:3], a[48:51], 0\n"
  "s_mul_i32 s62, s54, 1536\n"
  "s_add_u32 m0, s65, 34816\n"
  "v_mfma_f32_16x16x32_bf16 v[68:71], a[0:3], a[52:55], 0\n"
  "buffer_load_dwordx4 v8, s[24:27], s54 offen lds\n"
  "v_mfma_f32_16x16x32_bf16 v[72:75], a[0:3], a[56:59], 0\n"
  "ds_read_b128 a[112:115], v17 offset:0\n"
  "v_mfma_f32_16x16x32_bf16 v[76:79], a[0:3], a[60:63], 0\n"
  "ds_read_b128 a[116:119], v17 offset:2048\n"
  "v_mfma_f32_16x16x32_bf16 v[80:83], a[4:7], a[48:51], 0\n"
  "ds_read_b128 a[120:123], v17 offset:4096\n"
  "v_mfma_f32_16x16x32_bf16 v[84:87], a[4:7], a[52:55], 0\n"
  "ds_read_b128 a[124:127], v17 offset:6144\n"
  "v_mfma_f32_16x16x32_bf16 v[88:91], a[4:7], a[56:59], 0\n"
  "s_add_u32 m0, s65, 35840\n"
  "v_mfma_f32_16x16x32_bf16 v[92:95], a[4:7], a[60:63], 0\n"
  "buffer_load_dwordx4 v9, s[24:27], s54 offen lds\n"
  "v_mfma_f32_16x16x32_bf16 v[96:99], a[8:11], a[48:51], 0\n"
  "ds_read_b64_tr_b16 a[64:65], v12 offset:24576\n"
  "v_mfma_f32_16x16x32_bf16 v[100:103], a[8:11], a[52:55], 0\n"
  "ds_read_b64_tr_b16 a[66:67], v12 offset:25088\n"
  "v_mfma_f32_16x16x32_bf16 v[104:107], a[8:11], a[56:59], 0\n"
  "ds_read_b64_tr_b16 a[68:69], v14 offset:24576\n"
  "v_mfma_f32_16x16x32_bf16 v[108:111], a[8:11], a[60:63], 0\n"
  "ds_read_b64_tr_b16 a[70:71], v14 offset:25088\n"
  "v_mfma_f32_16x16x32_bf16 v[112:115], a[12:15], a[48:51], 0\n"
  "s_add_u32 m0, s64, 0\n"
  "s_nop 0\n"
  "v_mfma_f32_16x16x32_bf16 v[116:119], a[12:15], a[52:55], 0\n"
  "buffer_load_dwordx4 v2, s[28:31], s62 offen lds\n"
  "v_mfma_f32_16x16x32_bf16 v[120:123], a[12:15], a[56:59], 0\n"
  "ds_read_b64_tr_b16 a[72:73], v12 offset:28672\n"
  "v_mfma_f32_16x16x32_bf16 v[124:127], a[12:15], a[60:63], 0\n"
  "ds_read_b64_tr_b16 a[74:75], v12 offset:29184\n"
  "v_mfma_f32_16x16x32_bf16 v[128:131], a[16:19], a[48:51], 0\n"
  "ds_read_b64_tr_b16 a[76:77], v14 offset:28672\n"
  "v_mfma_f32_16x16x32_bf16 v[132:135], a[16:19], a[52:55], 0\n"
  "ds_read_b64_tr_b16 a[78:79], v14 offset:29184\n"
  "v_mfma_f32_16x16x32_bf16 v[136:139], a[16:19], a[56:59], 0\n"
  "s_add_u32 m0, s64, 4096\n"
  "v_mfma_f32_16x16x32_bf16 v[140:143], a[16:19], a[60:63], 0\n"
  "buffer_load_dwordx4 v2, s[32:35], s62 offen lds\n"
  "v_mfma_f32_16x16x32_bf16 v[144:147], a[20:23], a[48:51], 0\n"
  "ds_read_b64_tr_b16 a[80:81], v12 offset:32768\n"
  "v_mfma_f32_16x16x32_bf16 v[148:151], a[20:23], a[52:55], 0\n"
  "ds_read_b64_tr_b16 a[82:83], v12 offset:33280\n"
  "v_mfma_f32_16x16x32_bf16 v[152:155], a[20:23], a[56:59], 0\n"
  "ds_read_b64_tr_b16 a[84:85], v14 offset:32768\n"
  "v_mfma_f32_16x16x32_bf16 v[156:159], a[20:23], a[60:63], 0\n"
  "ds_read_b64_tr_b16 a[86:87], v14 offset:33280\n"
  "v_mfma_f32_16x16x32_bf16 v[160:163], a[24:27], a[48:51], 0\n"
  "s_add_u32 m0, s64, 8064\n"
  "v_mfma_f32_16x16x32_bf16 v[164:167], a[24:27], a[52:55], 0\n"
  "buffer_load_dwordx4 v2, s[28:31], s62 offen offset:128 lds\n"
  "v_mfma_f32_16x16x32_bf16 v[168:171], a[24:27], a[56:59], 0\n"
  "ds_read_b64_tr_b16 a[88:89], v12 offset:36864\n"
  "v_mfma_f32_16x16x32_bf16 v[172:175], a[24:27], a[60:63], 0\n"
  "ds_read_b64_tr_b16 a[90:91], v12 offset:37376\n"
  "v_mfma_f32_16x16x32_bf16 v[176:179], a[28:31], a[48:51], 0\n"
  "ds_read_b64_tr_b16 a[92:93], v14 offset:36864\n"
  "v_mfma_f32_16x16x32_bf16 v[180:183], a[28:31], a[52:55], 0\n"
  "ds_read_b64_tr_b16 a[94:95], v14 offset:37376\n"
  "v_mfma_f32_16x16x32_bf16 v[184:187], a[28:31], a[56:59], 0\n"
  "s_add_u32 m0, s64, 12160\n"
  "v_mfma_f32_16x16x32_bf16 v[188:191], a[28:31], a[60:63], 0\n"
  "buffer_load_dwordx4 v2, s[32:35], s62 offen offset:128 lds\n"
  "v_mfma_f32_16x16x32_bf16 v[192:195], a[32:35], a[48:51], 0\n"
  "ds_read_b64_tr_b16 a[96:97], v12 offset:40960\n"
  "v_mfma_f32_16x16x32_bf16 v[196:199], a[32:35], a[52:55], 0\n"
  "ds_read_b64_tr_b16 a[98:99], v12 offset:41472\n"
  "v_mfma_f32_16x16x32_bf16 v[200:203], a[32:35], a[56:59], 0\n"
  "ds_read_b64_tr_b16 a[100:101], v14 offset:40960\n"
  "v_mfma_f32_16x16x32_bf16 v[204:207], a[32:35], a[60:63], 0\n"
  "ds_read_b64_tr_b16 a[102:103], v14 offset:41472\n"
  "v_mfma_f32_16x16x32_bf16 v[208:211], a[36:39], a[48:51], 0\n"
  "s_add_u32 m0, s64, 16128\n"
  "v_mfma_f32_16x16x32_bf16 v[212:215], a[36:39], a[52:55], 0\n"
  "buffer_load_dwordx4 v2, s[28:31], s62 offen offset:256 lds\n"
  "v_mfma_f32_16x16x32_bf16 v[216:219], a[36:39], a[56:59], 0\n"
  "ds_read_b64_tr_b16 a[104:105], v12 offset:45056\n"
  "v_mfma_f32_16x16x32_bf16 v[220:223], a[36:39], a[60:63], 0\n"
  "ds_read_b64_tr_b16 a[106:107], v12 offset:45568\n"
  "v_mfma_f32_16x16x32_bf16 v[224:227], a[40:43], a[48:51], 0\n"
  "ds_read_b64_tr_b16 a[108:109], v14 offset:45056\n"
  "v_mfma_f32_16x16x32_bf16 v[228:231], a[40:43], a[52:55], 0\n"
  "ds_read_b64_tr_b16 a[110:111], v14 offset:45568\n"
  "v_mfma_f32_16x16x32_bf16 v[232:235], a[40:43], a[56:59], 0\n"
  "s_add_u32 m0, s64, 20224\n"
  "v_mfma_f32_16x16x32_bf16 v[236:239], a[40:43], a[60:63], 0\n"
  "buffer_load_dwordx4 v2, s[32:35], s62 offen offset:256 lds\n"
  "v_mfma_f32_16x16x32_bf16 v[240:243], a[44:47], a[48:51], 0\n"
  "v_mfma_f32_16x16x32_bf16 v[244:247], a[44:47], a[52:55], 0\n"
  "v_mfma_f32_16x16x32_bf16 v[248:251], a[44:47], a[56:59], 0\n"
  "v_mfma_f32_16x16x32_bf16 v[252:255], a[44:47], a[60:63], 0\n"
  "s_cmp_eq_u32 s51, 0\n"
  "s_cbranch_scc0 .Lcs1\n"
  "v_mfma_f32_16x16x32_bf16 a[148:151], a[0:3], v[52:55], 0\n"
  "v_mfma_f32_16x16x32_bf16 a[152:155], a[4:7], v[52:55], 0\n"
  "v_mfma_f32_16x16x32_bf16 a[156:159], a[8:11], v[52:55], 0\n"
  "v_mfma_f32_16x16x32_bf16 a[160:163], a[12:15], v[52:55], 0\n"
  "v_mfma_f32_16x16x32_bf16 a[164:167], a[16:19], v[52:55], 0\n"
  "v_mfma_f32_16x16x32_bf16 a[168:171], a[20:23], v[52:55], 0\n"
  ".Lcs1:\n"
  "s_cmp_eq_u32 s51, 1\n"
  "s_cbranch_scc0 .Lcs2\n"
  "s_nop 0\n"
  "s_nop 0\n"
  "v_mfma_f32_16x16x32_bf16 a[148:151], a[24:27], v[52:55], 0\n"
  "v_mfma_f32_16x16x32_bf16 a[152:155], a[28:31], v[52:55], 0\n"
  "v_mfma_f32_16x16x32_bf16 a[156:159], a[32:35], v[52:55], 0\n"
  "v_mfma_f32_16x16x32_bf16 a[160:163], a[36:39], v[52:55], 0\n"
  "v_mfma_f32_16x16x32_bf16 a[164:167], a[40:43], v[52:55], 0\n"
  "v_mfma_f32_16x16x32_bf16 a[168:171], a[44:47], v[52:55], 0\n"
  ".Lcs2:\n"
  "s_waitcnt vmcnt(21) lgkmcnt(0)\n"
  "s_barrier\n"
  "v_mfma_f32_16x16x32_bf16 v[64:67], a[64:67], a[112:115], v[64:67]\n"
  "s_add_u32 s63, s62, 98304\n"
  "s_add_u32 m0, s65, 0\n"
  "s_nop 0\n"
  "v_mfma_f32_16x16x32_bf16 v[68:71], a[64:67], a[116:119], v[68:71]\n"
  "buffer_load_dwordx4 v6, s[24:27], s55 offen lds\n"
  "v_mfma_f32_16x16x32_bf16 v[72:75], a[64:67], a[120:123], v[72:75]\n"
  "ds_read_b128 a[48:51], v16 offset:16384\n"
  "v_mfma_f32_16x16x32_bf16 v[76:79], a[64:67], a[124:127], v[76:79]\n"
  "ds_read_b128 a[52:55], v16 offset:18432\n"
  "v_mfma_f32_16x16x32_bf16 v[80:83], a[68:71], a[112:115], v[80:83]\n"
  "ds_read_b128 a[56:59], v16 offset:20480\n"
  "v_mfma_f32_16x16x32_bf16 v[84:87], a[68:71], a[116:119], v[84:87]\n"
  "ds_read_b128 a[60:63], v16 offset:22528\n"
  "v_mfma_f32_16x16x32_bf16 v[88:91], a[68:71], a[120:123], v[88:91]\n"
  "s_add_u32 m0, s65, 1024\n"
  "v_mfma_f32_16x16x32_bf16 v[92:95], a[68:71], a[124:127], v[92:95]\n"
  "buffer_load_dwordx4 v7, s[24:27], s55 offen lds\n"
  "v_mfma_f32_16x16x32_bf16 v[96:99], a[72:75], a[112:115], v[96:99]\n"
  "ds_read_b64_tr_b16 a[0:1], v13 offset:0\n"
  "v_mfma_f32_16x16x32_bf16 v[100:103], a[72:75], a[116:119], v[100:103]\n"
  "ds_read_b64_tr_b16 a[2:3], v13 offset:512\n"
  "v_mfma_f32_16x16x32_bf16 v[104:107], a[72:75], a[120:123], v[104:107]\n"
  "ds_read_b64_tr_b16 a[4:5], v15 offset:0\n"
  "v_mfma_f32_16x16x32_bf16 v[108:111], a[72:75], a[124:127], v[108:111]\n"
  "ds_read_b64_tr_b16 a[6:7], v15 offset:512\n"
  "v_mfma_f32_16x16x32_bf16 v[112:115], a[76:79], a[112:115], v[112:115]\n"
  "s_add_u32 m0, s64, 24576\n"
  "v_mfma_f32_16x16x32_bf16 v[116:119], a[76:79], a[116:119], v[116:119]\n"
  "buffer_load_dwordx4 v2, s[28:31], s63 offen lds\n"
  "v_mfma_f32_16x16x32_bf16 v[120:123], a[76:79], a[120:123], v[120:123]\n"
  "ds_read_b64_tr_b16 a[8:9], v13 offset:4096\n"
  "v_mfma_f32_16x16x32_bf16 v[124:127], a[76:79], a[124:127], v[124:127]\n"
  "ds_read_b64_tr_b16 a[10:11], v13 offset:4608\n"
  "v_mfma_f32_16x16x32_bf16 v[128:131], a[80:83], a[112:115], v[128:131]\n"
  "ds_read_b64_tr_b16 a[12:13], v15 offset:4096\n"
  "v_mfma_f32_16x16x32_bf16 v[132:135], a[80:83], a[116:119], v[132:135]\n"
  "ds_read_b64_tr_b16 a[14:15], v15 offset:4608\n"
  "v_mfma_f32_16x16x32_bf16 v[136:139], a[80:83], a[120:123], v[136:139]\n"
  "s_add_u32 m0, s64, 28672\n"
  "v_mfma_f32_16x16x32_bf16 v[140:143], a[80:83], a[124:127], v[140:143]\n"
  "buffer_load_dwordx4 v2, s[32:35], s63 offen lds\n"
  "v_mfma_f32_16x16x32_bf16 v[144:147], a[84:87], a[112:115], v[144:147]\n"
  "ds_read_b64_tr_b16 a[16:17], v13 offset:8192\n"
  "v_mfma_f32_16x16x32_bf16 v[148:151], a[84:87], a[116:119], v[148:151]\n"
  "ds_read_b64_tr_b16 a[18:19], v13 offset:8704\n"
  "v_mfma_f32_16x16x32_bf16 v[152:155], a[84:87], a[120:123], v[152:155]\n"
  "ds_read_b64_tr_b16 a[20:21], v15 offset:8192\n"
  "v_mfma_f32_16x16x32_bf16 v[156:159], a[84:87], a[124:127], v[156:159]\n"
  "ds_read_b64_tr_b16 a[22:23], v15 offset:8704\n"
  "v_mfma_f32_16x16x32_bf16 v[160:163], a[88:91], a[112:115], v[160:163]\n"
  "s_add_u32 m0, s64, 32640\n"
  "v_mfma_f32_16x16x32_bf16 v[164:167], a[88:91], a[116:119], v[164:167]\n"
  "buffer_load_dwordx4 v2, s[28:31], s63 offen offset:128 lds\n"
  "v_mfma_f32_16x16x32_bf16 v[168:171], a[88:91], a[120:123], v[168:171]\n"
  "ds_read_b64_tr_b16 a[24:25], v13 offset:12288\n"
  "v_mfma_f32_16x16x32_bf16 v[172:175], a[88:91], a[124:127], v[172:175]\n"
  "ds_read_b64_tr_b16 a[26:27], v13 offset:12800\n"
  "v_mfma_f32_16x16x32_bf16 v[176:179], a[92:95], a[112:115], v[176:179]\n"
  "ds_read_b64_tr_b16 a[28:29], v15 offset:12288\n"
  "v_mfma_f32_16x16x32_bf16 v[180:183], a[92:95], a[116:119], v[180:183]\n"
  "ds_read_b64_tr_b16 a[30:31], v15 offset:12800\n"
  "v_mfma_f32_16x16x32_bf16 v[184:187], a[92:95], a[120:123], v[184:187]\n"
  "s_add_u32 m0, s64, 36736\n"
  "v_mfma_f32_16x16x32_bf16 v[188:191], a[92:95], a[124:127], v[188:191]\n"
  "buffer_load_dwordx4 v2, s[32:35], s63 offen offset:128 lds\n"
  "v_mfma_f32_16x16x32_bf16 v[192:195], a[96:99], a[112:115], v[192:195]\n"
  "ds_read_b64_tr_b16 a[32:33], v13 offset:16384\n"
  "v_mfma_f32_16x16x32_bf16 v[196:199], a[96:99], a[116:119], v[196:199]\n"
  "ds_read_b64_tr_b16 a[34:35], v13 offset:16896\n"
  "v_mfma_f32_16x16x32_bf16 v[200:203], a[96:99], a[120:123], v[200:203]\n"
  "ds_read_b64_tr_b16 a[36:37], v15 offset:16384\n"
  "v_mfma_f32_16x16x32_bf16 v[204:207], a[96:99], a[124:127], v[204:207]\n"
  "ds_read_b64_tr_b16 a[38:39], v15 offset:16896\n"
  "v_mfma_f32_16x16x32_bf16 v[208:211], a[100:103], a[112:115], v[208:211]\n"
  "s_add_u32 m0, s64, 40704\n"
  "v_mfma_f32_16x16x32_bf16 v[212:215], a[100:103], a[116:119], v[212:215]\n"
  "buffer_load_dwordx4 v2, s[28:31], s63 offen offset:256 lds\n"
  "v_mfma_f32_16x16x32_bf16 v[216:219], a[100:103], a[120:123], v[216:219]\n"
  "ds_read_b64_tr_b16 a[40:41], v13 offset:20480\n"
  "v_mfma_f32_16x16x32_bf16 v[220:223], a[100:103], a[124:127], v[220:223]\n"
  "ds_read_b64_tr_b16 a[42:43], v13 offset:20992\n"
  "v_mfma_f32_16x16x32_bf16 v[224:227], a[104:107], a[112:115], v[224:227]\n"
  "ds_read_b64_tr_b16 a[44:45], v15 offset:20480\n"
  "v_mfma_f32_16x16x32_bf16 v[228:231], a[104:107], a[116:119], v[228:231]\n"
  "ds_read_b64_tr_b16 a[46:47], v15 offset:20992\n"
  "v_mfma_f32_16x16x32_bf16 v[232:235], a[104:107], a[120:123], v[232:235]\n"
  "s_add_u32 m0, s64, 44800\n"
  "v_mfma_f32_16x16x32_bf16 v[236:239], a[104:107], a[124:127], v[236:239]\n"
  "buffer_load_dwordx4 v2, s[32:35], s63 offen offset:256 lds\n"
  "v_mfma_f32_16x16x32_bf16 v[240:243], a[108:111], a[112:115], v[240:243]\n"
  "v_mfma_f32_16x16x32_bf16 v[244:247], a[108:111], a[116:119], v[244:247]\n"
  "v_mfma_f32_16x16x32_bf16 v[248:251], a[108:111], a[120:123], v[248:251]\n"
  "v_mfma_f32_16x16x32_bf16 v[252:255], a[108:111], a[124:127], v[252:255]\n"
  "s_cmp_eq_u32 s51, 0\n"
  "s_cbranch_scc0 .Lcs3\n"
  "v_mfma_f32_16x16x32_bf16 a[148:151], a[64:67], v[52:55], a[148:151]\n"
  "v_mfma_f32_16x16x32_bf16 a[152:155], a[68:71], v[52:55], a[152:155]\n"
  "v_mfma_f32_16x16x32_bf16 a[156:159], a[72:75], v[52:55], a[156:159]\n"
  "v_mfma_f32_16x16x32_bf16 a[160:163], a[76:79], v[52:55], a[160:163]\n"
  "v_mfma_f32_16x16x32_bf16 a[164:167], a[80:83], v[52:55], a[164:167]\n"
  "v_mfma_f32_16x16x32_bf16 a[168:171], a[84:87], v[52:55], a[168:171]\n"
  ".Lcs3:\n"
  "s_cmp_eq_u32 s51, 1\n"
  "s_cbranch_scc0 .Lcs4\n"
  "v_mfma_f32_16x16x32_bf16 a[148:151], a[88:91], v[52:55], a[148:151]\n"
  "v_mfma_f32_16x16x32_bf16 a[152:155], a[92:95], v[52:55], a[152:155]\n"
  "v_mfma_f32_16x16x32_bf16 a[156:159], a[96:99], v[52:55], a[156:159]\n"
  "v_mfma_f32_16x16x32_bf16 a[160:163], a[100:103], v[52:55], a[160:163]\n"
  "v_mfma_f32_16x16x32_bf16 a[164:167], a[104:107], v[52:55], a[164:167]\n"
  "v_mfma_f32_16x16x32_bf16 a[168:171], a[108:111], v[52:55], a[168:171]\n"
  ".Lcs4:\n"
  "s_waitcnt vmcnt(23) lgkmcnt(0)\n"
  "s_barrier\n"
  "v_mfma_f32_16x16x32_bf16 v[64:67], a[0:3], a[48:51], v[64:67]\n"
  "s_mul_i32 s62, s55, 1536\n"
  "s_add_u32 m0, s65, 2048\n"
  "v_mfma_f32_16x16x32_bf16 v[68:71], a[0:3], a[52:55], v[68:71]\n"
  "buffer_load_dwordx4 v8, s[24:27], s55 offen lds\n"
  "v_mfma_f32_16x16x32_bf16 v[72:75], a[0:3], a[56:59], v[72:75]\n"
  "ds_read_b128 a[112:115], v17 offset:16384\n"
  "v_mfma_f32_16x16x32_bf16 v[76:79], a[0:3], a[60:63], v[76:79]\n"
  "ds_read_b128 a[116:119], v17 offset:18432\n"
  "v_mfma_f32_16x16x32_bf16 v[80:83], a[4:7], a[48:51], v[80:83]\n"
  "ds_read_b128 a[120:123], v17 offset:20480\n"
  "v_mfma_f32_16x16x32_bf16 v[84:87], a[4:7], a[52:55], v[84:87]\n"
  "ds_read_b128 a[124:127], v17 offset:22528\n"
  "v_mfma_f32_16x16x32_bf16 v[88:91], a[4:7], a[56:59], v[88:91]\n"
  "s_add_u32 m0, s65, 3072\n"
  "v_mfma_f32_16x16x32_bf16 v[92:95], a[4:7], a[60:63], v[92:95]\n"
  "buffer_load_dwordx4 v9, s[24:27], s55 offen lds\n"
  "v_mfma_f32_16x16x32_bf16 v[96:99], a[8:11], a[48:51], v[96:99]\n"
  "ds_read_b64_tr_b16 a[64:65], v13 offset:24576\n"
  "v_mfma_f32_16x16x32_bf16 v[100:103], a[8:11], a[52:55], v[100:103]\n"
  "ds_read_b64_tr_b16 a[66:67], v13 offset:25088\n"
  "v_mfma_f32_16x16x32_bf16 v[104:107], a[8:11], a[56:59], v[104:107]\n"
  "ds_read_b64_tr_b16 a[68:69], v15 offset:24576\n"
  "v_mfma_f32_16x16x32_bf16 v[108:111], a[8:11], a[60:63], v[108:111]\n"
  "ds_read_b64_tr_b16 a[70:71], v15 offset:25088\n"
  "v_mfma_f32_16x16x32_bf16 v[112:115], a[12:15], a[48:51], v[112:115]\n"
  "s_add_u32 m0, s64, 49152\n"
  "v_mfma_f32_16x16x32_bf16 v[116:119], a[12:15], a[52:55], v[116:119]\n"
  "buffer_load_dwordx4 v2, s[28:31], s62 offen lds\n"
  "v_mfma_f32_16x16x32_bf16 v[120:123], a[12:15], a[56:59], v[120:123]\n"
  "ds_read_b64_tr_b16 a[72:73], v13 offset:28672\n"
  "v_mfma_f32_16x16x32_bf16 v[124:127], a[12:15], a[60:63], v[124:127]\n"
  "ds_read_b64_tr_b16 a[74:75], v13 offset:29184\n"
  "v_mfma_f32_16x16x32_bf16 v[128:131], a[16:19], a[48:51], v[128:131]\n"
  "ds_read_b64_tr_b16 a[76:77], v15 offset:28672\n"
  "v_mfma_f32_16x16x32_bf16 v[132:135], a[16:19], a[52:55], v[132:135]\n"
  "ds_read_b64_tr_b16 a[78:79], v15 offset:29184\n"
  "v_mfma_f32_16x16x32_bf16 v[136:139], a[16:19], a[56:59], v[136:139]\n"
  "s_add_u32 m0, s64, 53248\n"
  "v_mfma_f32_16x16x32_bf16 v[140:143], a[16:19], a[60:63], v[140:143]\n"
  "buffer_load_dwordx4 v2, s[32:35], s62 offen lds\n"
  "v_mfma_f32_16x16x32_bf16 v[144:147], a[20:23], a[48:51], v[144:147]\n"
  "ds_read_b64_tr_b16 a[80:81], v13 offset:32768\n"
  "v_mfma_f32_16x16x32_bf16 v[148:151], a[20:23], a[52:55], v[148:151]\n"
  "ds_read_b64_tr_b16 a[82:83], v13 offset:33280\n"
  "v_mfma_f32_16x16x32_bf16 v[152:155], a[20:23], a[56:59], v[152:155]\n"
  "ds_read_b64_tr_b16 a[84:85], v15 offset:32768\n"
  "v_mfma_f32_16x16x32_bf16 v[156:159], a[20:23], a[60:63], v[156:159]\n"
  "ds_read_b64_tr_b16 a[86:87], v15 offset:33280\n"
  "v_mfma_f32_16x16x32_bf16 v[160:163], a[24:27], a[48:51], v[160:163]\n"
  "s_add_u32 m0, s64, 57216\n"
  "v_mfma_f32_16x16x32_bf16 v[164:167], a[24:27], a[52:55], v[164:167]\n"
  "buffer_load_dwordx4 v2, s[28:31], s62 offen offset:128 lds\n"
  "v_mfma_f32_16x16x32_bf16 v[168:171], a[24:27], a[56:59], v[168:171]\n"
  "ds_read_b64_tr_b16 a[88:89], v13 offset:36864\n"
  "v_mfma_f32_16x16x32_bf16 v[172:175], a[24:27], a[60:63], v[172:175]\n"
  "ds_read_b64_tr_b16 a[90:91], v13 offset:37376\n"
  "v_mfma_f32_16x16x32_bf16 v[176:179], a[28:31], a[48:51], v[176:179]\n"
  "ds_read_b64_tr_b16 a[92:93], v15 offset:36864\n"
  "v_mfma_f32_16x16x32_bf16 v[180:183], a[28:31], a[52:55], v[180:183]\n"
  "ds_read_b64_tr_b16 a[94:95], v15 offset:37376\n"
  "v_mfma_f32_16x16x32_bf16 v[184:187], a[28:31], a[56:59], v[184:187]\n"
  "s_add_u32 m0, s64, 61312\n"
  "v_mfma_f32_16x16x32_bf16 v[188:191], a[28:31], a[60:63], v[188:191]\n"
  "buffer_load_dwordx4 v2, s[32:35], s62 offen offset:128 lds\n"
  "v_mfma_f32_16x16x32_bf16 v[192:195], a[32:35], a[48:51], v[192:195]\n"
  "ds_read_b64_tr_b16 a[96:97], v13 offset:40960\n"
  "v_mfma_f32_16x16x32_bf16 v[196:199], a[32:35], a[52:55], v[196:199]\n"
  "ds_read_b64_tr_b16 a[98:99], v13 offset:41472\n"
  "v_mfma_f32_16x16x32_bf16 v[200:203], a[32:35], a[56:59], v[200:203]\n"
  "ds_read_b64_tr_b16 a[100:101], v15 offset:40960\n"
  "v_mfma_f32_16x16x32_bf16 v[204:207], a[32:35], a[60:63], v[204:207]\n"
  "ds_read_b64_tr_b16 a[102:103], v15 offset:41472\n"
  "v_mfma_f32_16x16x32_bf16 v[208:211], a[36:39], a[48:51], v[208:211]\n"
  "s_add_u32 m0, s64, 65280\n"
  "v_mfma_f32_16x16x32_bf16 v[212:215], a[36:39], a[52:55], v[212:215]\n"
  "buffer_load_dwordx4 v2, s[28:31], s62 offen offset:256 lds\n"
  "v_mfma_f32_16x16x32_bf16 v[216:219], a[36:39], a[56:59], v[216:219]\n"
  "ds_read_b64_tr_b16 a[104:105], v13 offset:45056\n"
  "v_mfma_f32_16x16x32_bf16 v[220:223], a[36:39], a[60:63], v[220:223]\n"
  "ds_read_b64_tr_b16 a[106:107], v13 offset:45568\n"
  "v_mfma_f32_16x16x32_bf16 v[224:227], a[40:43], a[48:51], v[224:227]\n"
  "ds_read_b64_tr_b16 a[108:109], v15 offset:45056\n"
  "v_mfma_f32_16x16x32_bf16 v[228:231], a[40:43], a[52:55], v[228:231]\n"
  "ds_read_b64_tr_b16 a[110:111], v15 offset:45568\n"
  "v_mfma_f32_16x16x32_bf16 v[232:235], a[40:43], a[56:59], v[232:235]\n"
  "s_add_u32 m0, s64, 69376\n"
  "v_mfma_f32_16x16x32_bf16 v[236:239], a[40:43], a[60:63], v[236:239]\n"
  "buffer_load_dwordx4 v2, s[32:35], s62 offen offset:256 lds\n"
  "v_mfma_f32_16x16x32_bf16 v[240:243], a[44:47], a[48:51], v[240:243]\n"
  "v_mfma_f32_16x16x32_bf16 v[244:247], a[44:47], a[52:55], v[244:247]\n"
  "v_mfma_f32_16x16x32_bf16 v[248:251], a[44:47], a[56:59], v[248:251]\n"
  "v_mfma_f32_16x16x32_bf16 v[252:255], a[44:47], a[60:63], v[252:255]\n"
  "s_cmp_eq_u32 s51, 2\n"
  "s_cbranch_scc0 .Lcs5\n"
  "v_mfma_f32_16x16x32_bf16 a[148:151], a[0:3], v[52:55], 0\n"
  "v_mfma_f32_16x16x32_bf16 a[152:155], a[4:7], v[52:55], 0\n"
  "v_mfma_f32_16x16x32_bf16 a[156:159], a[8:11], v[52:55], 0\n"
  "v_mfma_f32_16x16x32_bf16 a[160:163], a[12:15], v[52:55], 0\n"
  "v_mfma_f32_16x16x32_bf16 a[164:167], a[16:19], v[52:55], 0\n"
  "v_mfma_f32_16x16x32_bf16 a[168:171], a[20:23], v[52:55], 0\n"
  ".Lcs5:\n"
  "s_cmp_eq_u32 s51, 3\n"
  "s_cbranch_scc0 .Lcs6\n"
  "s_nop 0\n"
  "s_nop 0\n"
  "v_mfma_f32_16x16x32_bf16 a[148:151], a[24:27], v[52:55], 0\n"
  "v_mfma_f32_16x16x32_bf16 a[152:155], a[28:31], v[52:55], 0\n"
  "v_mfma_f32_16x16x32_bf16 a[156:159], a[32:35], v[52:55], 0\n"
  "v_mfma_f32_16x16x32_bf16 a[160:163], a[36:39], v[52:55], 0\n"
  "v_mfma_f32_16x16x32_bf16 a[164:167], a[40:43], v[52:55], 0\n"
  "v_mfma_f32_16x16x32_bf16 a[168:171], a[44:47], v[52:55], 0\n"
  ".Lcs6:\n"
  "s_waitcnt vmcnt(16) lgkmcnt(0)\n"
  "s_barrier\n"
  "v_mfma_f32_16x16x32_bf16 v[64:67], a[64:67], a[112:115], v[64:67]\n"
  "s_add_u32 s63, s62, 98304\n"
  "s_add_u32 m0, s65, 16384\n"
  "ds_read_b64_tr_b16 a[128:129], v49 offset:0\n"
  "v_mfma_f32_16x16x32_bf16 v[68:71], a[64:67], a[116:119], v[68:71]\n"
  "buffer_load_dwordx4 v6, s[24:27], s56 offen lds\n"
  "v_mfma_f32_16x16x32_bf16 v[72:75], a[64:67], a[120:123], v[72:75]\n"
  "ds_read_b128 a[48:51], v16 offset:32768\n"
  "v_mfma_f32_16x16x32_bf16 v[76:79], a[64:67], a[124:127], v[76:79]\n"
  "ds_read_b128 a[52:55], v16 offset:34816\n"
  "v_mfma_f32_16x16x32_bf16 v[80:83], a[68:71], a[112:115], v[80:83]\n"
  "ds_read_b128 a[56:59], v16 offset:36864\n"
  "ds_read_b64_tr_b16 a[130:131], v49 offset:640\n"
  "v_mfma_f32_16x16x32_bf16 v[84:87], a[68:71], a[116:119], v[84:87]\n"
  "ds_read_b128 a[60:63], v16 offset:38912\n"
  "v_mfma_f32_16x16x32_bf16 v[88:91], a[68:71], a[120:123], v[88:91]\n"
  "s_add_u32 m0, s65, 17408\n"
  "v_mfma_f32_16x16x32_bf16 v[92:95], a[68:71], a[124:127], v[92:95]\n"
  "buffer_load_dwordx4 v7, s[24:27], s56 offen lds\n"
  "v_mfma_f32_16x16x32_bf16 v[96:99], a[72:75], a[112:115], v[96:99]\n"
  "ds_read_b64_tr_b16 a[0:1], v12 offset:0\n"
  "v_mfma_f32_16x16x32_bf16 v[100:103], a[72:75], a[116:119], v[100:103]\n"
  "ds_read_b64_tr_b16 a[2:3], v12 offset:512\n"
  "ds_read_b64_tr_b16 a[132:133], v49 offset:8\n"
  "v_mfma_f32_16x16x32_bf16 v[104:107], a[72:75], a[120:123], v[104:107]\n"
  "ds_read_b64_tr_b16 a[4:5], v14 offset:0\n"
  "v_mfma_f32_16x16x32_bf16 v[108:111], a[72:75], a[124:127], v[108:111]\n"
  "ds_read_b64_tr_b16 a[6:7], v14 offset:512\n"
  "v_mfma_f32_16x16x32_bf16 v[112:115], a[76:79], a[112:115], v[112:115]\n"
  "s_add_u32 m0, s64, 73728\n"
  "v_mfma_f32_16x16x32_bf16 v[116:119], a[76:79], a[116:119], v[116:119]\n"
  "buffer_load_dwordx4 v2, s[28:31], s63 offen lds\n"
  "v_mfma_f32_16x16x32_bf16 v[120:123], a[76:79], a[120:123], v[120:123]\n"
  "ds_read_b64_tr_b16 a[8:9], v12 offset:4096\n"
  "ds_read_b64_tr_b16 a[134:135], v49 offset:648\n"
  "v_mfma_f32_16x16x32_bf16 v[124:127], a[76:79], a[124:127], v[124:127]\n"
  "ds_read_b64_tr_b16 a[10:11], v12 offset:4608\n"
  "v_mfma_f32_16x16x32_bf16 v[128:131], a[80:83], a[112:115], v[128:131]\n"
  "ds_read_b64_tr_b16 a[12:13], v14 offset:4096\n"
  "v_mfma_f32_16x16x32_bf16 v[132:135], a[80:83], a[116:119], v[132:135]\n"
  "ds_read_b64_tr_b16 a[14:15], v14 offset:4608\n"
  "v_mfma_f32_16x16x32_bf16 v[136:139], a[80:83], a[120:123], v[136:139]\n"
  "s_add_u32 m0, s64, 77824\n"
  "ds_read_b64_tr_b16 a[136:137], v49 offset:16\n"
  "v_mfma_f32_16x16x32_bf16 v[140:143], a[80:83], a[124:127], v[140:143]\n"
  "buffer_load_dwordx4 v2, s[32:35], s63 offen lds\n"
  "v_mfma_f32_16x16x32_bf16 v[144:147], a[84:87], a[112:115], v[144:147]\n"
  "ds_read_b64_tr_b16 a[16:17], v12 offset:8192\n"
  "v_mfma_f32_16x16x32_bf16 v[148:151], a[84:87], a[116:119], v[148:151]\n"
  "ds_read_b64_tr_b16 a[18:19], v12 offset:8704\n"
  "v_mfma_f32_16x16x32_bf16 v[152:155], a[84:87], a[120:123], v[152:155]\n"
  "ds_read_b64_tr_b16 a[20:21], v14 offset:8192\n"
  "v_mfma_f32_16x16x32_bf16 v[156:159], a[84:87], a[124:127], v[156:159]\n"
  "ds_read_b64_tr_b16 a[22:23], v14 offset:8704\n"
  "ds_read_b64_tr_b16 a[138:139], v49 offset:656\n"
  "v_mfma_f32_16x16x32_bf16 v[160:163], a[88:91], a[112:115], v[160:163]\n"
  "s_add_u32 m0, s64, 81792\n"
  "v_mfma_f32_16x16x32_bf16 v[164:167], a[88:91], a[116:119], v[164:167]\n"
  "buffer_load_dwordx4 v2, s[28:31], s63 offen offset:128 lds\n"
  "v_mfma_f32_16x16x32_bf16 v[168:171], a[88:91], a[120:123], v[168:171]\n"
  "ds_read_b64_tr_b16 a[24:25], v12 offset:12288\n"
  "v_mfma_f32_16x16x32_bf16 v[172:175], a[88:91], a[124:127], v[172:175]\n"
  "ds_read_b64_tr_b16 a[26:27], v12 offset:12800\n"
  "v_mfma_f32_16x16x32_bf16 v[176:179], a[92:95], a[112:115], v[176:179]\n"
  "ds_read_b64_tr_b16 a[28:29], v14 offset:12288\n"
  "ds_read_b64_tr_b16 a[140:141], v49 offset:24\n"
  "v_mfma_f32_16x16x32_bf16 v[180:183], a[92:95], a[116:119], v[180:183]\n"
  "ds_read_b64_tr_b16 a[30:31], v14 offset:12800\n"
  "v_mfma_f32_16x16x32_bf16 v[184:187], a[92:95], a[120:123], v[184:187]\n"
  "s_add_u32 m0, s64, 85888\n"
  "v_mfma_f32_16x16x32_bf16 v[188:191], a[92:95], a[124:127], v[188:191]\n"
  "buffer_load_dwordx4 v2, s[32:35], s63 offen offset:128 lds\n"
  "v_mfma_f32_16x16x32_bf16 v[192:195], a[96:99], a[112:115], v[192:195]\n"
  "ds_read_b64_tr_b16 a[32:33], v12 offset:16384\n"
  "ds_read_b64_tr_b16 a[142:143], v49 offset:664\n"
  "v_mfma_f32_16x16x32_bf16 v[196:199], a[96:99], a[116:119], v[196:199]\n"
  "ds_read_b64_tr_b16 a[34:35], v12 offset:16896\n"
  "v_mfma_f32_16x16x32_bf16 v[200:203], a[96:99], a[120:123], v[200:203]\n"
  "ds_read_b64_tr_b16 a[36:37], v14 offset:16384\n"
  "v_mfma_f32_16x16x32_bf16 v[204:207], a[96:99], a[124:127], v[204:207]\n"
  "ds_read_b64_tr_b16 a[38:39], v14 offset:16896\n"
  "v_mfma_f32_16x16x32_bf16 v[208:211], a[100:103], a[112:115], v[208:211]\n"
  "s_add_u32 m0, s64, 89856\n"
  "v_mfma_f32_16x16x32_bf16 v[212:215], a[100:103], a[116:119], v[212:215]\n"
  "buffer_load_dwordx4 v2, s[28:31], s63 offen offset:256 lds\n"
  "ds_read_b64_tr_b16 a[144:145], v49 offset:32\n"
  "v_mfma_f32_16x16x32_bf16 v[216:219], a[100:103], a[120:123], v[216:219]\n"
  "ds_read_b64_tr_b16 a[40:41], v12 offset:20480\n"
  "v_mfma_f32_16x16x32_bf16 v[220:223], a[100:103], a[124:127], v[220:223]\n"
  "ds_read_b64_tr_b16 a[42:43], v12 offset:20992\n"
  "v_mfma_f32_16x16x32_bf16 v[224:227], a[104:107], a[112:115], v[224:227]\n"
  "ds_read_b64_tr_b16 a[44:45], v14 offset:20480\n"
  "v_mfma_f32_16x16x32_bf16 v[228:231], a[104:107], a[116:119], v[228:231]\n"
  "ds_read_b64_tr_b16 a[46:47], v14 offset:20992\n"
  "v_mfma_f32_16x16x32_bf16 v[232:235], a[104:107], a[120:123], v[232:235]\n"
  "s_add_u32 m0, s64, 93952\n"
  "ds_read_b64_tr_b16 a[146:147], v49 offset:672\n"
  "v_mfma_f32_16x16x32_bf16 v[236:239], a[104:107], a[124:127], v[236:239]\n"
  "buffer_load_dwordx4 v2, s[32:35], s63 offen offset:256 lds\n"
  "v_mfma_f32_16x16x32_bf16 v[240:243], a[108:111], a[112:115], v[240:243]\n"
  "v_mfma_f32_16x16x32_bf16 v[244:247], a[108:111], a[116:119], v[244:247]\n"
  "v_mfma_f32_16x16x32_bf16 v[248:251], a[108:111], a[120:123], v[248:251]\n"
  "v_mfma_f32_16x16x32_bf16 v[252:255], a[108:111], a[124:127], v[252:255]\n"
  "s_cmp_eq_u32 s51, 2\n"
  "s_cbranch_scc0 .Lcs7\n"
  "v_mfma_f32_16x16x32_bf16 a[148:151], a[64:67], v[52:55], a[148:151]\n"
  "v_mfma_f32_16x16x32_bf16 a[152:155], a[68:71], v[52:55], a[152:155]\n"
  "v_mfma_f32_16x16x32_bf16 a[156:159], a[72:75], v[52:55], a[156:159]\n"
  "v_mfma_f32_16x16x32_bf16 a[160:163], a[76:79], v[52:55], a[160:163]\n"
  "v_mfma_f32_16x16x32_bf16 a[164:167], a[80:83], v[52:55], a[164:167]\n"
  "v_mfma_f32_16x16x32_bf16 a[168:171], a[84:87], v[52:55], a[168:171]\n"
  ".Lcs7:\n"
  "s_cmp_eq_u32 s51, 3\n"
  "s_cbranch_scc0 .Lcs8\n"
  "v_mfma_f32_16x16x32_bf16 a[148:151], a[88:91], v[52:55], a[148:151]\n"
  "v_mfma_f32_16x16x32_bf16 a[152:155], a[92:95], v[52:55], a[152:155]\n"
  "v_mfma_f32_16x16x32_bf16 a[156:159], a[96:99], v[52:55], a[156:159]\n"
  "v_mfma_f32_16x16x32_bf16 a[160:163], a[100:103], v[52:55], a[160:163]\n"
  "v_mfma_f32_16x16x32_bf16 a[164:167], a[104:107], v[52:55], a[164:167]\n"
  "v_mfma_f32_16x16x32_bf16 a[168:171], a[108:111], v[52:55], a[168:171]\n"
  ".Lcs8:\n"
  "s_waitcnt vmcnt(16) lgkmcnt(0)\n"
  "s_barrier\n"
  "v_mfma_f32_16x16x32_bf16 v[64:67], a[0:3], a[48:51], v[64:67]\n"
  "s_mul_i32 s62, s56, 1536\n"
  "s_add_u32 m0, s65, 18432\n"
  "buffer_store_dwordx4 a[128:131], v48, s[36:39], s76 offen sc1\n"
  "v_mfma_f32_16x16x32_bf16 v[68:71], a[0:3], a[52:55], v[68:71]\n"
  "buffer_load_dwordx4 v8, s[24:27], s56 offen lds\n"
  "v_mfma_f32_16x16x32_bf16 v[72:75], a[0:3], a[56:59], v[72:75]\n"
  "ds_read_b128 a[112:115], v17 offset:32768\n"
  "v_mfma_f32_16x16x32_bf16 v[76:79], a[0:3], a[60:63], v[76:79]\n"
  "ds_read_b128 a[116:119], v17 offset:34816\n"
  "v_mfma_f32_16x16x32_bf16 v[80:83], a[4:7], a[48:51], v[80:83]\n"
  "ds_read_b128 a[120:123], v17 offset:36864\n"
  "v_mfma_f32_16x16x32_bf16 v[84:87], a[4:7], a[52:55], v[84:87]\n"
  "ds_read_b128 a[124:127], v17 offset:38912\n"
  "v_mfma_f32_16x16x32_bf16 v[88:91], a[4:7], a[56:59], v[88:91]\n"
  "s_add_u32 m0, s65, 19456\n"
  "v_mfma_f32_16x16x32_bf16 v[92:95], a[4:7], a[60:63], v[92:95]\n"
  "buffer_load_dwordx4 v9, s[24:27], s56 offen lds\n"
  "buffer_store_dwordx4 a[132:135], v48, s[36:39], s76 offen offset:1024 sc1\n"
  "v_mfma_f32_16x16x32_bf16 v[96:99], a[8:11], a[48:51], v[96:99]\n"
  "ds_read_b64_tr_b16 a[64:65], v12 offset:24576\n"
  "v_mfma_f32_16x16x32_bf16 v[100:103], a[8:11], a[52:55], v[100:103]\n"
  "ds_read_b64_tr_b16 a[66:67], v12 offset:25088\n"
  "v_mfma_f32_16x16x32_bf16 v[104:107], a[8:11], a[56:59], v[104:107]\n"
  "ds_read_b64_tr_b16 a[68:69], v14 offset:24576\n"
  "v_mfma_f32_16x16x32_bf16 v[108:111], a[8:11], a[60:63], v[108:111]\n"
  "ds_read_b64_tr_b16 a[70:71], v14 offset:25088\n"
  "v_mfma_f32_16x16x32_bf16 v[112:115], a[12:15], a[48:51], v[112:115]\n"
  "s_add_u32 m0, s64, 0\n"
  "s_nop 0\n"
  "v_mfma_f32_16x16x32_bf16 v[116:119], a[12:15], a[52:55], v[116:119]\n"
  "buffer_load_dwordx4 v2, s[28:31], s62 offen lds\n"
  "v_mfma_f32_16x16x32_bf16 v[120:123], a[12:15], a[56:59], v[120:123]\n"
  "ds_read_b64_tr_b16 a[72:73], v12 offset:28672\n"
  "v_mfma_f32_16x16x32_bf16 v[124:127], a[12:15], a[60:63], v[124:127]\n"
  "ds_read_b64_tr_b16 a[74:75], v12 offset:29184\n"
  "buffer_store_dwordx4 a[136:139], v48, s[36:39], s76 offen offset:2048 sc1\n"
  "v_mfma_f32_16x16x32_bf16 v[128:131], a[16:19], a[48:51], v[128:131]\n"
  "ds_read_b64_tr_b16 a[76:77], v14 offset:28672\n"
  "v_mfma_f32_16x16x32_bf16 v[132:135], a[16:19], a[52:55], v[132:135]\n"
  "ds_read_b64_tr_b16 a[78:79], v14 offset:29184\n"
  "v_mfma_f32_16x16x32_bf16 v[136:139], a[16:19], a[56:59], v[136:139]\n"
  "s_add_u32 m0, s64, 4096\n"
  "v_mfma_f32_16x16x32_bf16 v[140:143], a[16:19], a[60:63], v[140:143]\n"
  "buffer_load_dwordx4 v2, s[32:35], s62 offen lds\n"
  "v_mfma_f32_16x16x32_bf16 v[144:147], a[20:23], a[48:51], v[144:147]\n"
  "ds_read_b64_tr_b16 a[80:81], v12 offset:32768\n"
  "v_mfma_f32_16x16x32_bf16 v[148:151], a[20:23], a[52:55], v[148:151]\n"
  "ds_read_b64_tr_b16 a[82:83], v12 offset:33280\n"
  "v_mfma_f32_16x16x32_bf16 v[152:155], a[20:23], a[56:59], v[152:155]\n"
  "ds_read_b64_tr_b16 a[84:85], v14 offset:32768\n"
  "v_mfma_f32_16x16x32_bf16 v[156:159], a[20:23], a[60:63], v[156:159]\n"
  "ds_read_b64_tr_b16 a[86:87], v14 offset:33280\n"
  "buffer_store_dwordx4 a[140:143], v48, s[36:39], s76 offen offset:3072 sc1\n"
  "v_mfma_f32_16x16x32_bf16 v[160:163], a[24:27], a[48:51], v[160:163]\n"
  "s_add_u32 m0, s64, 8064\n"
  "v_mfma_f32_16x16x32_bf16 v[164:167], a[24:27], a[52:55], v[164:167]\n"
  "buffer_load_dwordx4 v2, s[28:31], s62 offen offset:128 lds\n"
  "v_mfma_f32_16x16x32_bf16 v[168:171], a[24:27], a[56:59], v[168:171]\n"
  "ds_read_b64_tr_b16 a[88:89], v12 offset:36864\n"
  "v_mfma_f32_16x16x32_bf16 v[172:175], a[24:27], a[60:63], v[172:175]\n"
  "ds_read_b64_tr_b16 a[90:91], v12 offset:37376\n"
  "v_mfma_f32_16x16x32_bf16 v[176:179], a[28:31], a[48:51], v[176:179]\n"
  "ds_read_b64_tr_b16 a[92:93], v14 offset:36864\n"
  "v_mfma_f32_16x16x32_bf16 v[180:183], a[28:31], a[52:55], v[180:183]\n"
  "ds_read_b64_tr_b16 a[94:95], v14 offset:37376\n"
  "v_mfma_f32_16x16x32_bf16 v[184:187], a[28:31], a[56:59], v[184:187]\n"
  "s_add_u32 m0, s64, 12160\n"
  "v_mfma_f32_16x16x32_bf16 v[188:191], a[28:31], a[60:63], v[188:191]\n"
  "buffer_load_dwordx4 v2, s[32:35], s62 offen offset:128 lds\n"
  "s_add_u32 s76, s76, 4096\n"
  "v_mfma_f32_16x16x32_bf16 v[192:195], a[32:35], a[48:51], v[192:195]\n"
  "ds_read_b64_tr_b16 a[96:97], v12 offset:40960\n"
  "v_mfma_f32_16x16x32_bf16 v[196:199], a[32:35], a[52:55], v[196:199]\n"
  "ds_read_b64_tr_b16 a[98:99], v12 offset:41472\n"
  "v_mfma_f32_16x16x32_bf16 v[200:203], a[32:35], a[56:59], v[200:203]\n"
  "ds_read_b64_tr_b16 a[100:101], v14 offset:40960\n"
  "v_mfma_f32_16x16x32_bf16 v[204:207], a[32:35], a[60:63], v[204:207]\n"
  "ds_read_b64_tr_b16 a[102:103], v14 offset:41472\n"
  "v_mfma_f32_16x16x32_bf16 v[208:211], a[36:39], a[48:51], v[208:211]\n"
  "s_add_u32 m0, s64, 16128\n"
  "v_mfma_f32_16x16x32_bf16 v[212:215], a[36:39], a[52:55], v[212:215]\n"
  "buffer_load_dwordx4 v2, s[28:31], s62 offen offset:256 lds\n"
  "v_mfma_f32_16x16x32_bf16 v[216:219], a[36:39], a[56:59], v[216:219]\n"
  "ds_read_b64_tr_b16 a[104:105], v12 offset:45056\n"
  "v_mfma_f32_16x16x32_bf16 v[220:223], a[36:39], a[60:63], v[220:223]\n"
  "ds_read_b64_tr_b16 a[106:107], v12 offset:45568\n"
  "buffer_store_dwordx4 a[144:147], v48, s[36:39], s76 offen sc1\n"
  "v_mfma_f32_16x16x32_bf16 v[224:227], a[40:43], a[48:51], v[224:227]\n"
  "ds_read_b64_tr_b16 a[108:109], v14 offset:45056\n"
  "v_mfma_f32_16x16x32_bf16 v[228:231], a[40:43], a[52:55], v[228:231]\n"
  "ds_read_b64_tr_b16 a[110:111], v14 offset:45568\n"
  "v_mfma_f32_16x16x32_bf16 v[232:235], a[40:43], a[56:59], v[232:235]\n"
  "s_add_u32 m0, s64, 20224\n"
  "v_mfma_f32_16x16x32_bf16 v[236:239], a[40:43], a[60:63], v[236:239]\n"
  "buffer_load_dwordx4 v2, s[32:35], s62 offen offset:256 lds\n"
  "v_mfma_f32_16x16x32_bf16 v[240:243], a[44:47], a[48:51], v[240:243]\n"
  "v_mfma_f32_16x16x32_bf16 v[244:247], a[44:47], a[52:55], v[244:247]\n"
  "v_mfma_f32_16x16x32_bf16 v[248:251], a[44:47], a[56:59], v[248:251]\n"
  "v_mfma_f32_16x16x32_bf16 v[252:255], a[44:47], a[60:63], v[252:255]\n"
  "s_cmp_eq_u32 s51, 4\n"
  "s_cbranch_scc0 .Lcs9\n"
  "v_mfma_f32_16x16x32_bf16 a[148:151], a[0:3], v[52:55], 0\n"
  "v_mfma_f32_16x16x32_bf16 a[152:155], a[4:7], v[52:55], 0\n"
  "v_mfma_f32_16x16x32_bf16 a[156:159], a[8:11], v[52:55], 0\n"
  "v_mfma_f32_16x16x32_bf16 a[160:163], a[12:15], v[52:55], 0\n"
  "v_mfma_f32_16x16x32_bf16 a[164:167], a[16:19], v[52:55], 0\n"
  "v_mfma_f32_16x16x32_bf16 a[168:171], a[20:23], v[52:55], 0\n"
  ".Lcs9:\n"
  "s_cmp_eq_u32 s51, 5\n"
  "s_cbranch_scc0 .Lcs10\n"
  "s_nop 0\n"
  "s_nop 0\n"
  "v_mfma_f32_16x16x32_bf16 a[148:151], a[24:27], v[52:55], 0\n"
  "v_mfma_f32_16x16x32_bf16 a[152:155], a[28:31], v[52:55], 0\n"
  "v_mfma_f32_16x16x32_bf16 a[156:159], a[32:35], v[52:55], 0\n"
  "v_mfma_f32_16x16x32_bf16 a[160:163], a[36:39], v[52:55], 0\n"
  "v_mfma_f32_16x16x32_bf16 a[164:167], a[40:43], v[52:55], 0\n"
  "v_mfma_f32_16x16x32_bf16 a[168:171], a[44:47], v[52:55], 0\n"
  ".Lcs10:\n"
  "s_waitcnt vmcnt(21) lgkmcnt(0)\n"
  "s_barrier\n"
  "v_mfma_f32_16x16x32_bf16 v[64:67], a[64:67], a[112:115], v[64:67]\n"
  "s_add_u32 s63, s62, 98304\n"
  "s_add_u32 m0, s65, 32768\n"
  "v_mfma_f32_16x16x32_bf16 v[68:71], a[64:67], a[116:119], v[68:71]\n"
  "buffer_load_dwordx4 v6, s[24:27], s57 offen lds\n"
  "v_mfma_f32_16x16x32_bf16 v[72:75], a[64:67], a[120:123], v[72:75]\n"
  "ds_read_b128 a[48:51], v16 offset:0\n"
  "v_mfma_f32_16x16x32_bf16 v[76:79], a[64:67], a[124:127], v[76:79]\n"
  "ds_read_b128 a[52:55], v16 offset:2048\n"
  "v_mfma_f32_16x16x32_bf16 v[80:83], a[68:71], a[112:115], v[80:83]\n"
  "ds_read_b128 a[56:59], v16 offset:4096\n"
  "v_mfma_f32_16x16x32_bf16 v[84:87], a[68:71], a[116:119], v[84:87]\n"
  "ds_read_b128 a[60:63], v16 offset:6144\n"
  "v_mfma_f32_16x16x32_bf16 v[88:91], a[68:71], a[120:123], v[88:91]\n"
  "s_add_u32 m0, s65, 33792\n"
  "v_mfma_f32_16x16x32_bf16 v[92:95], a[68:71], a[124:127], v[92:95]\n"
  "buffer_load_dwordx4 v7, s[24:27], s57 offen lds\n"
  "v_mfma_f32_16x16x32_bf16 v[96:99], a[72:75], a[112:115], v[96:99]\n"
  "ds_read_b64_tr_b16 a[0:1], v13 offset:0\n"
  "v_mfma_f32_16x16x32_bf16 v[100:103], a[72:75], a[116:119], v[100:103]\n"
  "ds_read_b64_tr_b16 a[2:3], v13 offset:512\n"
  "v_mfma_f32_16x16x32_bf16 v[104:107], a[72:75], a[120:123], v[104:107]\n"
  "ds_read_b64_tr_b16 a[4:5], v15 offset:0\n"
  "v_mfma_f32_16x16x32_bf16 v[108:111], a[72:75], a[124:127], v[108:111]\n"
  "ds_read_b64_tr_b16 a[6:7], v15 offset:512\n"
  "v_mfma_f32_16x16x32_bf16 v[112:115], a[76:79], a[112:115], v[112:115]\n"
  "s_add_u32 m0, s64, 24576\n"
  "v_mfma_f32_16x16x32_bf16 v[116:119], a[76:79], a[116:119], v[116:119]\n"
  "buffer_load_dwordx4 v2, s[28:31], s63 offen lds\n"
  "v_mfma_f32_16x16x32_bf16 v[120:123], a[76:79], a[120:123], v[120:123]\n"
  "ds_read_b64_tr_b16 a[8:9], v13 offset:4096\n"
  "v_mfma_f32_16x16x32_bf16 v[124:127], a[76:79], a[124:127], v[124:127]\n"
  "ds_read_b64_tr_b16 a[10:11], v13 offset:4608\n"
  "v_mfma_f32_16x16x32_bf16 v[128:131], a[80:83], a[112:115], v[128:131]\n"
  "ds_read_b64_tr_b16 a[12:13], v15 offset:4096\n"
  "v_mfma_f32_16x16x32_bf16 v[132:135], a[80:83], a[116:119], v[132:135]\n"
  "ds_read_b64_tr_b16 a[14:15], v15 offset:4608\n"
  "v_mfma_f32_16x16x32_bf16 v[136:139], a[80:83], a[120:123], v[136:139]\n"
  "s_add_u32 m0, s64, 28672\n"
  "v_mfma_f32_16x16x32_bf16 v[140:143], a[80:83], a[124:127], v[140:143]\n"
  "buffer_load_dwordx4 v2, s[32:35], s63 offen lds\n"
  "v_mfma_f32_16x16x32_bf16 v[144:147], a[84:87], a[112:115], v[144:147]\n"
  "ds_read_b64_tr_b16 a[16:17], v13 offset:8192\n"
  "v_mfma_f32_16x16x32_bf16 v[148:151], a[84:87], a[116:119], v[148:151]\n"
  "ds_read_b64_tr_b16 a[18:19], v13 offset:8704\n"
  "v_mfma_f32_16x16x32_bf16 v[152:155], a[84:87], a[120:123], v[152:155]\n"
  "ds_read_b64_tr_b16 a[20:21], v15 offset:8192\n"
  "v_mfma_f32_16x16x32_bf16 v[156:159], a[84:87], a[124:127], v[156:159]\n"
  "ds_read_b64_tr_b16 a[22:23], v15 offset:8704\n"
  "v_mfma_f32_16x16x32_bf16 v[160:163], a[88:91], a[112:115], v[160:163]\n"
  "s_add_u32 m0, s64, 32640\n"
  "v_mfma_f32_16x16x32_bf16 v[164:167], a[88:91], a[116:119], v[164:167]\n"
  "buffer_load_dwordx4 v2, s[28:31], s63 offen offset:128 lds\n"
  "v_mfma_f32_16x16x32_bf16 v[168:171], a[88:91], a[120:123], v[168:171]\n"
  "ds_read_b64_tr_b16 a[24:25], v13 offset:12288\n"
  "v_mfma_f32_16x16x32_bf16 v[172:175], a[88:91], a[124:127], v[172:175]\n"
  "ds_read_b64_tr_b16 a[26:27], v13 offset:12800\n"
  "v_mfma_f32_16x16x32_bf16 v[176:179], a[92:95], a[112:115], v[176:179]\n"
  "ds_read_b64_tr_b16 a[28:29], v15 offset:12288\n"
  "v_mfma_f32_16x16x32_bf16 v[180:183], a[92:95], a[116:119], v[180:183]\n"
  "ds_read_b64_tr_b16 a[30:31], v15 offset:12800\n"
  "v_mfma_f32_16x16x32_bf16 v[184:187], a[92:95], a[120:123], v[184:187]\n"
  "s_add_u32 m0, s64, 36736\n"
  "v_mfma_f32_16x16x32_bf16 v[188:191], a[92:95], a[124:127], v[188:191]\n"
  "buffer_load_dwordx4 v2, s[32:35], s63 offen offset:128 lds\n"
  "v_mfma_f32_16x16x32_bf16 v[192:195], a[96:99], a[112:115], v[192:195]\n"
  "ds_read_b64_tr_b16 a[32:33], v13 offset:16384\n"
  "v_mfma_f32_16x16x32_bf16 v[196:199], a[96:99], a[116:119], v[196:199]\n"
  "ds_read_b64_tr_b16 a[34:35], v13 offset:16896\n"
  "v_mfma_f32_16x16x32_bf16 v[200:203], a[96:99], a[120:123], v[200:203]\n"
  "ds_read_b64_tr_b16 a[36:37], v15 offset:16384\n"
  "v_mfma_f32_16x16x32_bf16 v[204:207], a[96:99], a[124:127], v[204:207]\n"
  "ds_read_b64_tr_b16 a[38:39], v15 offset:16896\n"
  "v_mfma_f32_16x16x32_bf16 v[208:211], a[100:103], a[112:115], v[208:211]\n"
  "s_add_u32 m0, s64, 40704\n"
  "v_mfma_f32_16x16x32_bf16 v[212:215], a[100:103], a[116:119], v[212:215]\n"
  "buffer_load_dwordx4 v2, s[28:31], s63 offen offset:256 lds\n"
  "v_mfma_f32_16x16x32_bf16 v[216:219], a[100:103], a[120:123], v[216:219]\n"
  "ds_read_b64_tr_b16 a[40:41], v13 offset:20480\n"
  "v_mfma_f32_16x16x32_bf16 v[220:223], a[100:103], a[124:127], v[220:223]\n"
  "ds_read_b64_tr_b16 a[42:43], v13 offset:20992\n"
  "v_mfma_f32_16x16x32_bf16 v[224:227], a[104:107], a[112:115], v[224:227]\n"
  "ds_read_b64_tr_b16 a[44:45], v15 offset:20480\n"
  "v_mfma_f32_16x16x32_bf16 v[228:231], a[104:107], a[116:119], v[228:231]\n"
  "ds_read_b64_tr_b16 a[46:47], v15 offset:20992\n"
  "v_mfma_f32_16x16x32_bf16 v[232:235], a[104:107], a[120:123], v[232:235]\n"
  "s_add_u32 m0, s64, 44800\n"
  "v_mfma_f32_16x16x32_bf16 v[236:239], a[104:107], a[124:127], v[236:239]\n"
  "buffer_load_dwordx4 v2, s[32:35], s63 offen offset:256 lds\n"
  "v_mfma_f32_16x16x32_bf16 v[240:243], a[108:111], a[112:115], v[240:243]\n"
  "v_mfma_f32_16x16x32_bf16 v[244:247], a[108:111], a[116:119], v[244:247]\n"
  "v_mfma_f32_16x16x32_bf16 v[248:251], a[108:111], a[120:123], v[248:251]\n"
  "v_mfma_f32_16x16x32_bf16 v[252:255], a[108:111], a[124:127], v[252:255]\n"
  "s_cmp_eq_u32 s51, 4\n"
  "s_cbranch_scc0 .Lcs11\n"
  "v_mfma_f32_16x16x32_bf16 a[148:151], a[64:67], v[52:55], a[148:151]\n"
  "v_mfma_f32_16x16x32_bf16 a[152:155], a[68:71], v[52:55], a[152:155]\n"
  "v_mfma_f32_16x16x32_bf16 a[156:159], a[72:75], v[52:55], a[156:159]\n"
  "v_mfma_f32_16x16x32_bf16 a[160:163], a[76:79], v[52:55], a[160:163]\n"
  "v_mfma_f32_16x16x32_bf16 a[164:167], a[80:83], v[52:55], a[164:167]\n"
  "v_mfma_f32_16x16x32_bf16 a[168:171], a[84:87], v[52:55], a[168:171]\n"
  ".Lcs11:\n"
  "s_cmp_eq_u32 s51, 5\n"
  "s_cbranch_scc0 .Lcs12\n"
  "v_mfma_f32_16x16x32_bf16 a[148:151], a[88:91], v[52:55], a[148:151]\n"
  "v_mfma_f32_16x16x32_bf16 a[152:155], a[92:95], v[52:55], a[152:155]\n"
  "v_mfma_f32_16x16x32_bf16 a[156:159], a[96:99], v[52:55], a[156:159]\n"
  "v_mfma_f32_16x16x32_bf16 a[160:163], a[100:103], v[52:55], a[160:163]\n"
  "v_mfma_f32_16x16x32_bf16 a[164:167], a[104:107], v[52:55], a[164:167]\n"
  "v_mfma_f32_16x16x32_bf16 a[168:171], a[108:111], v[52:55], a[168:171]\n"
  ".Lcs12:\n"
  "s_waitcnt vmcnt(21) lgkmcnt(0)\n"
  "s_barrier\n"
  "v_mfma_f32_16x16x32_bf16 v[64:67], a[0:3], a[48:51], v[64:67]\n"
  "s_mul_i32 s62, s57, 1536\n"
  "s_add_u32 m0, s65, 34816\n"
  "v_mfma_f32_16x16x32_bf16 v[68:71], a[0:3], a[52:55], v[68:71]\n"
  "buffer_load_dwordx4 v8, s[24:27], s57 offen lds\n"
  "v_mfma_f32_16x16x32_bf16 v[72:75], a[0:3], a[56:59], v[72:75]\n"
  "ds_read_b128 a[112:115], v17 offset:0\n"
  "v_mfma_f32_16x16x32_bf16 v[76:79], a[0:3], a[60:63], v[76:79]\n"
  "ds_read_b128 a[116:119], v17 offset:2048\n"
  "v_mfma_f32_16x16x32_bf16 v[80:83], a[4:7], a[48:51], v[80:83]\n"
  "ds_read_b128 a[120:123], v17 offset:4096\n"
  "v_mfma_f32_16x16x32_bf16 v[84:87], a[4:7], a[52:55], v[84:87]\n"
  "ds_read_b128 a[124:127], v17 offset:6144\n"
  "v_mfma_f32_16x16x32_bf16 v[88:91], a[4:7], a[56:59], v[88:91]\n"
  "s_add_u32 m0, s65, 35840\n"
  "v_mfma_f32_16x16x32_bf16 v[92:95], a[4:7], a[60:63], v[92:95]\n"
  "buffer_load_dwordx4 v9, s[24:27], s57 offen lds\n"
  "v_mfma_f32_16x16x32_bf16 v[96:99], a[8:11], a[48:51], v[96:99]\n"
  "ds_read_b64_tr_b16 a[64:65], v13 offset:24576\n"
  "v_mfma_f32_16x16x32_bf16 v[100:103], a[8:11], a[52:55], v[100:103]\n"
  "ds_read_b64_tr_b16 a[66:67], v13 offset:25088\n"
  "v_mfma_f32_16x16x32_bf16 v[104:107], a[8:11], a[56:59], v[104:107]\n"
  "ds_read_b64_tr_b16 a[68:69], v15 offset:24576\n"
  "v_mfma_f32_16x16x32_bf16 v[108:111], a[8:11], a[60:63], v[108:111]\n"
  "ds_read_b64_tr_b16 a[70:71], v15 offset:25088\n"
  "v_mfma_f32_16x16x32_bf16 v[112:115], a[12:15], a[48:51], v[112:115]\n"
  "s_add_u32 m0, s64, 49152\n"
  "v_mfma_f32_16x16x32_bf16 v[116:119], a[12:15], a[52:55], v[116:119]\n"
  "buffer_load_dwordx4 v2, s[28:31], s62 offen lds\n"
  "v_mfma_f32_16x16x32_bf16 v[120:123], a[12:15], a[56:59], v[120:123]\n"
  "ds_read_b64_tr_b16 a[72:73], v13 offset:28672\n"
  "v_mfma_f32_16x16x32_bf16 v[124:127], a[12:15], a[60:63], v[124:127]\n"
  "ds_read_b64_tr_b16 a[74:75], v13 offset:29184\n"
  "v_mfma_f32_16x16x32_bf16 v[128:131], a[16:19], a[48:51], v[128:131]\n"
  "ds_read_b64_tr_b16 a[76:77], v15 offset:28672\n"
  "v_mfma_f32_16x16x32_bf16 v[132:135], a[16:19], a[52:55], v[132:135]\n"
  "ds_read_b64_tr_b16 a[78:79], v15 offset:29184\n"
  "v_mfma_f32_16x16x32_bf16 v[136:139], a[16:19], a[56:59], v[136:139]\n"
  "s_add_u32 m0, s64, 53248\n"
  "v_mfma_f32_16x16x32_bf16 v[140:143], a[16:19], a[60:63], v[140:143]\n"
  "buffer_load_dwordx4 v2, s[32:35], s62 offen lds\n"
  "v_mfma_f32_16x16x32_bf16 v[144:147], a[20:23], a[48:51], v[144:147]\n"
  "ds_read_b64_tr_b16 a[80:81], v13 offset:32768\n"
  "v_mfma_f32_16x16x32_bf16 v[148:151], a[20:23], a[52:55], v[148:151]\n"
  "ds_read_b64_tr_b16 a[82:83], v13 offset:33280\n"
  "v_mfma_f32_16x16x32_bf16 v[152:155], a[20:23], a[56:59], v[152:155]\n"
  "ds_read_b64_tr_b16 a[84:85], v15 offset:32768\n"
  "v_mfma_f32_16x16x32_bf16 v[156:159], a[20:23], a[60:63], v[156:159]\n"
  "ds_read_b64_tr_b16 a[86:87], v15 offset:33280\n"
  "v_mfma_f32_16x16x32_bf16 v[160:163], a[24:27], a[48:51], v[160:163]\n"
  "s_add_u32 m0, s64, 57216\n"
  "v_mfma_f32_16x16x32_bf16 v[164:167], a[24:27], a[52:55], v[164:167]\n"
  "buffer_load_dwordx4 v2, s[28:31], s62 offen offset:128 lds\n"
  "v_mfma_f32_16x16x32_bf16 v[168:171], a[24:27], a[56:59], v[168:171]\n"
  "ds_read_b64_tr_b16 a[88:89], v13 offset:36864\n"
  "v_mfma_f32_16x16x32_bf16 v[172:175], a[24:27], a[60:63], v[172:175]\n"
  "ds_read_b64_tr_b16 a[90:91], v13 offset:37376\n"
  "v_mfma_f32_16x16x32_bf16 v[176:179], a[28:31], a[48:51], v[176:179]\n"
  "ds_read_b64_tr_b16 a[92:93], v15 offset:36864\n"
  "v_mfma_f32_16x16x32_bf16 v[180:183], a[28:31], a[52:55], v[180:183]\n"
  "ds_read_b64_tr_b16 a[94:95], v15 offset:37376\n"
  "v_mfma_f32_16x16x32_bf16 v[184:187], a[28:31], a[56:59], v[184:187]\n"
  "s_add_u32 m0, s64, 61312\n"
  "v_mfma_f32_16x16x32_bf16 v[188:191], a[28:31], a[60:63], v[188:191]\n"
  "buffer_load_dwordx4 v2, s[32:35], s62 offen offset:128 lds\n"
  "v_mfma_f32_16x16x32_bf16 v[192:195], a[32:35], a[48:51], v[192:195]\n"
  "ds_read_b64_tr_b16 a[96:97], v13 offset:40960\n"
  "v_mfma_f32_16x16x32_bf16 v[196:199], a[32:35], a[52:55], v[196:199]\n"
  "ds_read_b64_tr_b16 a[98:99], v13 offset:41472\n"
  "v_mfma_f32_16x16x32_bf16 v[200:203], a[32:35], a[56:59], v[200:203]\n"
  "ds_read_b64_tr_b16 a[100:101], v15 offset:40960\n"
  "v_mfma_f32_16x16x32_bf16 v[204:207], a[32:35], a[60:63], v[204:207]\n"
  "ds_read_b64_tr_b16 a[102:103], v15 offset:41472\n"
  "v_mfma_f32_16x16x32_bf16 v[208:211], a[36:39], a[48:51], v[208:211]\n"
  "s_add_u32 m0, s64, 65280\n"
  "v_mfma_f32_16x16x32_bf16 v[212:215], a[36:39], a[52:55], v[212:215]\n"
  "buffer_load_dwordx4 v2, s[28:31], s62 offen offset:256 lds\n"
  "v_mfma_f32_16x16x32_bf16 v[216:219], a[36:39], a[56:59], v[216:219]\n"
  "ds_read_b64_tr_b16 a[104:105], v13 offset:45056\n"
  "v_mfma_f32_16x16x32_bf16 v[220:223], a[36:39], a[60:63], v[220:223]\n"
  "ds_read_b64_tr_b16 a[106:107], v13 offset:45568\n"
  "v_mfma_f32_16x16x32_bf16 v[224:227], a[40:43], a[48:51], v[224:227]\n"
  "ds_read_b64_tr_b16 a[108:109], v15 offset:45056\n"
  "v_mfma_f32_16x16x32_bf16 v[228:231], a[40:43], a[52:55], v[228:231]\n"
  "ds_read_b64_tr_b16 a[110:111], v15 offset:45568\n"
  "v_mfma_f32_16x16x32_bf16 v[232:235], a[40:43], a[56:59], v[232:235]\n"
  "s_add_u32 m0, s64, 69376\n"
  "v_mfma_f32_16x16x32_bf16 v[236:239], a[40:43], a[60:63], v[236:239]\n"
  "buffer_load_dwordx4 v2, s[32:35], s62 offen offset:256 lds\n"
  "v_mfma_f32_16x16x32_bf16 v[240:243], a[44:47], a[48:51], v[240:243]\n"
  "v_mfma_f32_16x16x32_bf16 v[244:247], a[44:47], a[52:55], v[244:247]\n"
  "v_mfma_f32_16x16x32_bf16 v[248:251], a[44:47], a[56:59], v[248:251]\n"
  "v_mfma_f32_16x16x32_bf16 v[252:255], a[44:47], a[60:63], v[252:255]\n"
  "s_cmp_eq_u32 s51, 6\n"
  "s_cbranch_scc0 .Lcs13\n"
  "v_mfma_f32_16x16x32_bf16 a[148:151], a[0:3], v[52:55], 0\n"
  "v_mfma_f32_16x16x32_bf16 a[152:155], a[4:7], v[52:55], 0\n"
  "v_mfma_f32_16x16x32_bf16 a[156:159], a[8:11], v[52:55], 0\n"
  "v_mfma_f32_16x16x32_bf16 a[160:163], a[12:15], v[52:55], 0\n"
  "v_mfma_f32_16x16x32_bf16 a[164:167], a[16:19], v[52:55], 0\n"
  "v_mfma_f32_16x16x32_bf16 a[168:171], a[20:23], v[52:55], 0\n"
  ".Lcs13:\n"
  "s_cmp_eq_u32 s51, 7\n"
  "s_cbranch_scc0 .Lcs14\n"
  "s_nop 0\n"
  "s_nop 0\n"
  "v_mfma_f32_16x16x32_bf16 a[148:151], a[24:27], v[52:55], 0\n"
  "v_mfma_f32_16x16x32_bf16 a[152:155], a[28:31], v[52:55], 0\n"
  "v_mfma_f32_16x16x32_bf16 a[156:159], a[32:35], v[52:55], 0\n"
  "v_mfma_f32_16x16x32_bf16 a[160:163], a[36:39], v[52:55], 0\n"
  "v_mfma_f32_16x16x32_bf16 a[164:167], a[40:43], v[52:55], 0\n"
  "v_mfma_f32_16x16x32_bf16 a[168:171], a[44:47], v[52:55], 0\n"
  ".Lcs14:\n"
  "s_waitcnt vmcnt(16) lgkmcnt(0)\n"
  "s_barrier\n"
  "v_mfma_f32_16x16x32_bf16 v[64:67], a[64:67], a[112:115], v[64:67]\n"
  "s_add_u32 s63, s62, 98304\n"
  "s_add_u32 m0, s65, 0\n"
  "s_lshl_b32 s70, s50, 3\n"
  "v_mfma_f32_16x16x32_bf16 v[68:71], a[64:67], a[116:119], v[68:71]\n"
  "buffer_load_dwordx4 v6, s[24:27], s58 offen lds\n"
  "s_add_u32 s70, s70, 4096\n"
  "v_mfma_f32_16x16x32_bf16 v[72:75], a[64:67], a[120:123], v[72:75]\n"
  "ds_read_b128 a[48:51], v16 offset:16384\n"
  "v_mov_b32_e64 v24, s44\n"
  "v_mfma_f32_16x16x32_bf16 v[76:79], a[64:67], a[124:127], v[76:79]\n"
  "ds_read_b128 a[52:55], v16 offset:18432\n"
  "v_mov_b32_e64 v25, s45\n"
  "v_mfma_f32_16x16x32_bf16 v[80:83], a[68:71], a[112:115], v[80:83]\n"
  "ds_read_b128 a[56:59], v16 offset:20480\n"
  "s_mov_b64 exec, s[88:89]\n"
  "s_nop 0\n"
  "buffer_store_dwordx2 v[24:25], off, s[36:39], s70 sc1\n"
  "s_mov_b64 exec, -1\n"
  "s_nop 0\n"
  "v_mfma_f32_16x16x32_bf16 v[84:87], a[68:71], a[116:119], v[84:87]\n"
  "ds_read_b128 a[60:63], v16 offset:22528\n"
  "v_mfma_f32_16x16x32_bf16 v[88:91], a[68:71], a[120:123], v[88:91]\n"
  "s_add_u32 m0, s65, 1024\n"
  "v_mfma_f32_16x16x32_bf16 v[92:95], a[68:71], a[124:127], v[92:95]\n"
  "buffer_load_dwordx4 v7, s[24:27], s58 offen lds\n"
  "v_mfma_f32_16x16x32_bf16 v[96:99], a[72:75], a[112:115], v[96:99]\n"
  "ds_read_b64_tr_b16 a[0:1], v12 offset:0\n"
  "v_mfma_f32_16x16x32_bf16 v[100:103], a[72:75], a[116:119], v[100:103]\n"
  "ds_read_b64_tr_b16 a[2:3], v12 offset:512\n"
  "v_mfma_f32_16x16x32_bf16 v[104:107], a[72:75], a[120:123], v[104:107]\n"
  "ds_read_b64_tr_b16 a[4:5], v14 offset:0\n"
  "v_mfma_f32_16x16x32_bf16 v[108:111], a[72:75], a[124:127], v[108:111]\n"
  "ds_read_b64_tr_b16 a[6:7], v14 offset:512\n"
  "v_mfma_f32_16x16x32_bf16 v[112:115], a[76:79], a[112:115], v[112:115]\n"
  "s_add_u32 m0, s64, 73728\n"
  "v_mfma_f32_16x16x32_bf16 v[116:119], a[76:79], a[116:119], v[116:119]\n"
  "buffer_load_dwordx4 v2, s[28:31], s63 offen lds\n"
  "v_mfma_f32_16x16x32_bf16 v[120:123], a[76:79], a[120:123], v[120:123]\n"
  "ds_read_b64_tr_b16 a[8:9], v12 offset:4096\n"
  "v_mfma_f32_16x16x32_bf16 v[124:127], a[76:79], a[124:127], v[124:127]\n"
  "ds_read_b64_tr_b16 a[10:11], v12 offset:4608\n"
  "v_mfma_f32_16x16x32_bf16 v[128:131], a[80:83], a[112:115], v[128:131]\n"
  "ds_read_b64_tr_b16 a[12:13], v14 offset:4096\n"
  "v_mfma_f32_16x16x32_bf16 v[132:135], a[80:83], a[116:119], v[132:135]\n"
  "ds_read_b64_tr_b16 a[14:15], v14 offset:4608\n"
  "v_mfma_f32_16x16x32_bf16 v[136:139], a[80:83], a[120:123], v[136:139]\n"
  "s_add_u32 m0, s64, 77824\n"
  "v_mfma_f32_16x16x32_bf16 v[140:143], a[80:83], a[124:127], v[140:143]\n"
  "buffer_load_dwordx4 v2, s[32:35], s63 offen lds\n"
  "v_mfma_f32_16x16x32_bf16 v[144:147], a[84:87], a[112:115], v[144:147]\n"
  "ds_read_b64_tr_b16 a[16:17], v12 offset:8192\n"
  "v_mfma_f32_16x16x32_bf16 v[148:151], a[84:87], a[116:119], v[148:151]\n"
  "ds_read_b64_tr_b16 a[18:19], v12 offset:8704\n"
  "v_mfma_f32_16x16x32_bf16 v[152:155], a[84:87], a[120:123], v[152:155]\n"
  "ds_read_b64_tr_b16 a[20:21], v14 offset:8192\n"
  "v_mfma_f32_16x16x32_bf16 v[156:159], a[84:87], a[124:127], v[156:159]\n"
  "ds_read_b64_tr_b16 a[22:23], v14 offset:8704\n"
  "v_mfma_f32_16x16x32_bf16 v[160:163], a[88:91], a[112:115], v[160:163]\n"
  "s_add_u32 m0, s64, 81792\n"
  "v_mfma_f32_16x16x32_bf16 v[164:167], a[88:91], a[116:119], v[164:167]\n"
  "buffer_load_dwordx4 v2, s[28:31], s63 offen offset:128 lds\n"
  "v_mfma_f32_16x16x32_bf16 v[168:171], a[88:91], a[120:123], v[168:171]\n"
  "ds_read_b64_tr_b16 a[24:25], v12 offset:12288\n"
  "v_mfma_f32_16x16x32_bf16 v[172:175], a[88:91], a[124:127], v[172:175]\n"
  "ds_read_b64_tr_b16 a[26:27], v12 offset:12800\n"
  "v_mfma_f32_16x16x32_bf16 v[176:179], a[92:95], a[112:115], v[176:179]\n"
  "ds_read_b64_tr_b16 a[28:29], v14 offset:12288\n"
  "v_mfma_f32_16x16x32_bf16 v[180:183], a[92:95], a[116:119], v[180:183]\n"
  "ds_read_b64_tr_b16 a[30:31], v14 offset:12800\n"
  "v_mfma_f32_16x16x32_bf16 v[184:187], a[92:95], a[120:123], v[184:187]\n"
  "s_add_u32 m0, s64, 85888\n"
  "v_mfma_f32_16x16x32_bf16 v[188:191], a[92:95], a[124:127], v[188:191]\n"
  "buffer_load_dwordx4 v2, s[32:35], s63 offen offset:128 lds\n"
  "v_mfma_f32_16x16x32_bf16 v[192:195], a[96:99], a[112:115], v[192:195]\n"
  "ds_read_b64_tr_b16 a[32:33], v12 offset:16384\n"
  "v_mfma_f32_16x16x32_bf16 v[196:199], a[96:99], a[116:119], v[196:199]\n"
  "ds_read_b64_tr_b16 a[34:35], v12 offset:16896\n"
  "v_mfma_f32_16x16x32_bf16 v[200:203], a[96:99], a[120:123], v[200:203]\n"
  "ds_read_b64_tr_b16 a[36:37], v14 offset:16384\n"
  "v_mfma_f32_16x16x32_bf16 v[204:207], a[96:99], a[124:127], v[204:207]\n"
  "ds_read_b64_tr_b16 a[38:39], v14 offset:16896\n"
  "v_mfma_f32_16x16x32_bf16 v[208:211], a[100:103], a[112:115], v[208:211]\n"
  "s_add_u32 m0, s64, 89856\n"
  "v_mfma_f32_16x16x32_bf16 v[212:215], a[100:103], a[116:119], v[212:215]\n"
  "buffer_load_dwordx4 v2, s[28:31], s63 offen offset:256 lds\n"
  "v_mfma_f32_16x16x32_bf16 v[216:219], a[100:103], a[120:123], v[216:219]\n"
  "ds_read_b64_tr_b16 a[40:41], v12 offset:20480\n"
  "v_mfma_f32_16x16x32_bf16 v[220:223], a[100:103], a[124:127], v[220:223]\n"
  "ds_read_b64_tr_b16 a[42:43], v12 offset:20992\n"
  "v_mfma_f32_16x16x32_bf16 v[224:227], a[104:107], a[112:115], v[224:227]\n"
  "ds_read_b64_tr_b16 a[44:45], v14 offset:20480\n"
  "v_mfma_f32_16x16x32_bf16 v[228:231], a[104:107], a[116:119], v[228:231]\n"
  "ds_read_b64_tr_b16 a[46:47], v14 offset:20992\n"
  "v_mfma_f32_16x16x32_bf16 v[232:235], a[104:107], a[120:123], v[232:235]\n"
  "s_add_u32 m0, s64, 93952\n"
  "v_mfma_f32_16x16x32_bf16 v[236:239], a[104:107], a[124:127], v[236:239]\n"
  "buffer_load_dwordx4 v2, s[32:35], s63 offen offset:256 lds\n"
  "v_mfma_f32_16x16x32_bf16 v[240:243], a[108:111], a[112:115], v[240:243]\n"
  "v_mfma_f32_16x16x32_bf16 v[244:247], a[108:111], a[116:119], v[244:247]\n"
  "v_mfma_f32_16x16x32_bf16 v[248:251], a[108:111], a[120:123], v[248:251]\n"
  "v_mfma_f32_16x16x32_bf16 v[252:255], a[108:111], a[124:127], v[252:255]\n"
  "s_cmp_eq_u32 s51, 6\n"
  "s_cbranch_scc0 .Lcs15\n"
  "v_mfma_f32_16x16x32_bf16 a[148:151], a[64:67], v[52:55], a[148:151]\n"
  "v_mfma_f32_16x16x32_bf16 a[152:155], a[68:71], v[52:55], a[152:155]\n"
  "v_mfma_f32_16x16x32_bf16 a[156:159], a[72:75], v[52:55], a[156:159]\n"
  "v_mfma_f32_16x16x32_bf16 a[160:163], a[76:79], v[52:55], a[160:163]\n"
  "v_mfma_f32_16x16x32_bf16 a[164:167], a[80:83], v[52:55], a[164:167]\n"
  "v_mfma_f32_16x16x32_bf16 a[168:171], a[84:87], v[52:55], a[168:171]\n"
  ".Lcs15:\n"
  "s_cmp_eq_u32 s51, 7\n"
  "s_cbranch_scc0 .Lcs16\n"
  "v_mfma_f32_16x16x32_bf16 a[148:151], a[88:91], v[52:55], a[148:151]\n"
  "v_mfma_f32_16x16x32_bf16 a[152:155], a[92:95], v[52:55], a[152:155]\n"
  "v_mfma_f32_16x16x32_bf16 a[156:159], a[96:99], v[52:55], a[156:159]\n"
  "v_mfma_f32_16x16x32_bf16 a[160:163], a[100:103], v[52:55], a[160:163]\n"
  "v_mfma_f32_16x16x32_bf16 a[164:167], a[104:107], v[52:55], a[164:167]\n"
  "v_mfma_f32_16x16x32_bf16 a[168:171], a[108:111], v[52:55], a[168:171]\n"
  ".Lcs16:\n"
  "s_waitcnt vmcnt(17) lgkmcnt(0)\n"
  "s_barrier\n"
  "v_mfma_f32_16x16x32_bf16 v[64:67], a[0:3], a[48:51], v[64:67]\n"
  "s_mul_i32 s62, s58, 1536\n"
  "s_add_u32 m0, s65, 2048\n"
  "v_mfma_f32_16x16x32_bf16 v[68:71], a[0:3], a[52:55], v[68:71]\n"
  "buffer_load_dwordx4 v8, s[24:27], s58 offen lds\n"
  "v_mfma_f32_16x16x32_bf16 v[72:75], a[0:3], a[56:59], v[72:75]\n"
  "ds_read_b128 a[112:115], v17 offset:16384\n"
  "v_mfma_f32_16x16x32_bf16 v[76:79], a[0:3], a[60:63], v[76:79]\n"
  "ds_read_b128 a[116:119], v17 offset:18432\n"
  "v_mfma_f32_16x16x32_bf16 v[80:83], a[4:7], a[48:51], v[80:83]\n"
  "ds_read_b128 a[120:123], v17 offset:20480\n"
  "v_mfma_f32_16x16x32_bf16 v[84:87], a[4:7], a[52:55], v[84:87]\n"
  "ds_read_b128 a[124:127], v17 offset:22528\n"
  "v_mfma_f32_16x16x32_bf16 v[88:91], a[4:7], a[56:59], v[88:91]\n"
  "s_add_u32 m0, s65, 3072\n"
  "v_mfma_f32_16x16x32_bf16 v[92:95], a[4:7], a[60:63], v[92:95]\n"
  "buffer_load_dwordx4 v9, s[24:27], s58 offen lds\n"
  "v_mfma_f32_16x16x32_bf16 v[96:99], a[8:11], a[48:51], v[96:99]\n"
  "ds_read_b64_tr_b16 a[64:65], v12 offset:24576\n"
  "v_mfma_f32_16x16x32_bf16 v[100:103], a[8:11], a[52:55], v[100:103]\n"
  "ds_read_b64_tr_b16 a[66:67], v12 offset:25088\n"
  "v_mfma_f32_16x16x32_bf16 v[104:107], a[8:11], a[56:59], v[104:107]\n"
  "ds_read_b64_tr_b16 a[68:69], v14 offset:24576\n"
  "v_mfma_f32_16x16x32_bf16 v[108:111], a[8:11], a[60:63], v[108:111]\n"
  "ds_read_b64_tr_b16 a[70:71], v14 offset:25088\n"
  "v_mfma_f32_16x16x32_bf16 v[112:115], a[12:15], a[48:51], v[112:115]\n"
  "s_add_u32 m0, s64, 0\n"
  "s_nop 0\n"
  "v_mfma_f32_16x16x32_bf16 v[116:119], a[12:15], a[52:55], v[116:119]\n"
  "buffer_load_dwordx4 v2, s[28:31], s62 offen lds\n"
  "v_mfma_f32_16x16x32_bf16 v[120:123], a[12:15], a[56:59], v[120:123]\n"
  "ds_read_b64_tr_b16 a[72:73], v12 offset:28672\n"
  "v_mfma_f32_16x16x32_bf16 v[124:127], a[12:15], a[60:63], v[124:127]\n"
  "ds_read_b64_tr_b16 a[74:75], v12 offset:29184\n"
  "v_mfma_f32_16x16x32_bf16 v[128:131], a[16:19], a[48:51], v[128:131]\n"
  "ds_read_b64_tr_b16 a[76:77], v14 offset:28672\n"
  "v_mfma_f32_16x16x32_bf16 v[132:135], a[16:19], a[52:55], v[132:135]\n"
  "ds_read_b64_tr_b16 a[78:79], v14 offset:29184\n"
  "v_mfma_f32_16x16x32_bf16 v[136:139], a[16:19], a[56:59], v[136:139]\n"
  "s_add_u32 m0, s64, 4096\n"
  "v_mfma_f32_16x16x32_bf16 v[140:143], a[16:19], a[60:63], v[140:143]\n"
  "buffer_load_dwordx4 v2, s[32:35], s62 offen lds\n"
  "v_mfma_f32_16x16x32_bf16 v[144:147], a[20:23], a[48:51], v[144:147]\n"
  "ds_read_b64_tr_b16 a[80:81], v12 offset:32768\n"
  "v_mfma_f32_16x16x32_bf16 v[148:151], a[20:23], a[52:55], v[148:151]\n"
  "ds_read_b64_tr_b16 a[82:83], v12 offset:33280\n"
  "v_mfma_f32_16x16x32_bf16 v[152:155], a[20:23], a[56:59], v[152:155]\n"
  "ds_read_b64_tr_b16 a[84:85], v14 offset:32768\n"
  "v_mfma_f32_16x16x32_bf16 v[156:159], a[20:23], a[60:63], v[156:159]\n"
  "ds_read_b64_tr_b16 a[86:87], v14 offset:33280\n"
  "v_mfma_f32_16x16x32_bf16 v[160:163], a[24:27], a[48:51], v[160:163]\n"
  "s_add_u32 m0, s64, 8064\n"
  "v_mfma_f32_16x16x32_bf16 v[164:167], a[24:27], a[52:55], v[164:167]\n"
  "buffer_load_dwordx4 v2, s[28:31], s62 offen offset:128 lds\n"
  "v_mfma_f32_16x16x32_bf16 v[168:171], a[24:27], a[56:59], v[168:171]\n"
  "ds_read_b64_tr_b16 a[88:89], v12 offset:36864\n"
  "v_mfma_f32_16x16x32_bf16 v[172:175], a[24:27], a[60:63], v[172:175]\n"
  "ds_read_b64_tr_b16 a[90:91], v12 offset:37376\n"
  "v_mfma_f32_16x16x32_bf16 v[176:179], a[28:31], a[48:51], v[176:179]\n"
  "ds_read_b64_tr_b16 a[92:93], v14 offset:36864\n"
  "v_mfma_f32_16x16x32_bf16 v[180:183], a[28:31], a[52:55], v[180:183]\n"
  "ds_read_b64_tr_b16 a[94:95], v14 offset:37376\n"
  "v_mfma_f32_16x16x32_bf16 v[184:187], a[28:31], a[56:59], v[184:187]\n"
  "s_add_u32 m0, s64, 12160\n"
  "v_mfma_f32_16x16x32_bf16 v[188:191], a[28:31], a[60:63], v[188:191]\n"
  "buffer_load_dwordx4 v2, s[32:35], s62 offen offset:128 lds\n"
  "v_mfma_f32_16x16x32_bf16 v[192:195], a[32:35], a[48:51], v[192:195]\n"
  "ds_read_b64_tr_b16 a[96:97], v12 offset:40960\n"
  "v_mfma_f32_16x16x32_bf16 v[196:199], a[32:35], a[52:55], v[196:199]\n"
  "ds_read_b64_tr_b16 a[98:99], v12 offset:41472\n"
  "v_mfma_f32_16x16x32_bf16 v[200:203], a[32:35], a[56:59], v[200:203]\n"
  "ds_read_b64_tr_b16 a[100:101], v14 offset:40960\n"
  "v_mfma_f32_16x16x32_bf16 v[204:207], a[32:35], a[60:63], v[204:207]\n"
  "ds_read_b64_tr_b16 a[102:103], v14 offset:41472\n"
  "v_mfma_f32_16x16x32_bf16 v[208:211], a[36:39], a[48:51], v[208:211]\n"
  "s_add_u32 m0, s64, 16128\n"
  "v_mfma_f32_16x16x32_bf16 v[212:215], a[36:39], a[52:55], v[212:215]\n"
  "buffer_load_dwordx4 v2, s[28:31], s62 offen offset:256 lds\n"
  "v_mfma_f32_16x16x32_bf16 v[216:219], a[36:39], a[56:59], v[216:219]\n"
  "ds_read_b64_tr_b16 a[104:105], v12 offset:45056\n"
  "v_mfma_f32_16x16x32_bf16 v[220:223], a[36:39], a[60:63], v[220:223]\n"
  "ds_read_b64_tr_b16 a[106:107], v12 offset:45568\n"
  "v_mfma_f32_16x16x32_bf16 v[224:227], a[40:43], a[48:51], v[224:227]\n"
  "ds_read_b64_tr_b16 a[108:109], v14 offset:45056\n"
  "v_mfma_f32_16x16x32_bf16 v[228:231], a[40:43], a[52:55], v[228:231]\n"
  "ds_read_b64_tr_b16 a[110:111], v14 offset:45568\n"
  "v_mfma_f32_16x16x32_bf16 v[232:235], a[40:43], a[56:59], v[232:235]\n"
  "s_add_u32 m0, s64, 20224\n"
  "v_mfma_f32_16x16x32_bf16 v[236:239], a[40:43], a[60:63], v[236:239]\n"
  "buffer_load_dwordx4 v2, s[32:35], s62 offen offset:256 lds\n"
  "v_mfma_f32_16x16x32_bf16 v[240:243], a[44:47], a[48:51], v[240:243]\n"
  "v_mfma_f32_16x16x32_bf16 v[244:247], a[44:47], a[52:55], v[244:247]\n"
  "v_mfma_f32_16x16x32_bf16 v[248:251], a[44:47], a[56:59], v[248:251]\n"
  "v_mfma_f32_16x16x32_bf16 v[252:255], a[44:47], a[60:63], v[252:255]\n"
  "s_cmp_eq_u32 s51, 8\n"
  "s_cbranch_scc0 .Lcs17\n"
  "v_mfma_f32_16x16x32_bf16 a[148:151], a[0:3], v[52:55], 0\n"
  "v_mfma_f32_16x16x32_bf16 a[152:155], a[4:7], v[52:55], 0\n"
  "v_mfma_f32_16x16x32_bf16 a[156:159], a[8:11], v[52:55], 0\n"
  "v_mfma_f32_16x16x32_bf16 a[160:163], a[12:15], v[52:55], 0\n"
  "v_mfma_f32_16x16x32_bf16 a[164:167], a[16:19], v[52:55], 0\n"
  "v_mfma_f32_16x16x32_bf16 a[168:171], a[20:23], v[52:55], 0\n"
  ".Lcs17:\n"
  "s_cmp_eq_u32 s51, 9\n"
  "s_cbranch_scc0 .Lcs18\n"
  "s_nop 0\n"
  "s_nop 0\n"
  "v_mfma_f32_16x16x32_bf16 a[148:151], a[24:27], v[52:55], 0\n"
  "v_mfma_f32_16x16x32_bf16 a[152:155], a[28:31], v[52:55], 0\n"
  "v_mfma_f32_16x16x32_bf16 a[156:159], a[32:35], v[52:55], 0\n"
  "v_mfma_f32_16x16x32_bf16 a[160:163], a[36:39], v[52:55], 0\n"
  "v_mfma_f32_16x16x32_bf16 a[164:167], a[40:43], v[52:55], 0\n"
  "v_mfma_f32_16x16x32_bf16 a[168:171], a[44:47], v[52:55], 0\n"
  ".Lcs18:\n"
  "s_waitcnt vmcnt(17) lgkmcnt(0)\n"
  "s_barrier\n"
  "v_mfma_f32_16x16x32_bf16 v[64:67], a[64:67], a[112:115], v[64:67]\n"
  "s_add_u32 s63, s62, 98304\n"
  "s_add_u32 m0, s65, 16384\n"
  "v_mfma_f32_16x16x32_bf16 v[68:71], a[64:67], a[116:119], v[68:71]\n"
  "buffer_load_dwordx4 v6, s[24:27], s59 offen lds\n"
  "v_mfma_f32_16x16x32_bf16 v[72:75], a[64:67], a[120:123], v[72:75]\n"
  "ds_read_b128 a[48:51], v16 offset:32768\n"
  "v_mfma_f32_16x16x32_bf16 v[76:79], a[64:67], a[124:127], v[76:79]\n"
  "ds_read_b128 a[52:55], v16 offset:34816\n"
  "v_mfma_f32_16x16x32_bf16 v[80:83], a[68:71], a[112:115], v[80:83]\n"
  "ds_read_b128 a[56:59], v16 offset:36864\n"
  "v_mfma_f32_16x16x32_bf16 v[84:87], a[68:71], a[116:119], v[84:87]\n"
  "ds_read_b128 a[60:63], v16 offset:38912\n"
  "v_mfma_f32_16x16x32_bf16 v[88:91], a[68:71], a[120:123], v[88:91]\n"
  "s_add_u32 m0, s65, 17408\n"
  "v_mfma_f32_16x16x32_bf16 v[92:95], a[68:71], a[124:127], v[92:95]\n"
  "buffer_load_dwordx4 v7, s[24:27], s59 offen lds\n"
  "v_mfma_f32_16x16x32_bf16 v[96:99], a[72:75], a[112:115], v[96:99]\n"
  "ds_read_b64_tr_b16 a[0:1], v13 offset:0\n"
  "v_mfma_f32_16x16x32_bf16 v[100:103], a[72:75], a[116:119], v[100:103]\n"
  "ds_read_b64_tr_b16 a[2:3], v13 offset:512\n"
  "v_mfma_f32_16x16x32_bf16 v[104:107], a[72:75], a[120:123], v[104:107]\n"
  "ds_read_b64_tr_b16 a[4:5], v15 offset:0\n"
  "v_mfma_f32_16x16x32_bf16 v[108:111], a[72:75], a[124:127], v[108:111]\n"
  "ds_read_b64_tr_b16 a[6:7], v15 offset:512\n"
  "v_mfma_f32_16x16x32_bf16 v[112:115], a[76:79], a[112:115], v[112:115]\n"
  "s_add_u32 m0, s64, 24576\n"
  "v_mfma_f32_16x16x32_bf16 v[116:119], a[76:79], a[116:119], v[116:119]\n"
  "buffer_load_dwordx4 v2, s[28:31], s63 offen lds\n"
  "v_mfma_f32_16x16x32_bf16 v[120:123], a[76:79], a[120:123], v[120:123]\n"
  "ds_read_b64_tr_b16 a[8:9], v13 offset:4096\n"
  "v_mfma_f32_16x16x32_bf16 v[124:127], a[76:79], a[124:127], v[124:127]\n"
  "ds_read_b64_tr_b16 a[10:11], v13 offset:4608\n"
  "v_mfma_f32_16x16x32_bf16 v[128:131], a[80:83], a[112:115], v[128:131]\n"
  "ds_read_b64_tr_b16 a[12:13], v15 offset:4096\n"
  "v_mfma_f32_16x16x32_bf16 v[132:135], a[80:83], a[116:119], v[132:135]\n"
  "ds_read_b64_tr_b16 a[14:15], v15 offset:4608\n"
  "v_mfma_f32_16x16x32_bf16 v[136:139], a[80:83], a[120:123], v[136:139]\n"
  "s_add_u32 m0, s64, 28672\n"
  "v_mfma_f32_16x16x32_bf16 v[140:143], a[80:83], a[124:127], v[140:143]\n"
  "buffer_load_dwordx4 v2, s[32:35], s63 offen lds\n"
  "v_mfma_f32_16x16x32_bf16 v[144:147], a[84:87], a[112:115], v[144:147]\n"
  "ds_read_b64_tr_b16 a[16:17], v13 offset:8192\n"
  "v_mfma_f32_16x16x32_bf16 v[148:151], a[84:87], a[116:119], v[148:151]\n"
  "ds_read_b64_tr_b16 a[18:19], v13 offset:8704\n"
  "v_mfma_f32_16x16x32_bf16 v[152:155], a[84:87], a[120:123], v[152:155]\n"
  "ds_read_b64_tr_b16 a[20:21], v15 offset:8192\n"
  "v_mfma_f32_16x16x32_bf16 v[156:159], a[84:87], a[124:127], v[156:159]\n"
  "ds_read_b64_tr_b16 a[22:23], v15 offset:8704\n"
  "v_mfma_f32_16x16x32_bf16 v[160:163], a[88:91], a[112:115], v[160:163]\n"
  "s_add_u32 m0, s64, 32640\n"
  "v_mfma_f32_16x16x32_bf16 v[164:167], a[88:91], a[116:119], v[164:167]\n"
  "buffer_load_dwordx4 v2, s[28:31], s63 offen offset:128 lds\n"
  "v_mfma_f32_16x16x32_bf16 v[168:171], a[88:91], a[120:123], v[168:171]\n"
  "ds_read_b64_tr_b16 a[24:25], v13 offset:12288\n"
  "v_mfma_f32_16x16x32_bf16 v[172:175], a[88:91], a[124:127], v[172:175]\n"
  "ds_read_b64_tr_b16 a[26:27], v13 offset:12800\n"
  "v_mfma_f32_16x16x32_bf16 v[176:179], a[92:95], a[112:115], v[176:179]\n"
  "ds_read_b64_tr_b16 a[28:29], v15 offset:12288\n"
  "v_mfma_f32_16x16x32_bf16 v[180:183], a[92:95], a[116:119], v[180:183]\n"
  "ds_read_b64_tr_b16 a[30:31], v15 offset:12800\n"
  "v_mfma_f32_16x16x32_bf16 v[184:187], a[92:95], a[120:123], v[184:187]\n"
  "s_add_u32 m0, s64, 36736\n"
  "v_mfma_f32_16x16x32_bf16 v[188:191], a[92:95], a[124:127], v[188:191]\n"
  "buffer_load_dwordx4 v2, s[32:35], s63 offen offset:128 lds\n"
  "v_mfma_f32_16x16x32_bf16 v[192:195], a[96:99], a[112:115], v[192:195]\n"
  "ds_read_b64_tr_b16 a[32:33], v13 offset:16384\n"
  "v_mfma_f32_16x16x32_bf16 v[196:199], a[96:99], a[116:119], v[196:199]\n"
  "ds_read_b64_tr_b16 a[34:35], v13 offset:16896\n"
  "v_mfma_f32_16x16x32_bf16 v[200:203], a[96:99], a[120:123], v[200:203]\n"
  "ds_read_b64_tr_b16 a[36:37], v15 offset:16384\n"
  "v_mfma_f32_16x16x32_bf16 v[204:207], a[96:99], a[124:127], v[204:207]\n"
  "ds_read_b64_tr_b16 a[38:39], v15 offset:16896\n"
  "v_mfma_f32_16x16x32_bf16 v[208:211], a[100:103], a[112:115], v[208:211]\n"
  "s_add_u32 m0, s64, 40704\n"
  "v_mfma_f32_16x16x32_bf16 v[212:215], a[100:103], a[116:119], v[212:215]\n"
  "buffer_load_dwordx4 v2, s[28:31], s63 offen offset:256 lds\n"
  "v_mfma_f32_16x16x32_bf16 v[216:219], a[100:103], a[120:123], v[216:219]\n"
  "ds_read_b64_tr_b16 a[40:41], v13 offset:20480\n"
  "v_mfma_f32_16x16x32_bf16 v[220:223], a[100:103], a[124:127], v[220:223]\n"
  "ds_read_b64_tr_b16 a[42:43], v13 offset:20992\n"
  "v_mfma_f32_16x16x32_bf16 v[224:227], a[104:107], a[112:115], v[224:227]\n"
  "ds_read_b64_tr_b16 a[44:45], v15 offset:20480\n"
  "v_mfma_f32_16x16x32_bf16 v[228:231], a[104:107], a[116:119], v[228:231]\n"
  "ds_read_b64_tr_b16 a[46:47], v15 offset:20992\n"
  "v_mfma_f32_16x16x32_bf16 v[232:235], a[104:107], a[120:123], v[232:235]\n"
  "s_add_u32 m0, s64, 44800\n"
  "v_mfma_f32_16x16x32_bf16 v[236:239], a[104:107], a[124:127], v[236:239]\n"
  "buffer_load_dwordx4 v2, s[32:35], s63 offen offset:256 lds\n"
  "v_mfma_f32_16x16x32_bf16 v[240:243], a[108:111], a[112:115], v[240:243]\n"
  "v_mfma_f32_16x16x32_bf16 v[244:247], a[108:111], a[116:119], v[244:247]\n"
  "v_mfma_f32_16x16x32_bf16 v[248:251], a[108:111], a[120:123], v[248:251]\n"
  "v_mfma_f32_16x16x32_bf16 v[252:255], a[108:111], a[124:127], v[252:255]\n"
  "s_cmp_eq_u32 s51, 8\n"
  "s_cbranch_scc0 .Lcs19\n"
  "v_mfma_f32_16x16x32_bf16 a[148:151], a[64:67], v[52:55], a[148:151]\n"
  "v_mfma_f32_16x16x32_bf16 a[152:155], a[68:71], v[52:55], a[152:155]\n"
  "v_mfma_f32_16x16x32_bf16 a[156:159], a[72:75], v[52:55], a[156:159]\n"
  "v_mfma_f32_16x16x32_bf16 a[160:163], a[76:79], v[52:55], a[160:163]\n"
  "v_mfma_f32_16x16x32_bf16 a[164:167], a[80:83], v[52:55], a[164:167]\n"
  "v_mfma_f32_16x16x32_bf16 a[168:171], a[84:87], v[52:55], a[168:171]\n"
  ".Lcs19:\n"
  "s_cmp_eq_u32 s51, 9\n"
  "s_cbranch_scc0 .Lcs20\n"
  "v_mfma_f32_16x16x32_bf16 a[148:151], a[88:91], v[52:55], a[148:151]\n"
  "v_mfma_f32_16x16x32_bf16 a[152:155], a[92:95], v[52:55], a[152:155]\n"
  "v_mfma_f32_16x16x32_bf16 a[156:159], a[96:99], v[52:55], a[156:159]\n"
  "v_mfma_f32_16x16x32_bf16 a[160:163], a[100:103], v[52:55], a[160:163]\n"
  "v_mfma_f32_16x16x32_bf16 a[164:167], a[104:107], v[52:55], a[164:167]\n"
  "v_mfma_f32_16x16x32_bf16 a[168:171], a[108:111], v[52:55], a[168:171]\n"
  ".Lcs20:\n"
  "s_waitcnt vmcnt(16) lgkmcnt(0)\n"
  "s_barrier\n"
  "v_mfma_f32_16x16x32_bf16 v[64:67], a[0:3], a[48:51], v[64:67]\n"
  "s_mul_i32 s62, s59, 1536\n"
  "s_add_u32 m0, s65, 18432\n"
  "buffer_store_dwordx4 a[148:151], v50, s[36:39], s77 offen sc1\n"
  "v_mfma_f32_16x16x32_bf16 v[68:71], a[0:3], a[52:55], v[68:71]\n"
  "buffer_load_dwordx4 v8, s[24:27], s59 offen lds\n"
  "buffer_store_dwordx4 a[152:155], v50, s[36:39], s77 offen offset:64 sc1\n"
  "v_mfma_f32_16x16x32_bf16 v[72:75], a[0:3], a[56:59], v[72:75]\n"
  "ds_read_b128 a[112:115], v17 offset:32768\n"
  "buffer_store_dwordx4 a[156:159], v50, s[36:39], s77 offen offset:128 sc1\n"
  "v_mfma_f32_16x16x32_bf16 v[76:79], a[0:3], a[60:63], v[76:79]\n"
  "ds_read_b128 a[116:119], v17 offset:34816\n"
  "buffer_store_dwordx4 a[160:163], v50, s[36:39], s77 offen offset:192 sc1\n"
  "v_mfma_f32_16x16x32_bf16 v[80:83], a[4:7], a[48:51], v[80:83]\n"
  "ds_read_b128 a[120:123], v17 offset:36864\n"
  "buffer_store_dwordx4 a[164:167], v50, s[36:39], s77 offen offset:256 sc1\n"
  "v_mfma_f32_16x16x32_bf16 v[84:87], a[4:7], a[52:55], v[84:87]\n"
  "ds_read_b128 a[124:127], v17 offset:38912\n"
  "buffer_store_dwordx4 a[168:171], v50, s[36:39], s77 offen offset:320 sc1\n"
  "v_mfma_f32_16x16x32_bf16 v[88:91], a[4:7], a[56:59], v[88:91]\n"
  "s_add_u32 m0, s65, 19456\n"
  "v_mfma_f32_16x16x32_bf16 v[92:95], a[4:7], a[60:63], v[92:95]\n"
  "buffer_load_dwordx4 v9, s[24:27], s59 offen lds\n"
  "v_mfma_f32_16x16x32_bf16 v[96:99], a[8:11], a[48:51], v[96:99]\n"
  "ds_read_b64_tr_b16 a[64:65], v13 offset:24576\n"
  "v_mfma_f32_16x16x32_bf16 v[100:103], a[8:11], a[52:55], v[100:103]\n"
  "ds_read_b64_tr_b16 a[66:67], v13 offset:25088\n"
  "v_mfma_f32_16x16x32_bf16 v[104:107], a[8:11], a[56:59], v[104:107]\n"
  "ds_read_b64_tr_b16 a[68:69], v15 offset:24576\n"
  "v_mfma_f32_16x16x32_bf16 v[108:111], a[8:11], a[60:63], v[108:111]\n"
  "ds_read_b64_tr_b16 a[70:71], v15 offset:25088\n"
  "v_mfma_f32_16x16x32_bf16 v[112:115], a[12:15], a[48:51], v[112:115]\n"
  "s_add_u32 m0, s64, 49152\n"
  "v_mfma_f32_16x16x32_bf16 v[116:119], a[12:15], a[52:55], v[116:119]\n"
  "buffer_load_dwordx4 v2, s[28:31], s62 offen lds\n"
  "v_mfma_f32_16x16x32_bf16 v[120:123], a[12:15], a[56:59], v[120:123]\n"
  "ds_read_b64_tr_b16 a[72:73], v13 offset:28672\n"
  "v_mfma_f32_16x16x32_bf16 v[124:127], a[12:15], a[60:63], v[124:127]\n"
  "ds_read_b64_tr_b16 a[74:75], v13 offset:29184\n"
  "v_mfma_f32_16x16x32_bf16 v[128:131], a[16:19], a[48:51], v[128:131]\n"
  "ds_read_b64_tr_b16 a[76:77], v15 offset:28672\n"
  "v_mfma_f32_16x16x32_bf16 v[132:135], a[16:19], a[52:55], v[132:135]\n"
  "ds_read_b64_tr_b16 a[78:79], v15 offset:29184\n"
  "v_mfma_f32_16x16x32_bf16 v[136:139], a[16:19], a[56:59], v[136:139]\n"
  "s_add_u32 m0, s64, 53248\n"
  "v_mfma_f32_16x16x32_bf16 v[140:143], a[16:19], a[60:63], v[140:143]\n"
  "buffer_load_dwordx4 v2, s[32:35], s62 offen lds\n"
  "v_mfma_f32_16x16x32_bf16 v[144:147], a[20:23], a[48:51], v[144:147]\n"
  "ds_read_b64_tr_b16 a[80:81], v13 offset:32768\n"
  "v_mfma_f32_16x16x32_bf16 v[148:151], a[20:23], a[52:55], v[148:151]\n"
  "ds_read_b64_tr_b16 a[82:83], v13 offset:33280\n"
  "v_mfma_f32_16x16x32_bf16 v[152:155], a[20:23], a[56:59], v[152:155]\n"
  "ds_read_b64_tr_b16 a[84:85], v15 offset:32768\n"
  "v_mfma_f32_16x16x32_bf16 v[156:159], a[20:23], a[60:63], v[156:159]\n"
  "ds_read_b64_tr_b16 a[86:87], v15 offset:33280\n"
  "v_mfma_f32_16x16x32_bf16 v[160:163], a[24:27], a[48:51], v[160:163]\n"
  "s_add_u32 m0, s64, 57216\n"
  "v_mfma_f32_16x16x32_bf16 v[164:167], a[24:27], a[52:55], v[164:167]\n"
  "buffer_load_dwordx4 v2, s[28:31], s62 offen offset:128 lds\n"
  "v_mfma_f32_16x16x32_bf16 v[168:171], a[24:27], a[56:59], v[168:171]\n"
  "ds_read_b64_tr_b16 a[88:89], v13 offset:36864\n"
  "v_mfma_f32_16x16x32_bf16 v[172:175], a[24:27], a[60:63], v[172:175]\n"
  "ds_read_b64_tr_b16 a[90:91], v13 offset:37376\n"
  "v_mfma_f32_16x16x32_bf16 v[176:179], a[28:31], a[48:51], v[176:179]\n"
  "ds_read_b64_tr_b16 a[92:93], v15 offset:36864\n"
  "v_mfma_f32_16x16x32_bf16 v[180:183], a[28:31], a[52:55], v[180:183]\n"
  "ds_read_b64_tr_b16 a[94:95], v15 offset:37376\n"
  "v_mfma_f32_16x16x32_bf16 v[184:187], a[28:31], a[56:59], v[184:187]\n"
  "s_add_u32 m0, s64, 61312\n"
  "v_mfma_f32_16x16x32_bf16 v[188:191], a[28:31], a[60:63], v[188:191]\n"
  "buffer_load_dwordx4 v2, s[32:35], s62 offen offset:128 lds\n"
  "v_mfma_f32_16x16x32_bf16 v[192:195], a[32:35], a[48:51], v[192:195]\n"
  "ds_read_b64_tr_b16 a[96:97], v13 offset:40960\n"
  "v_mfma_f32_16x16x32_bf16 v[196:199], a[32:35], a[52:55], v[196:199]\n"
  "ds_read_b64_tr_b16 a[98:99], v13 offset:41472\n"
  "v_mfma_f32_16x16x32_bf16 v[200:203], a[32:35], a[56:59], v[200:203]\n"
  "ds_read_b64_tr_b16 a[100:101], v15 offset:40960\n"
  "v_mfma_f32_16x16x32_bf16 v[204:207], a[32:35], a[60:63], v[204:207]\n"
  "ds_read_b64_tr_b16 a[102:103], v15 offset:41472\n"
  "v_mfma_f32_16x16x32_bf16 v[208:211], a[36:39], a[48:51], v[208:211]\n"
  "s_add_u32 m0, s64, 65280\n"
  "v_mfma_f32_16x16x32_bf16 v[212:215], a[36:39], a[52:55], v[212:215]\n"
  "buffer_load_dwordx4 v2, s[28:31], s62 offen offset:256 lds\n"
  "v_mfma_f32_16x16x32_bf16 v[216:219], a[36:39], a[56:59], v[216:219]\n"
  "ds_read_b64_tr_b16 a[104:105], v13 offset:45056\n"
  "v_mfma_f32_16x16x32_bf16 v[220:223], a[36:39], a[60:63], v[220:223]\n"
  "ds_read_b64_tr_b16 a[106:107], v13 offset:45568\n"
  "v_mfma_f32_16x16x32_bf16 v[224:227], a[40:43], a[48:51], v[224:227]\n"
  "ds_read_b64_tr_b16 a[108:109], v15 offset:45056\n"
  "v_mfma_f32_16x16x32_bf16 v[228:231], a[40:43], a[52:55], v[228:231]\n"
  "ds_read_b64_tr_b16 a[110:111], v15 offset:45568\n"
  "v_mfma_f32_16x16x32_bf16 v[232:235], a[40:43], a[56:59], v[232:235]\n"
  "s_add_u32 m0, s64, 69376\n"
  "v_mfma_f32_16x16x32_bf16 v[236:239], a[40:43], a[60:63], v[236:239]\n"
  "buffer_load_dwordx4 v2, s[32:35], s62 offen offset:256 lds\n"
  "v_mfma_f32_16x16x32_bf16 v[240:243], a[44:47], a[48:51], v[240:243]\n"
  "v_mfma_f32_16x16x32_bf16 v[244:247], a[44:47], a[52:55], v[244:247]\n"
  "v_mfma_f32_16x16x32_bf16 v[248:251], a[44:47], a[56:59], v[248:251]\n"
  "v_mfma_f32_16x16x32_bf16 v[252:255], a[44:47], a[60:63], v[252:255]\n"
  "s_waitcnt vmcnt(22) lgkmcnt(0)\n"
  "s_barrier\n"
  "v_mfma_f32_16x16x32_bf16 v[64:67], a[64:67], a[112:115], v[64:67]\n"
  "s_add_u32 s63, s62, 98304\n"
  "s_add_u32 m0, s65, 32768\n"
  "v_mfma_f32_16x16x32_bf16 v[68:71], a[64:67], a[116:119], v[68:71]\n"
  "buffer_load_dwordx4 v6, s[24:27], s60 offen lds\n"
  "v_mfma_f32_16x16x32_bf16 v[72:75], a[64:67], a[120:123], v[72:75]\n"
  "ds_read_b128 a[48:51], v16 offset:0\n"
  "v_mfma_f32_16x16x32_bf16 v[76:79], a[64:67], a[124:127], v[76:79]\n"
  "ds_read_b128 a[52:55], v16 offset:2048\n"
  "v_mfma_f32_16x16x32_bf16 v[80:83], a[68:71], a[112:115], v[80:83]\n"
  "ds_read_b128 a[56:59], v16 offset:4096\n"
  "v_mfma_f32_16x16x32_bf16 v[84:87], a[68:71], a[116:119], v[84:87]\n"
  "ds_read_b128 a[60:63], v16 offset:6144\n"
  "v_mfma_f32_16x16x32_bf16 v[88:91], a[68:71], a[120:123], v[88:91]\n"
  "s_add_u32 m0, s65, 33792\n"
  "v_mfma_f32_16x16x32_bf16 v[92:95], a[68:71], a[124:127], v[92:95]\n"
  "buffer_load_dwordx4 v7, s[24:27], s60 offen lds\n"
  "v_mfma_f32_16x16x32_bf16 v[96:99], a[72:75], a[112:115], v[96:99]\n"
  "ds_read_b64_tr_b16 a[0:1], v12 offset:0\n"
  "v_mfma_f32_16x16x32_bf16 v[100:103], a[72:75], a[116:119], v[100:103]\n"
  "ds_read_b64_tr_b16 a[2:3], v12 offset:512\n"
  "v_mfma_f32_16x16x32_bf16 v[104:107], a[72:75], a[120:123], v[104:107]\n"
  "ds_read_b64_tr_b16 a[4:5], v14 offset:0\n"
  "v_mfma_f32_16x16x32_bf16 v[108:111], a[72:75], a[124:127], v[108:111]\n"
  "ds_read_b64_tr_b16 a[6:7], v14 offset:512\n"
  "v_mfma_f32_16x16x32_bf16 v[112:115], a[76:79], a[112:115], v[112:115]\n"
  "s_add_u32 m0, s64, 73728\n"
  "v_mfma_f32_16x16x32_bf16 v[116:119], a[76:79], a[116:119], v[116:119]\n"
  "buffer_load_dwordx4 v2, s[28:31], s63 offen lds\n"
  "v_mfma_f32_16x16x32_bf16 v[120:123], a[76:79], a[120:123], v[120:123]\n"
  "ds_read_b64_tr_b16 a[8:9], v12 offset:4096\n"
  "v_mfma_f32_16x16x32_bf16 v[124:127], a[76:79], a[124:127], v[124:127]\n"
  "ds_read_b64_tr_b16 a[10:11], v12 offset:4608\n"
  "v_mfma_f32_16x16x32_bf16 v[128:131], a[80:83], a[112:115], v[128:131]\n"
  "ds_read_b64_tr_b16 a[12:13], v14 offset:4096\n"
  "v_mfma_f32_16x16x32_bf16 v[132:135], a[80:83], a[116:119], v[132:135]\n"
  "ds_read_b64_tr_b16 a[14:15], v14 offset:4608\n"
  "v_mfma_f32_16x16x32_bf16 v[136:139], a[80:83], a[120:123], v[136:139]\n"
  "s_add_u32 m0, s64, 77824\n"
  "v_mfma_f32_16x16x32_bf16 v[140:143], a[80:83], a[124:127], v[140:143]\n"
  "buffer_load_dwordx4 v2, s[32:35], s63 offen lds\n"
  "v_mfma_f32_16x16x32_bf16 v[144:147], a[84:87], a[112:115], v[144:147]\n"
  "ds_read_b64_tr_b16 a[16:17], v12 offset:8192\n"
  "v_mfma_f32_16x16x32_bf16 v[148:151], a[84:87], a[116:119], v[148:151]\n"
  "ds_read_b64_tr_b16 a[18:19], v12 offset:8704\n"
  "v_mfma_f32_16x16x32_bf16 v[152:155], a[84:87], a[120:123], v[152:155]\n"
  "ds_read_b64_tr_b16 a[20:21], v14 offset:8192\n"
  "v_mfma_f32_16x16x32_bf16 v[156:159], a[84:87], a[124:127], v[156:159]\n"
  "ds_read_b64_tr_b16 a[22:23], v14 offset:8704\n"
  "v_mfma_f32_16x16x32_bf16 v[160:163], a[88:91], a[112:115], v[160:163]\n"
  "s_add_u32 m0, s64, 81792\n"
  "v_mfma_f32_16x16x32_bf16 v[164:167], a[88:91], a[116:119], v[164:167]\n"
  "buffer_load_dwordx4 v2, s[28:31], s63 offen offset:128 lds\n"
  "v_mfma_f32_16x16x32_bf16 v[168:171], a[88:91], a[120:123], v[168:171]\n"
  "ds_read_b64_tr_b16 a[24:25], v12 offset:12288\n"
  "v_mfma_f32_16x16x32_bf16 v[172:175], a[88:91], a[124:127], v[172:175]\n"
  "ds_read_b64_tr_b16 a[26:27], v12 offset:12800\n"
  "v_mfma_f32_16x16x32_bf16 v[176:179], a[92:95], a[112:115], v[176:179]\n"
  "ds_read_b64_tr_b16 a[28:29], v14 offset:12288\n"
  "v_mfma_f32_16x16x32_bf16 v[180:183], a[92:95], a[116:119], v[180:183]\n"
  "ds_read_b64_tr_b16 a[30:31], v14 offset:12800\n"
  "v_mfma_f32_16x16x32_bf16 v[184:187], a[92:95], a[120:123], v[184:187]\n"
  "s_add_u32 m0, s64, 85888\n"
  "v_mfma_f32_16x16x32_bf16 v[188:191], a[92:95], a[124:127], v[188:191]\n"
  "buffer_load_dwordx4 v2, s[32:35], s63 offen offset:128 lds\n"
  "v_mfma_f32_16x16x32_bf16 v[192:195], a[96:99], a[112:115], v[192:195]\n"
  "ds_read_b64_tr_b16 a[32:33], v12 offset:16384\n"
  "v_mfma_f32_16x16x32_bf16 v[196:199], a[96:99], a[116:119], v[196:199]\n"
  "ds_read_b64_tr_b16 a[34:35], v12 offset:16896\n"
  "v_mfma_f32_16x16x32_bf16 v[200:203], a[96:99], a[120:123], v[200:203]\n"
  "ds_read_b64_tr_b16 a[36:37], v14 offset:16384\n"
  "v_mfma_f32_16x16x32_bf16 v[204:207], a[96:99], a[124:127], v[204:207]\n"
  "ds_read_b64_tr_b16 a[38:39], v14 offset:16896\n"
  "v_mfma_f32_16x16x32_bf16 v[208:211], a[100:103], a[112:115], v[208:211]\n"
  "s_add_u32 m0, s64, 89856\n"
  "v_mfma_f32_16x16x32_bf16 v[212:215], a[100:103], a[116:119], v[212:215]\n"
  "buffer_load_dwordx4 v2, s[28:31], s63 offen offset:256 lds\n"
  "v_mfma_f32_16x16x32_bf16 v[216:219], a[100:103], a[120:123], v[216:219]\n"
  "ds_read_b64_tr_b16 a[40:41], v12 offset:20480\n"
  "v_mfma_f32_16x16x32_bf16 v[220:223], a[100:103], a[124:127], v[220:223]\n"
  "ds_read_b64_tr_b16 a[42:43], v12 offset:20992\n"
  "v_mfma_f32_16x16x32_bf16 v[224:227], a[104:107], a[112:115], v[224:227]\n"
  "ds_read_b64_tr_b16 a[44:45], v14 offset:20480\n"
  "v_mfma_f32_16x16x32_bf16 v[228:231], a[104:107], a[116:119], v[228:231]\n"
  "ds_read_b64_tr_b16 a[46:47], v14 offset:20992\n"
  "v_mfma_f32_16x16x32_bf16 v[232:235], a[104:107], a[120:123], v[232:235]\n"
  "s_add_u32 m0, s64, 93952\n"
  "v_mfma_f32_16x16x32_bf16 v[236:239], a[104:107], a[124:127], v[236:239]\n"
  "buffer_load_dwordx4 v2, s[32:35], s63 offen offset:256 lds\n"
  "v_mfma_f32_16x16x32_bf16 v[240:243], a[108:111], a[112:115], v[240:243]\n"
  "v_mfma_f32_16x16x32_bf16 v[244:247], a[108:111], a[116:119], v[244:247]\n"
  "v_mfma_f32_16x16x32_bf16 v[248:251], a[108:111], a[120:123], v[248:251]\n"
  "v_mfma_f32_16x16x32_bf16 v[252:255], a[108:111], a[124:127], v[252:255]\n"
  "s_waitcnt vmcnt(15) lgkmcnt(0)\n"
  "s_barrier\n"
  "v_mfma_f32_16x16x32_bf16 v[64:67], a[0:3], a[48:51], v[64:67]\n"
  "s_mul_i32 s62, s60, 1536\n"
  "s_add_u32 m0, s65, 34816\n"
  "s_lshl_b32 s70, s48, 4\n"
  "s_nop 0\n"
  "v_mfma_f32_16x16x32_bf16 v[68:71], a[0:3], a[52:55], v[68:71]\n"
  "buffer_load_dwordx4 v8, s[24:27], s60 offen lds\n"
  "s_add_u32 s70, s70, s47\n"
  "s_nop 0\n"
  "v_mfma_f32_16x16x32_bf16 v[72:75], a[0:3], a[56:59], v[72:75]\n"
  "ds_read_b128 a[112:115], v17 offset:0\n"
  "s_lshl_b32 s70, s70, 3\n"
  "s_nop 0\n"
  "v_mfma_f32_16x16x32_bf16 v[76:79], a[0:3], a[60:63], v[76:79]\n"
  "ds_read_b128 a[116:119], v17 offset:2048\n"
  "s_add_u32 s70, s70, 8192\n"
  "v_mfma_f32_16x16x32_bf16 v[80:83], a[4:7], a[48:51], v[80:83]\n"
  "ds_read_b128 a[120:123], v17 offset:4096\n"
  "v_mov_b32_e64 v24, s44\n"
  "v_mfma_f32_16x16x32_bf16 v[84:87], a[4:7], a[52:55], v[84:87]\n"
  "ds_read_b128 a[124:127], v17 offset:6144\n"
  "v_mov_b32_e64 v25, s45\n"
  "v_mfma_f32_16x16x32_bf16 v[88:91], a[4:7], a[56:59], v[88:91]\n"
  "s_add_u32 m0, s65, 35840\n"
  "s_mov_b64 exec, s[88:89]\n"
  "s_nop 0\n"
  "buffer_store_dwordx2 v[24:25], off, s[36:39], s70 sc1\n"
  "s_mov_b64 exec, -1\n"
  "s_nop 0\n"
  "v_mfma_f32_16x16x32_bf16 v[92:95], a[4:7], a[60:63], v[92:95]\n"
  "buffer_load_dwordx4 v9, s[24:27], s60 offen lds\n"
  "v_mfma_f32_16x16x32_bf16 v[96:99], a[8:11], a[48:51], v[96:99]\n"
  "ds_read_b64_tr_b16 a[64:65], v12 offset:24576\n"
  "v_mfma_f32_16x16x32_bf16 v[100:103], a[8:11], a[52:55], v[100:103]\n"
  "ds_read_b64_tr_b16 a[66:67], v12 offset:25088\n"
  "v_mfma_f32_16x16x32_bf16 v[104:107], a[8:11], a[56:59], v[104:107]\n"
  "ds_read_b64_tr_b16 a[68:69], v14 offset:24576\n"
  "v_mfma_f32_16x16x32_bf16 v[108:111], a[8:11], a[60:63], v[108:111]\n"
  "ds_read_b64_tr_b16 a[70:71], v14 offset:25088\n"
  "v_mfma_f32_16x16x32_bf16 v[112:115], a[12:15], a[48:51], v[112:115]\n"
  "s_add_u32 m0, s64, 0\n"
  "s_nop 0\n"
  "v_mfma_f32_16x16x32_bf16 v[116:119], a[12:15], a[52:55], v[116:119]\n"
  "buffer_load_dwordx4 v2, s[28:31], s62 offen lds\n"
  "v_mfma_f32_16x16x32_bf16 v[120:123], a[12:15], a[56:59], v[120:123]\n"
  "ds_read_b64_tr_b16 a[72:73], v12 offset:28672\n"
  "v_mfma_f32_16x16x32_bf16 v[124:127], a[12:15], a[60:63], v[124:127]\n"
  "ds_read_b64_tr_b16 a[74:75], v12 offset:29184\n"
  "v_mfma_f32_16x16x32_bf16 v[128:131], a[16:19], a[48:51], v[128:131]\n"
  "ds_read_b64_tr_b16 a[76:77], v14 offset:28672\n"
  "v_mfma_f32_16x16x32_bf16 v[132:135], a[16:19], a[52:55], v[132:135]\n"
  "ds_read_b64_tr_b16 a[78:79], v14 offset:29184\n"
  "v_mfma_f32_16x16x32_bf16 v[136:139], a[16:19], a[56:59], v[136:139]\n"
  "s_add_u32 m0, s64, 4096\n"
  "v_mfma_f32_16x16x32_bf16 v[140:143], a[16:19], a[60:63], v[140:143]\n"
  "buffer_load_dwordx4 v2, s[32:35], s62 offen lds\n"
  "v_mfma_f32_16x16x32_bf16 v[144:147], a[20:23], a[48:51], v[144:147]\n"
  "ds_read_b64_tr_b16 a[80:81], v12 offset:32768\n"
  "v_mfma_f32_16x16x32_bf16 v[148:151], a[20:23], a[52:55], v[148:151]\n"
  "ds_read_b64_tr_b16 a[82:83], v12 offset:33280\n"
  "v_mfma_f32_16x16x32_bf16 v[152:155], a[20:23], a[56:59], v[152:155]\n"
  "ds_read_b64_tr_b16 a[84:85], v14 offset:32768\n"
  "v_mfma_f32_16x16x32_bf16 v[156:159], a[20:23], a[60:63], v[156:159]\n"
  "ds_read_b64_tr_b16 a[86:87], v14 offset:33280\n"
  "v_mfma_f32_16x16x32_bf16 v[160:163], a[24:27], a[48:51], v[160:163]\n"
  "s_add_u32 m0, s64, 8064\n"
  "v_mfma_f32_16x16x32_bf16 v[164:167], a[24:27], a[52:55], v[164:167]\n"
  "buffer_load_dwordx4 v2, s[28:31], s62 offen offset:128 lds\n"
  "v_mfma_f32_16x16x32_bf16 v[168:171], a[24:27], a[56:59], v[168:171]\n"
  "ds_read_b64_tr_b16 a[88:89], v12 offset:36864\n"
  "v_mfma_f32_16x16x32_bf16 v[172:175], a[24:27], a[60:63], v[172:175]\n"
  "ds_read_b64_tr_b16 a[90:91], v12 offset:37376\n"
  "v_mfma_f32_16x16x32_bf16 v[176:179], a[28:31], a[48:51], v[176:179]\n"
  "ds_read_b64_tr_b16 a[92:93], v14 offset:36864\n"
  "v_mfma_f32_16x16x32_bf16 v[180:183], a[28:31], a[52:55], v[180:183]\n"
  "ds_read_b64_tr_b16 a[94:95], v14 offset:37376\n"
  "v_mfma_f32_16x16x32_bf16 v[184:187], a[28:31], a[56:59], v[184:187]\n"
  "s_add_u32 m0, s64, 12160\n"
  "v_mfma_f32_16x16x32_bf16 v[188:191], a[28:31], a[60:63], v[188:191]\n"
  "buffer_load_dwordx4 v2, s[32:35], s62 offen offset:128 lds\n"
  "v_mfma_f32_16x16x32_bf16 v[192:195], a[32:35], a[48:51], v[192:195]\n"
  "ds_read_b64_tr_b16 a[96:97], v12 offset:40960\n"
  "v_mfma_f32_16x16x32_bf16 v[196:199], a[32:35], a[52:55], v[196:199]\n"
  "ds_read_b64_tr_b16 a[98:99], v12 offset:41472\n"
  "v_mfma_f32_16x16x32_bf16 v[200:203], a[32:35], a[56:59], v[200:203]\n"
  "ds_read_b64_tr_b16 a[100:101], v14 offset:40960\n"
  "v_mfma_f32_16x16x32_bf16 v[204:207], a[32:35], a[60:63], v[204:207]\n"
  "ds_read_b64_tr_b16 a[102:103], v14 offset:41472\n"
  "v_mfma_f32_16x16x32_bf16 v[208:211], a[36:39], a[48:51], v[208:211]\n"
  "s_add_u32 m0, s64, 16128\n"
  "v_mfma_f32_16x16x32_bf16 v[212:215], a[36:39], a[52:55], v[212:215]\n"
  "buffer_load_dwordx4 v2, s[28:31], s62 offen offset:256 lds\n"
  "v_mfma_f32_16x16x32_bf16 v[216:219], a[36:39], a[56:59], v[216:219]\n"
  "ds_read_b64_tr_b16 a[104:105], v12 offset:45056\n"
  "v_mfma_f32_16x16x32_bf16 v[220:223], a[36:39], a[60:63], v[220:223]\n"
  "ds_read_b64_tr_b16 a[106:107], v12 offset:45568\n"
  "v_mfma_f32_16x16x32_bf16 v[224:227], a[40:43], a[48:51], v[224:227]\n"
  "ds_read_b64_tr_b16 a[108:109], v14 offset:45056\n"
  "v_mfma_f32_16x16x32_bf16 v[228:231], a[40:43], a[52:55], v[228:231]\n"
  "ds_read_b64_tr_b16 a[110:111], v14 offset:45568\n"
  "v_mfma_f32_16x16x32_bf16 v[232:235], a[40:43], a[56:59], v[232:235]\n"
  "s_add_u32 m0, s64, 20224\n"
  "v_mfma_f32_16x16x32_bf16 v[236:239], a[40:43], a[60:63], v[236:239]\n"
  "buffer_load_dwordx4 v2, s[32:35], s62 offen offset:256 lds\n"
  "v_mfma_f32_16x16x32_bf16 v[240:243], a[44:47], a[48:51], v[240:243]\n"
  "v_mfma_f32_16x16x32_bf16 v[244:247], a[44:47], a[52:55], v[244:247]\n"
  "v_mfma_f32_16x16x32_bf16 v[248:251], a[44:47], a[56:59], v[248:251]\n"
  "v_mfma_f32_16x16x32_bf16 v[252:255], a[44:47], a[60:63], v[252:255]\n"
  "s_waitcnt vmcnt(17) lgkmcnt(0)\n"
  "s_barrier\n"
  "v_mfma_f32_16x16x32_bf16 v[64:67], a[64:67], a[112:115], v[64:67]\n"
  "s_add_u32 s63, s62, 98304\n"
  "s_add_u32 m0, s65, 0\n"
  "s_nop 0\n"
  "v_mfma_f32_16x16x32_bf16 v[68:71], a[64:67], a[116:119], v[68:71]\n"
  "buffer_load_dwordx4 v6, s[24:27], s61 offen lds\n"
  "v_mfma_f32_16x16x32_bf16 v[72:75], a[64:67], a[120:123], v[72:75]\n"
  "ds_read_b128 a[48:51], v16 offset:16384\n"
  "v_mfma_f32_16x16x32_bf16 v[76:79], a[64:67], a[124:127], v[76:79]\n"
  "ds_read_b128 a[52:55], v16 offset:18432\n"
  "v_mfma_f32_16x16x32_bf16 v[80:83], a[68:71], a[112:115], v[80:83]\n"
  "ds_read_b128 a[56:59], v16 offset:20480\n"
  "v_mfma_f32_16x16x32_bf16 v[84:87], a[68:71], a[116:119], v[84:87]\n"
  "ds_read_b128 a[60:63], v16 offset:22528\n"
  "v_mfma_f32_16x16x32_bf16 v[88:91], a[68:71], a[120:123], v[88:91]\n"
  "s_add_u32 m0, s65, 1024\n"
  "v_mfma_f32_16x16x32_bf16 v[92:95], a[68:71], a[124:127], v[92:95]\n"
  "buffer_load_dwordx4 v7, s[24:27], s61 offen lds\n"
  "v_mfma_f32_16x16x32_bf16 v[96:99], a[72:75], a[112:115], v[96:99]\n"
  "ds_read_b64_tr_b16 a[0:1], v13 offset:0\n"
  "v_mfma_f32_16x16x32_bf16 v[100:103], a[72:75], a[116:119], v[100:103]\n"
  "ds_read_b64_tr_b16 a[2:3], v13 offset:512\n"
  "v_mfma_f32_16x16x32_bf16 v[104:107], a[72:75], a[120:123], v[104:107]\n"
  "ds_read_b64_tr_b16 a[4:5], v15 offset:0\n"
  "v_mfma_f32_16x16x32_bf16 v[108:111], a[72:75], a[124:127], v[108:111]\n"
  "ds_read_b64_tr_b16 a[6:7], v15 offset:512\n"
  "v_mfma_f32_16x16x32_bf16 v[112:115], a[76:79], a[112:115], v[112:115]\n"
  "s_add_u32 m0, s64, 24576\n"
  "v_mfma_f32_16x16x32_bf16 v[116:119], a[76:79], a[116:119], v[116:119]\n"
  "buffer_load_dwordx4 v2, s[28:31], s63 offen lds\n"
  "v_mfma_f32_16x16x32_bf16 v[120:123], a[76:79], a[120:123], v[120:123]\n"
  "ds_read_b64_tr_b16 a[8:9], v13 offset:4096\n"
  "v_mfma_f32_16x16x32_bf16 v[124:127], a[76:79], a[124:127], v[124:127]\n"
  "ds_read_b64_tr_b16 a[10:11], v13 offset:4608\n"
  "v_mfma_f32_16x16x32_bf16 v[128:131], a[80:83], a[112:115], v[128:131]\n"
  "ds_read_b64_tr_b16 a[12:13], v15 offset:4096\n"
  "v_mfma_f32_16x16x32_bf16 v[132:135], a[80:83], a[116:119], v[132:135]\n"
  "ds_read_b64_tr_b16 a[14:15], v15 offset:4608\n"
  "v_mfma_f32_16x16x32_bf16 v[136:139], a[80:83], a[120:123], v[136:139]\n"
  "s_add_u32 m0, s64, 28672\n"
  "v_mfma_f32_16x16x32_bf16 v[140:143], a[80:83], a[124:127], v[140:143]\n"
  "buffer_load_dwordx4 v2, s[32:35], s63 offen lds\n"
  "v_mfma_f32_16x16x32_bf16 v[144:147], a[84:87], a[112:115], v[144:147]\n"
  "ds_read_b64_tr_b16 a[16:17], v13 offset:8192\n"
  "v_mfma_f32_16x16x32_bf16 v[148:151], a[84:87], a[116:119], v[148:151]\n"
  "ds_read_b64_tr_b16 a[18:19], v13 offset:8704\n"
  "v_mfma_f32_16x16x32_bf16 v[152:155], a[84:87], a[120:123], v[152:155]\n"
  "ds_read_b64_tr_b16 a[20:21], v15 offset:8192\n"
  "v_mfma_f32_16x16x32_bf16 v[156:159], a[84:87], a[124:127], v[156:159]\n"
  "ds_read_b64_tr_b16 a[22:23], v15 offset:8704\n"
  "v_mfma_f32_16x16x32_bf16 v[160:163], a[88:91], a[112:115], v[160:163]\n"
  "s_add_u32 m0, s64, 32640\n"
  "v_mfma_f32_16x16x32_bf16 v[164:167], a[88:91], a[116:119], v[164:167]\n"
  "buffer_load_dwordx4 v2, s[28:31], s63 offen offset:128 lds\n"
  "v_mfma_f32_16x16x32_bf16 v[168:171], a[88:91], a[120:123], v[168:171]\n"
  "ds_read_b64_tr_b16 a[24:25], v13 offset:12288\n"
  "v_mfma_f32_16x16x32_bf16 v[172:175], a[88:91], a[124:127], v[172:175]\n"
  "ds_read_b64_tr_b16 a[26:27], v13 offset:12800\n"
  "v_mfma_f32_16x16x32_bf16 v[176:179], a[92:95], a[112:115], v[176:179]\n"
  "ds_read_b64_tr_b16 a[28:29], v15 offset:12288\n"
  "v_mfma_f32_16x16x32_bf16 v[180:183], a[92:95], a[116:119], v[180:183]\n"
  "ds_read_b64_tr_b16 a[30:31], v15 offset:12800\n"
  "v_mfma_f32_16x16x32_bf16 v[184:187], a[92:95], a[120:123], v[184:187]\n"
  "s_add_u32 m0, s64, 36736\n"
  "v_mfma_f32_16x16x32_bf16 v[188:191], a[92:95], a[124:127], v[188:191]\n"
  "buffer_load_dwordx4 v2, s[32:35], s63 offen offset:128 lds\n"
  "v_mfma_f32_16x16x32_bf16 v[192:195], a[96:99], a[112:115], v[192:195]\n"
  "ds_read_b64_tr_b16 a[32:33], v13 offset:16384\n"
  "v_mfma_f32_16x16x32_bf16 v[196:199], a[96:99], a[116:119], v[196:199]\n"
  "ds_read_b64_tr_b16 a[34:35], v13 offset:16896\n"
  "v_mfma_f32_16x16x32_bf16 v[200:203], a[96:99], a[120:123], v[200:203]\n"
  "ds_read_b64_tr_b16 a[36:37], v15 offset:16384\n"
  "v_mfma_f32_16x16x32_bf16 v[204:207], a[96:99], a[124:127], v[204:207]\n"
  "ds_read_b64_tr_b16 a[38:39], v15 offset:16896\n"
  "v_mfma_f32_16x16x32_bf16 v[208:211], a[100:103], a[112:115], v[208:211]\n"
  "s_add_u32 m0, s64, 40704\n"
  "v_mfma_f32_16x16x32_bf16 v[212:215], a[100:103], a[116:119], v[212:215]\n"
  "buffer_load_dwordx4 v2, s[28:31], s63 offen offset:256 lds\n"
  "v_mfma_f32_16x16x32_bf16 v[216:219], a[100:103], a[120:123], v[216:219]\n"
  "ds_read_b64_tr_b16 a[40:41], v13 offset:20480\n"
  "v_mfma_f32_16x16x32_bf16 v[220:223], a[100:103], a[124:127], v[220:223]\n"
  "ds_read_b64_tr_b16 a[42:43], v13 offset:20992\n"
  "v_mfma_f32_16x16x32_bf16 v[224:227], a[104:107], a[112:115], v[224:227]\n"
  "ds_read_b64_tr_b16 a[44:45], v15 offset:20480\n"
  "v_mfma_f32_16x16x32_bf16 v[228:231], a[104:107], a[116:119], v[228:231]\n"
  "ds_read_b64_tr_b16 a[46:47], v15 offset:20992\n"
  "v_mfma_f32_16x16x32_bf16 v[232:235], a[104:107], a[120:123], v[232:235]\n"
  "s_add_u32 m0, s64, 44800\n"
  "v_mfma_f32_16x16x32_bf16 v[236:239], a[104:107], a[124:127], v[236:239]\n"
  "buffer_load_dwordx4 v2, s[32:35], s63 offen offset:256 lds\n"
  "v_mfma_f32_16x16x32_bf16 v[240:243], a[108:111], a[112:115], v[240:243]\n"
  "v_mfma_f32_16x16x32_bf16 v[244:247], a[108:111], a[116:119], v[244:247]\n"
  "v_mfma_f32_16x16x32_bf16 v[248:251], a[108:111], a[120:123], v[248:251]\n"
  "v_mfma_f32_16x16x32_bf16 v[252:255], a[108:111], a[124:127], v[252:255]\n"
  "s_waitcnt vmcnt(17) lgkmcnt(0)\n"
  "s_barrier\n"
  "v_mfma_f32_16x16x32_bf16 v[64:67], a[0:3], a[48:51], v[64:67]\n"
  "s_mul_i32 s62, s61, 1536\n"
  "s_add_u32 m0, s65, 2048\n"
  "s_movk_i32 s70, 4096\n"
  "s_nop 0\n"
  "v_mfma_f32_16x16x32_bf16 v[68:71], a[0:3], a[52:55], v[68:71]\n"
  "buffer_load_dwordx4 v8, s[24:27], s61 offen lds\n"
  "buffer_load_dwordx4 v[40:43], v18, s[36:39], s70 offen sc1\n"
  "v_mfma_f32_16x16x32_bf16 v[72:75], a[0:3], a[56:59], v[72:75]\n"
  "ds_read_b128 a[112:115], v17 offset:16384\n"
  "buffer_load_dwordx4 v[44:47], v18, s[36:39], s70 offen offset:1024 sc1\n"
  "v_mfma_f32_16x16x32_bf16 v[76:79], a[0:3], a[60:63], v[76:79]\n"
  "ds_read_b128 a[116:119], v17 offset:18432\n"
  "v_mfma_f32_16x16x32_bf16 v[80:83], a[4:7], a[48:51], v[80:83]\n"
  "ds_read_b128 a[120:123], v17 offset:20480\n"
  "v_mfma_f32_16x16x32_bf16 v[84:87], a[4:7], a[52:55], v[84:87]\n"
  "ds_read_b128 a[124:127], v17 offset:22528\n"
  "v_mfma_f32_16x16x32_bf16 v[88:91], a[4:7], a[56:59], v[88:91]\n"
  "s_add_u32 m0, s65, 3072\n"
  "v_mfma_f32_16x16x32_bf16 v[92:95], a[4:7], a[60:63], v[92:95]\n"
  "buffer_load_dwordx4 v9, s[24:27], s61 offen lds\n"
  "v_mfma_f32_16x16x32_bf16 v[96:99], a[8:11], a[48:51], v[96:99]\n"
  "ds_read_b64_tr_b16 a[64:65], v13 offset:24576\n"
  "v_mfma_f32_16x16x32_bf16 v[100:103], a[8:11], a[52:55], v[100:103]\n"
  "ds_read_b64_tr_b16 a[66:67], v13 offset:25088\n"
  "v_mfma_f32_16x16x32_bf16 v[104:107], a[8:11], a[56:59], v[104:107]\n"
  "ds_read_b64_tr_b16 a[68:69], v15 offset:24576\n"
  "v_mfma_f32_16x16x32_bf16 v[108:111], a[8:11], a[60:63], v[108:111]\n"
  "ds_read_b64_tr_b16 a[70:71], v15 offset:25088\n"
  "v_mfma_f32_16x16x32_bf16 v[112:115], a[12:15], a[48:51], v[112:115]\n"
  "s_add_u32 m0, s64, 49152\n"
  "v_mfma_f32_16x16x32_bf16 v[116:119], a[12:15], a[52:55], v[116:119]\n"
  "buffer_load_dwordx4 v2, s[28:31], s62 offen lds\n"
  "v_mfma_f32_16x16x32_bf16 v[120:123], a[12:15], a[56:59], v[120:123]\n"
  "ds_read_b64_tr_b16 a[72:73], v13 offset:28672\n"
  "v_mfma_f32_16x16x32_bf16 v[124:127], a[12:15], a[60:63], v[124:127]\n"
  "ds_read_b64_tr_b16 a[74:75], v13 offset:29184\n"
  "v_mfma_f32_16x16x32_bf16 v[128:131], a[16:19], a[48:51], v[128:131]\n"
  "ds_read_b64_tr_b16 a[76:77], v15 offset:28672\n"
  "v_mfma_f32_16x16x32_bf16 v[132:135], a[16:19], a[52:55], v[132:135]\n"
  "ds_read_b64_tr_b16 a[78:79], v15 offset:29184\n"
  "v_mfma_f32_16x16x32_bf16 v[136:139], a[16:19], a[56:59], v[136:139]\n"
  "s_add_u32 m0, s64, 53248\n"
  "v_mfma_f32_16x16x32_bf16 v[140:143], a[16:19], a[60:63], v[140:143]\n"
  "buffer_load_dwordx4 v2, s[32:35], s62 offen lds\n"
  "v_mfma_f32_16x16x32_bf16 v[144:147], a[20:23], a[48:51], v[144:147]\n"
  "ds_read_b64_tr_b16 a[80:81], v13 offset:32768\n"
  "v_mfma_f32_16x16x32_bf16 v[148:151], a[20:23], a[52:55], v[148:151]\n"
  "ds_read_b64_tr_b16 a[82:83], v13 offset:33280\n"
  "v_mfma_f32_16x16x32_bf16 v[152:155], a[20:23], a[56:59], v[152:155]\n"
  "ds_read_b64_tr_b16 a[84:85], v15 offset:32768\n"
  "v_mfma_f32_16x16x32_bf16 v[156:159], a[20:23], a[60:63], v[156:159]\n"
  "ds_read_b64_tr_b16 a[86:87], v15 offset:33280\n"
  "v_mfma_f32_16x16x32_bf16 v[160:163], a[24:27], a[48:51], v[160:163]\n"
  "s_add_u32 m0, s64, 57216\n"
  "v_mfma_f32_16x16x32_bf16 v[164:167], a[24:27], a[52:55], v[164:167]\n"
  "buffer_load_dwordx4 v2, s[28:31], s62 offen offset:128 lds\n"
  "v_mfma_f32_16x16x32_bf16 v[168:171], a[24:27], a[56:59], v[168:171]\n"
  "ds_read_b64_tr_b16 a[88:89], v13 offset:36864\n"
  "v_mfma_f32_16x16x32_bf16 v[172:175], a[24:27], a[60:63], v[172:175]\n"
  "ds_read_b64_tr_b16 a[90:91], v13 offset:37376\n"
  "v_mfma_f32_16x16x32_bf16 v[176:179], a[28:31], a[48:51], v[176:179]\n"
  "ds_read_b64_tr_b16 a[92:93], v15 offset:36864\n"
  "v_mfma_f32_16x16x32_bf16 v[180:183], a[28:31], a[52:55], v[180:183]\n"
  "ds_read_b64_tr_b16 a[94:95], v15 offset:37376\n"
  "v_mfma_f32_16x16x32_bf16 v[184:187], a[28:31], a[56:59], v[184:187]\n"
  "s_add_u32 m0, s64, 61312\n"
  "v_mfma_f32_16x16x32_bf16 v[188:191], a[28:31], a[60:63], v[188:191]\n"
  "buffer_load_dwordx4 v2, s[32:35], s62 offen offset:128 lds\n"
  "v_mfma_f32_16x16x32_bf16 v[192:195], a[32:35], a[48:51], v[192:195]\n"
  "ds_read_b64_tr_b16 a[96:97], v13 offset:40960\n"
  "v_mfma_f32_16x16x32_bf16 v[196:199], a[32:35], a[52:55], v[196:199]\n"
  "ds_read_b64_tr_b16 a[98:99], v13 offset:41472\n"
  "v_mfma_f32_16x16x32_bf16 v[200:203], a[32:35], a[56:59], v[200:203]\n"
  "ds_read_b64_tr_b16 a[100:101], v15 offset:40960\n"
  "v_mfma_f32_16x16x32_bf16 v[204:207], a[32:35], a[60:63], v[204:207]\n"
  "ds_read_b64_tr_b16 a[102:103], v15 offset:41472\n"
  "v_mfma_f32_16x16x32_bf16 v[208:211], a[36:39], a[48:51], v[208:211]\n"
  "s_add_u32 m0, s64, 65280\n"
  "v_mfma_f32_16x16x32_bf16 v[212:215], a[36:39], a[52:55], v[212:215]\n"
  "buffer_load_dwordx4 v2, s[28:31], s62 offen offset:256 lds\n"
  "v_mfma_f32_16x16x32_bf16 v[216:219], a[36:39], a[56:59], v[216:219]\n"
  "ds_read_b64_tr_b16 a[104:105], v13 offset:45056\n"
  "v_mfma_f32_16x16x32_bf16 v[220:223], a[36:39], a[60:63], v[220:223]\n"
  "ds_read_b64_tr_b16 a[106:107], v13 offset:45568\n"
  "v_mfma_f32_16x16x32_bf16 v[224:227], a[40:43], a[48:51], v[224:227]\n"
  "ds_read_b64_tr_b16 a[108:109], v15 offset:45056\n"
  "v_mfma_f32_16x16x32_bf16 v[228:231], a[40:43], a[52:55], v[228:231]\n"
  "ds_read_b64_tr_b16 a[110:111], v15 offset:45568\n"
  "v_mfma_f32_16x16x32_bf16 v[232:235], a[40:43], a[56:59], v[232:235]\n"
  "s_add_u32 m0, s64, 69376\n"
  "v_mfma_f32_16x16x32_bf16 v[236:239], a[40:43], a[60:63], v[236:239]\n"
  "buffer_load_dwordx4 v2, s[32:35], s62 offen offset:256 lds\n"
  "v_mfma_f32_16x16x32_bf16 v[240:243], a[44:47], a[48:51], v[240:243]\n"
  "v_mfma_f32_16x16x32_bf16 v[244:247], a[44:47], a[52:55], v[244:247]\n"
  "v_mfma_f32_16x16x32_bf16 v[248:251], a[44:47], a[56:59], v[248:251]\n"
  "v_mfma_f32_16x16x32_bf16 v[252:255], a[44:47], a[60:63], v[252:255]\n"
  "s_waitcnt vmcnt(18) lgkmcnt(0)\n"
  "s_barrier\n"
  "v_mfma_f32_16x16x32_bf16 v[64:67], a[64:67], a[112:115], v[64:67]\n"
  "s_add_u32 s63, s62, 98304\n"
  "s_add_u32 m0, s64, 73728\n"
  "v_and_b32_e64 v32, 15, v0\n"
  "v_mfma_f32_16x16x32_bf16 v[68:71], a[64:67], a[116:119], v[68:71]\n"
  "buffer_load_dwordx4 v2, s[28:31], s63 offen lds\n"
  "v_lshlrev_b32_e64 v32, 3, v32\n"
  "v_mfma_f32_16x16x32_bf16 v[72:75], a[64:67], a[120:123], v[72:75]\n"
  "ds_read_b128 a[48:51], v16 offset:32768\n"
  "s_lshl_b32 s70, s48, 7\n"
  "s_nop 0\n"
  "v_mfma_f32_16x16x32_bf16 v[76:79], a[64:67], a[124:127], v[76:79]\n"
  "ds_read_b128 a[52:55], v16 offset:34816\n"
  "s_add_u32 s70, s70, 8192\n"
  "v_mfma_f32_16x16x32_bf16 v[80:83], a[68:71], a[112:115], v[80:83]\n"
  "ds_read_b128 a[56:59], v16 offset:36864\n"
  "buffer_load_dwordx2 v[34:35], v32, s[36:39], s70 offen sc1\n"
  "v_mfma_f32_16x16x32_bf16 v[84:87], a[68:71], a[116:119], v[84:87]\n"
  "ds_read_b128 a[60:63], v16 offset:38912\n"
  "v_mfma_f32_16x16x32_bf16 v[88:91], a[68:71], a[120:123], v[88:91]\n"
  "s_add_u32 m0, s64, 77824\n"
  "v_mfma_f32_16x16x32_bf16 v[92:95], a[68:71], a[124:127], v[92:95]\n"
  "buffer_load_dwordx4 v2, s[32:35], s63 offen lds\n"
  "v_mfma_f32_16x16x32_bf16 v[96:99], a[72:75], a[112:115], v[96:99]\n"
  "ds_read_b64_tr_b16 a[0:1], v12 offset:0\n"
  "v_mfma_f32_16x16x32_bf16 v[100:103], a[72:75], a[116:119], v[100:103]\n"
  "ds_read_b64_tr_b16 a[2:3], v12 offset:512\n"
  "v_mfma_f32_16x16x32_bf16 v[104:107], a[72:75], a[120:123], v[104:107]\n"
  "ds_read_b64_tr_b16 a[4:5], v14 offset:0\n"
  "v_mfma_f32_16x16x32_bf16 v[108:111], a[72:75], a[124:127], v[108:111]\n"
  "ds_read_b64_tr_b16 a[6:7], v14 offset:512\n"
  "v_mfma_f32_16x16x32_bf16 v[112:115], a[76:79], a[112:115], v[112:115]\n"
  "s_add_u32 m0, s64, 81792\n"
  "v_mfma_f32_16x16x32_bf16 v[116:119], a[76:79], a[116:119], v[116:119]\n"
  "buffer_load_dwordx4 v2, s[28:31], s63 offen offset:128 lds\n"
  "v_mfma_f32_16x16x32_bf16 v[120:123], a[76:79], a[120:123], v[120:123]\n"
  "ds_read_b64_tr_b16 a[8:9], v12 offset:4096\n"
  "v_mfma_f32_16x16x32_bf16 v[124:127], a[76:79], a[124:127], v[124:127]\n"
  "ds_read_b64_tr_b16 a[10:11], v12 offset:4608\n"
  "v_mfma_f32_16x16x32_bf16 v[128:131], a[80:83], a[112:115], v[128:131]\n"
  "ds_read_b64_tr_b16 a[12:13], v14 offset:4096\n"
  "v_mfma_f32_16x16x32_bf16 v[132:135], a[80:83], a[116:119], v[132:135]\n"
  "ds_read_b64_tr_b16 a[14:15], v14 offset:4608\n"
  "v_mfma_f32_16x16x32_bf16 v[136:139], a[80:83], a[120:123], v[136:139]\n"
  "s_add_u32 m0, s64, 85888\n"
  "v_mfma_f32_16x16x32_bf16 v[140:143], a[80:83], a[124:127], v[140:143]\n"
  "buffer_load_dwordx4 v2, s[32:35], s63 offen offset:128 lds\n"
  "v_mfma_f32_16x16x32_bf16 v[144:147], a[84:87], a[112:115], v[144:147]\n"
  "ds_read_b64_tr_b16 a[16:17], v12 offset:8192\n"
  "v_mfma_f32_16x16x32_bf16 v[148:151], a[84:87], a[116:119], v[148:151]\n"
  "ds_read_b64_tr_b16 a[18:19], v12 offset:8704\n"
  "v_mfma_f32_16x16x32_bf16 v[152:155], a[84:87], a[120:123], v[152:155]\n"
  "ds_read_b64_tr_b16 a[20:21], v14 offset:8192\n"
  "v_mfma_f32_16x16x32_bf16 v[156:159], a[84:87], a[124:127], v[156:159]\n"
  "ds_read_b64_tr_b16 a[22:23], v14 offset:8704\n"
  "v_mfma_f32_16x16x32_bf16 v[160:163], a[88:91], a[112:115], v[160:163]\n"
  "s_add_u32 m0, s64, 89856\n"
  "v_mfma_f32_16x16x32_bf16 v[164:167], a[88:91], a[116:119], v[164:167]\n"
  "buffer_load_dwordx4 v2, s[28:31], s63 offen offset:256 lds\n"
  "v_mfma_f32_16x16x32_bf16 v[168:171], a[88:91], a[120:123], v[168:171]\n"
  "ds_read_b64_tr_b16 a[24:25], v12 offset:12288\n"
  "v_mfma_f32_16x16x32_bf16 v[172:175], a[88:91], a[124:127], v[172:175]\n"
  "ds_read_b64_tr_b16 a[26:27], v12 offset:12800\n"
  "v_mfma_f32_16x16x32_bf16 v[176:179], a[92:95], a[112:115], v[176:179]\n"
  "ds_read_b64_tr_b16 a[28:29], v14 offset:12288\n"
  "v_mfma_f32_16x16x32_bf16 v[180:183], a[92:95], a[116:119], v[180:183]\n"
  "ds_read_b64_tr_b16 a[30:31], v14 offset:12800\n"
  "v_mfma_f32_16x16x32_bf16 v[184:187], a[92:95], a[120:123], v[184:187]\n"
  "s_add_u32 m0, s64, 93952\n"
  "v_mfma_f32_16x16x32_bf16 v[188:191], a[92:95], a[124:127], v[188:191]\n"
  "buffer_load_dwordx4 v2, s[32:35], s63 offen offset:256 lds\n"
  "v_mfma_f32_16x16x32_bf16 v[192:195], a[96:99], a[112:115], v[192:195]\n"
  "ds_read_b64_tr_b16 a[32:33], v12 offset:16384\n"
  "v_mfma_f32_16x16x32_bf16 v[196:199], a[96:99], a[116:119], v[196:199]\n"
  "ds_read_b64_tr_b16 a[34:35], v12 offset:16896\n"
  "v_mfma_f32_16x16x32_bf16 v[200:203], a[96:99], a[120:123], v[200:203]\n"
  "ds_read_b64_tr_b16 a[36:37], v14 offset:16384\n"
  "v_mfma_f32_16x16x32_bf16 v[204:207], a[96:99], a[124:127], v[204:207]\n"
  "ds_read_b64_tr_b16 a[38:39], v14 offset:16896\n"
  "v_mfma_f32_16x16x32_bf16 v[208:211], a[100:103], a[112:115], v[208:211]\n"
  "ds_read_b64_tr_b16 a[40:41], v12 offset:20480\n"
  "v_mfma_f32_16x16x32_bf16 v[212:215], a[100:103], a[116:119], v[212:215]\n"
  "ds_read_b64_tr_b16 a[42:43], v12 offset:20992\n"
  "v_mfma_f32_16x16x32_bf16 v[216:219], a[100:103], a[120:123], v[216:219]\n"
  "ds_read_b64_tr_b16 a[44:45], v14 offset:20480\n"
  "v_mfma_f32_16x16x32_bf16 v[220:223], a[100:103], a[124:127], v[220:223]\n"
  "ds_read_b64_tr_b16 a[46:47], v14 offset:20992\n"
  "v_mfma_f32_16x16x32_bf16 v[224:227], a[104:107], a[112:115], v[224:227]\n"
  "v_mfma_f32_16x16x32_bf16 v[228:231], a[104:107], a[116:119], v[228:231]\n"
  "v_mfma_f32_16x16x32_bf16 v[232:235], a[104:107], a[120:123], v[232:235]\n"
  "v_mfma_f32_16x16x32_bf16 v[236:239], a[104:107], a[124:127], v[236:239]\n"
  "v_mfma_f32_16x16x32_bf16 v[240:243], a[108:111], a[112:115], v[240:243]\n"
  "v_mfma_f32_16x16x32_bf16 v[244:247], a[108:111], a[116:119], v[244:247]\n"
  "v_mfma_f32_16x16x32_bf16 v[248:251], a[108:111], a[120:123], v[248:251]\n"
  "v_mfma_f32_16x16x32_bf16 v[252:255], a[108:111], a[124:127], v[252:255]\n"
  "s_waitcnt vmcnt(5) lgkmcnt(0)\n"
  "s_barrier\n"
  "v_cmp_ne_u64_e64 s[66:67], s[44:45], v[40:41]\n"
  "v_cmp_ne_u64_e64 s[68:69], s[44:45], v[42:43]\n"
  "s_nop 0\n"
  "s_or_b64 s[66:67], s[66:67], s[68:69]\n"
  "v_cmp_ne_u64_e64 s[68:69], s[44:45], v[44:45]\n"
  "s_nop 0\n"
  "s_or_b64 s[66:67], s[66:67], s[68:69]\n"
  "v_cmp_ne_u64_e64 s[68:69], s[44:45], v[46:47]\n"
  "s_nop 0\n"
  "s_or_b64 s[66:67], s[66:67], s[68:69]\n"
  "s_cmp_eq_u64 s[66:67], 0\n"
  "s_cbranch_scc1 .Lpok21\n"
  "s_mov_b32 s87, 0x40000\n"
  ".Lpsp21:\n"
  "s_sleep 2\n"
  "s_movk_i32 s70, 4096\n"
  "buffer_load_dwordx4 v[40:43], v18, s[36:39], s70 offen sc1\n"
  "buffer_load_dwordx4 v[44:47], v18, s[36:39], s70 offen offset:1024 sc1\n"
  "s_waitcnt vmcnt(0)\n"
  "s_nop 0\n"
  "v_cmp_ne_u64_e64 s[66:67], s[44:45], v[40:41]\n"
  "v_cmp_ne_u64_e64 s[68:69], s[44:45], v[42:43]\n"
  "s_nop 0\n"
  "s_or_b64 s[66:67], s[66:67], s[68:69]\n"
  "v_cmp_ne_u64_e64 s[68:69], s[44:45], v[44:45]\n"
  "s_nop 0\n"
  "s_or_b64 s[66:67], s[66:67], s[68:69]\n"
  "v_cmp_ne_u64_e64 s[68:69], s[44:45], v[46:47]\n"
  "s_nop 0\n"
  "s_or_b64 s[66:67], s[66:67], s[68:69]\n"
  "s_cmp_eq_u64 s[66:67], 0\n"
  "s_cbranch_scc1 .Lpok21\n"
  "s_sub_u32 s87, s87, 1\n"
  "s_cmp_lg_u32 s87, 0\n"
  "s_cbranch_scc1 .Lpsp21\n"
  ".Lpok21:\n"
  "s_nop 0\n"
  "v_cmp_ne_u64_e64 s[66:67], s[44:45], v[34:35]\n"
  "s_nop 0\n"
  "s_cmp_eq_u64 s[66:67], 0\n"
  "s_cbranch_scc1 .Lpok22\n"
  "s_nop 0\n"
  "s_mov_b32 s87, 0x40000\n"
  ".Lpsp22:\n"
  "s_sleep 2\n"
  "v_and_b32 v32, 15, v0\n"
  "v_lshlrev_b32 v32, 3, v32\n"
  "s_lshl_b32 s70, s48, 7\n"
  "s_add_u32 s70, s70, 8192\n"
  "buffer_load_dwordx2 v[34:35], v32, s[36:39], s70 offen sc1\n"
  "s_waitcnt vmcnt(0)\n"
  "s_nop 0\n"
  "v_cmp_ne_u64_e64 s[66:67], s[44:45], v[34:35]\n"
  "s_nop 0\n"
  "s_cmp_eq_u64 s[66:67], 0\n"
  "s_cbranch_scc1 .Lpok22\n"
  "s_sub_u32 s87, s87, 1\n"
  "s_cmp_lg_u32 s87, 0\n"
  "s_cbranch_scc1 .Lpsp22\n"
  ".Lpok22:\n"
  "v_mfma_f32_16x16x32_bf16 v[64:67], a[0:3], a[48:51], v[64:67]\n"
  "ds_read_b128 a[112:115], v17 offset:32768\n"
  "s_mul_i32 s70, s48, 15360\n"
  "v_mfma_f32_16x16x32_bf16 v[68:71], a[0:3], a[52:55], v[68:71]\n"
  "ds_read_b128 a[116:119], v17 offset:34816\n"
  "s_lshl_b32 s71, s46, 12\n"
  "s_nop 0\n"
  "v_mfma_f32_16x16x32_bf16 v[72:75], a[0:3], a[56:59], v[72:75]\n"
  "ds_read_b128 a[120:123], v17 offset:36864\n"
  "v_mfma_f32_16x16x32_bf16 v[76:79], a[0:3], a[60:63], v[76:79]\n"
  "ds_read_b128 a[124:127], v17 offset:38912\n"
  "s_add_u32 s70, s70, s71\n"
  "s_nop 0\n"
  "v_mfma_f32_16x16x32_bf16 v[80:83], a[4:7], a[48:51], v[80:83]\n"
  "ds_read_b64_tr_b16 a[64:65], v12 offset:24576\n"
  "v_mfma_f32_16x16x32_bf16 v[84:87], a[4:7], a[52:55], v[84:87]\n"
  "ds_read_b64_tr_b16 a[66:67], v12 offset:25088\n"
  "s_add_u32 s70, s70, 65536\n"
  "v_mfma_f32_16x16x32_bf16 v[88:91], a[4:7], a[56:59], v[88:91]\n"
  "ds_read_b64_tr_b16 a[68:69], v14 offset:24576\n"
  "v_mfma_f32_16x16x32_bf16 v[92:95], a[4:7], a[60:63], v[92:95]\n"
  "ds_read_b64_tr_b16 a[70:71], v14 offset:25088\n"
  "s_add_u32 m0, s71, 147456\n"
  "v_mfma_f32_16x16x32_bf16 v[96:99], a[8:11], a[48:51], v[96:99]\n"
  "ds_read_b64_tr_b16 a[72:73], v12 offset:28672\n"
  "v_mfma_f32_16x16x32_bf16 v[100:103], a[8:11], a[52:55], v[100:103]\n"
  "ds_read_b64_tr_b16 a[74:75], v12 offset:29184\n"
  "buffer_load_dwordx4 v18, s[36:39], s70 offen sc1 lds\n"
  "v_mfma_f32_16x16x32_bf16 v[104:107], a[8:11], a[56:59], v[104:107]\n"
  "ds_read_b64_tr_b16 a[76:77], v14 offset:28672\n"
  "buffer_load_dwordx4 v18, s[36:39], s70 offen offset:1024 sc1 lds\n"
  "v_mfma_f32_16x16x32_bf16 v[108:111], a[8:11], a[60:63], v[108:111]\n"
  "ds_read_b64_tr_b16 a[78:79], v14 offset:29184\n"
  "v_mfma_f32_16x16x32_bf16 v[112:115], a[12:15], a[48:51], v[112:115]\n"
  "ds_read_b64_tr_b16 a[80:81], v12 offset:32768\n"
  "buffer_load_dwordx4 v18, s[36:39], s70 offen offset:2048 sc1 lds\n"
  "v_mfma_f32_16x16x32_bf16 v[116:119], a[12:15], a[52:55], v[116:119]\n"
  "ds_read_b64_tr_b16 a[82:83], v12 offset:33280\n"
  "v_mfma_f32_16x16x32_bf16 v[120:123], a[12:15], a[56:59], v[120:123]\n"
  "ds_read_b64_tr_b16 a[84:85], v14 offset:32768\n"
  "buffer_load_dwordx4 v18, s[36:39], s70 offen offset:3072 sc1 lds\n"
  "v_mfma_f32_16x16x32_bf16 v[124:127], a[12:15], a[60:63], v[124:127]\n"
  "ds_read_b64_tr_b16 a[86:87], v14 offset:33280\n"
  "v_mfma_f32_16x16x32_bf16 v[128:131], a[16:19], a[48:51], v[128:131]\n"
  "ds_read_b64_tr_b16 a[88:89], v12 offset:36864\n"
  "v_and_b32_e64 v32, 15, v0\n"
  "v_mfma_f32_16x16x32_bf16 v[132:135], a[16:19], a[52:55], v[132:135]\n"
  "ds_read_b64_tr_b16 a[90:91], v12 offset:37376\n"
  "v_mfma_f32_16x16x32_bf16 v[136:139], a[16:19], a[56:59], v[136:139]\n"
  "ds_read_b64_tr_b16 a[92:93], v14 offset:36864\n"
  "v_lshlrev_b32_e64 v32, 3, v32\n"
  "v_mfma_f32_16x16x32_bf16 v[140:143], a[16:19], a[60:63], v[140:143]\n"
  "ds_read_b64_tr_b16 a[94:95], v14 offset:37376\n"
  "s_lshl_b32 s70, s47, 10\n"
  "s_nop 0\n"
  "v_mfma_f32_16x16x32_bf16 v[144:147], a[20:23], a[48:51], v[144:147]\n"
  "ds_read_b64_tr_b16 a[96:97], v12 offset:40960\n"
  "v_mfma_f32_16x16x32_bf16 v[148:151], a[20:23], a[52:55], v[148:151]\n"
  "ds_read_b64_tr_b16 a[98:99], v12 offset:41472\n"
  "s_lshr_b32 s71, s46, 1\n"
  "s_nop 0\n"
  "v_mfma_f32_16x16x32_bf16 v[152:155], a[20:23], a[56:59], v[152:155]\n"
  "ds_read_b64_tr_b16 a[100:101], v14 offset:40960\n"
  "v_mfma_f32_16x16x32_bf16 v[156:159], a[20:23], a[60:63], v[156:159]\n"
  "ds_read_b64_tr_b16 a[102:103], v14 offset:41472\n"
  "s_lshl_b32 s71, s71, 9\n"
  "s_nop 0\n"
  "v_mfma_f32_16x16x32_bf16 v[160:163], a[24:27], a[48:51], v[160:163]\n"
  "ds_read_b64_tr_b16 a[104:105], v12 offset:45056\n"
  "v_mfma_f32_16x16x32_bf16 v[164:167], a[24:27], a[52:55], v[164:167]\n"
  "ds_read_b64_tr_b16 a[106:107], v12 offset:45568\n"
  "s_add_u32 s70, s70, s71\n"
  "s_nop 0\n"
  "v_mfma_f32_16x16x32_bf16 v[168:171], a[24:27], a[56:59], v[168:171]\n"
  "ds_read_b64_tr_b16 a[108:109], v14 offset:45056\n"
  "v_mfma_f32_16x16x32_bf16 v[172:175], a[24:27], a[60:63], v[172:175]\n"
  "ds_read_b64_tr_b16 a[110:111], v14 offset:45568\n"
  "s_add_u32 s70, s70, 32768\n"
  "v_mfma_f32_16x16x32_bf16 v[176:179], a[28:31], a[48:51], v[176:179]\n"
  "buffer_load_dwordx2 v[24:25], v32, s[36:39], s70 offen sc1\n"
  "v_mfma_f32_16x16x32_bf16 v[180:183], a[28:31], a[52:55], v[180:183]\n"
  "v_mfma_f32_16x16x32_bf16 v[184:187], a[28:31], a[56:59], v[184:187]\n"
  "buffer_load_dwordx2 v[26:27], v32, s[36:39], s70 offen offset:128 sc1\n"
  "v_mfma_f32_16x16x32_bf16 v[188:191], a[28:31], a[60:63], v[188:191]\n"
  "v_mfma_f32_16x16x32_bf16 v[192:195], a[32:35], a[48:51], v[192:195]\n"
  "buffer_load_dwordx2 v[28:29], v32, s[36:39], s70 offen offset:256 sc1\n"
  "v_mfma_f32_16x16x32_bf16 v[196:199], a[32:35], a[52:55], v[196:199]\n"
  "v_mfma_f32_16x16x32_bf16 v[200:203], a[32:35], a[56:59], v[200:203]\n"
  "buffer_load_dwordx2 v[30:31], v32, s[36:39], s70 offen offset:384 sc1\n"
  "v_mfma_f32_16x16x32_bf16 v[204:207], a[32:35], a[60:63], v[204:207]\n"
  "v_mfma_f32_16x16x32_bf16 v[208:211], a[36:39], a[48:51], v[208:211]\n"
  "s_add_u32 s72, s80, 0\n"
  "s_nop 0\n"
  "v_mfma_f32_16x16x32_bf16 v[212:215], a[36:39], a[52:55], v[212:215]\n"
  "buffer_load_dwordx4 a[128:131], v18, s[36:39], s72 offen sc1\n"
  "v_mfma_f32_16x16x32_bf16 v[216:219], a[36:39], a[56:59], v[216:219]\n"
  "v_mfma_f32_16x16x32_bf16 v[220:223], a[36:39], a[60:63], v[220:223]\n"
  "buffer_load_dwordx4 a[132:135], v18, s[36:39], s72 offen offset:1024 sc1\n"
  "v_mfma_f32_16x16x32_bf16 v[224:227], a[40:43], a[48:51], v[224:227]\n"
  "v_mfma_f32_16x16x32_bf16 v[228:231], a[40:43], a[52:55], v[228:231]\n"
  "buffer_load_dwordx4 a[136:139], v18, s[36:39], s72 offen offset:2048 sc1\n"
  "v_mfma_f32_16x16x32_bf16 v[232:235], a[40:43], a[56:59], v[232:235]\n"
  "v_mfma_f32_16x16x32_bf16 v[236:239], a[40:43], a[60:63], v[236:239]\n"
  "buffer_load_dwordx4 a[140:143], v18, s[36:39], s72 offen offset:3072 sc1\n"
  "v_mfma_f32_16x16x32_bf16 v[240:243], a[44:47], a[48:51], v[240:243]\n"
  "v_mfma_f32_16x16x32_bf16 v[244:247], a[44:47], a[52:55], v[244:247]\n"
  "buffer_load_dwordx4 a[144:147], v19, s[36:39], s72 offen sc1\n"
  "v_mfma_f32_16x16x32_bf16 v[248:251], a[44:47], a[56:59], v[248:251]\n"
  "v_mfma_f32_16x16x32_bf16 v[252:255], a[44:47], a[60:63], v[252:255]\n"
  "s_waitcnt lgkmcnt(0)\n"
  "s_barrier\n"
  "v_mfma_f32_16x16x32_bf16 v[64:67], a[64:67], a[112:115], v[64:67]\n"
  "ds_read_b128 a[48:51], v16 offset:0\n"
  "v_min_u32 v57, 95, v1\n"
  "v_mfma_f32_16x16x32_bf16 v[68:71], a[64:67], a[116:119], v[68:71]\n"
  "ds_read_b128 a[52:55], v16 offset:2048\n"
  "v_mfma_f32_16x16x32_bf16 v[72:75], a[64:67], a[120:123], v[72:75]\n"
  "ds_read_b128 a[56:59], v16 offset:4096\n"
  "v_mfma_f32_16x16x32_bf16 v[76:79], a[64:67], a[124:127], v[76:79]\n"
  "ds_read_b128 a[60:63], v16 offset:6144\n"
  "v_lshlrev_b32_e64 v57, 4, v57\n"
  "v_mfma_f32_16x16x32_bf16 v[80:83], a[68:71], a[112:115], v[80:83]\n"
  "ds_read_b64_tr_b16 a[0:1], v13 offset:0\n"
  "v_mfma_f32_16x16x32_bf16 v[84:87], a[68:71], a[116:119], v[84:87]\n"
  "ds_read_b64_tr_b16 a[2:3], v13 offset:512\n"
  "v_mfma_f32_16x16x32_bf16 v[88:91], a[68:71], a[120:123], v[88:91]\n"
  "ds_read_b64_tr_b16 a[4:5], v15 offset:0\n"
  "v_add_u32 v56, 147456, v57\n"
  "v_mfma_f32_16x16x32_bf16 v[92:95], a[68:71], a[124:127], v[92:95]\n"
  "ds_read_b64_tr_b16 a[6:7], v15 offset:512\n"
  "v_mfma_f32_16x16x32_bf16 v[96:99], a[72:75], a[112:115], v[96:99]\n"
  "ds_read_b64_tr_b16 a[8:9], v13 offset:4096\n"
  "v_mfma_f32_16x16x32_bf16 v[100:103], a[72:75], a[116:119], v[100:103]\n"
  "ds_read_b64_tr_b16 a[10:11], v13 offset:4608\n"
  "v_add_u32 v57, 131072, v57\n"
  "v_mfma_f32_16x16x32_bf16 v[104:107], a[72:75], a[120:123], v[104:107]\n"
  "ds_read_b64_tr_b16 a[12:13], v15 offset:4096\n"
  "v_mfma_f32_16x16x32_bf16 v[108:111], a[72:75], a[124:127], v[108:111]\n"
  "ds_read_b64_tr_b16 a[14:15], v15 offset:4608\n"
  "v_mfma_f32_16x16x32_bf16 v[112:115], a[76:79], a[112:115], v[112:115]\n"
  "ds_read_b64_tr_b16 a[16:17], v13 offset:8192\n"
  "s_add_u32 s72, s80, 5120\n"
  "v_mfma_f32_16x16x32_bf16 v[116:119], a[76:79], a[116:119], v[116:119]\n"
  "ds_read_b64_tr_b16 a[18:19], v13 offset:8704\n"
  "v_mfma_f32_16x16x32_bf16 v[120:123], a[76:79], a[120:123], v[120:123]\n"
  "ds_read_b64_tr_b16 a[20:21], v15 offset:8192\n"
  "v_mfma_f32_16x16x32_bf16 v[124:127], a[76:79], a[124:127], v[124:127]\n"
  "ds_read_b64_tr_b16 a[22:23], v15 offset:8704\n"
  "buffer_load_dwordx4 a[148:151], v18, s[36:39], s72 offen sc1\n"
  "v_mfma_f32_16x16x32_bf16 v[128:131], a[80:83], a[112:115], v[128:131]\n"
  "ds_read_b64_tr_b16 a[24:25], v13 offset:12288\n"
  "v_mfma_f32_16x16x32_bf16 v[132:135], a[80:83], a[116:119], v[132:135]\n"
  "ds_read_b64_tr_b16 a[26:27], v13 offset:12800\n"
  "v_mfma_f32_16x16x32_bf16 v[136:139], a[80:83], a[120:123], v[136:139]\n"
  "ds_read_b64_tr_b16 a[28:29], v15 offset:12288\n"
  "buffer_load_dwordx4 a[152:155], v18, s[36:39], s72 offen offset:1024 sc1\n"
  "v_mfma_f32_16x16x32_bf16 v[140:143], a[80:83], a[124:127], v[140:143]\n"
  "ds_read_b64_tr_b16 a[30:31], v15 offset:12800\n"
  "v_mfma_f32_16x16x32_bf16 v[144:147], a[84:87], a[112:115], v[144:147]\n"
  "ds_read_b64_tr_b16 a[32:33], v13 offset:16384\n"
  "v_mfma_f32_16x16x32_bf16 v[148:151], a[84:87], a[116:119], v[148:151]\n"
  "ds_read_b64_tr_b16 a[34:35], v13 offset:16896\n"
  "buffer_load_dwordx4 a[156:159], v18, s[36:39], s72 offen offset:2048 sc1\n"
  "v_mfma_f32_16x16x32_bf16 v[152:155], a[84:87], a[120:123], v[152:155]\n"
  "ds_read_b64_tr_b16 a[36:37], v15 offset:16384\n"
  "v_mfma_f32_16x16x32_bf16 v[156:159], a[84:87], a[124:127], v[156:159]\n"
  "ds_read_b64_tr_b16 a[38:39], v15 offset:16896\n"
  "v_mfma_f32_16x16x32_bf16 v[160:163], a[88:91], a[112:115], v[160:163]\n"
  "ds_read_b64_tr_b16 a[40:41], v13 offset:20480\n"
  "v_mfma_f32_16x16x32_bf16 v[164:167], a[88:91], a[116:119], v[164:167]\n"
  "ds_read_b64_tr_b16 a[42:43], v13 offset:20992\n"
  "buffer_load_dwordx4 a[160:163], v18, s[36:39], s72 offen offset:3072 sc1\n"
  "v_mfma_f32_16x16x32_bf16 v[168:171], a[88:91], a[120:123], v[168:171]\n"
  "ds_read_b64_tr_b16 a[44:45], v15 offset:20480\n"
  "v_mfma_f32_16x16x32_bf16 v[172:175], a[88:91], a[124:127], v[172:175]\n"
  "ds_read_b64_tr_b16 a[46:47], v15 offset:20992\n"
  "v_mfma_f32_16x16x32_bf16 v[176:179], a[92:95], a[112:115], v[176:179]\n"
  "buffer_load_dwordx4 a[164:167], v19, s[36:39], s72 offen sc1\n"
  "v_mfma_f32_16x16x32_bf16 v[180:183], a[92:95], a[116:119], v[180:183]\n"
  "v_mfma_f32_16x16x32_bf16 v[184:187], a[92:95], a[120:123], v[184:187]\n"
  "v_mfma_f32_16x16x32_bf16 v[188:191], a[92:95], a[124:127], v[188:191]\n"
  "s_add_u32 s72, s80, 10240\n"
  "v_mfma_f32_16x16x32_bf16 v[192:195], a[96:99], a[112:115], v[192:195]\n"
  "v_mfma_f32_16x16x32_bf16 v[196:199], a[96:99], a[116:119], v[196:199]\n"
  "v_mfma_f32_16x16x32_bf16 v[200:203], a[96:99], a[120:123], v[200:203]\n"
  "buffer_load_dwordx4 a[168:171], v18, s[36:39], s72 offen sc1\n"
  "v_mfma_f32_16x16x32_bf16 v[204:207], a[96:99], a[124:127], v[204:207]\n"
  "v_mfma_f32_16x16x32_bf16 v[208:211], a[100:103], a[112:115], v[208:211]\n"
  "v_mfma_f32_16x16x32_bf16 v[212:215], a[100:103], a[116:119], v[212:215]\n"
  "buffer_load_dwordx4 a[172:175], v18, s[36:39], s72 offen offset:1024 sc1\n"
  "v_mfma_f32_16x16x32_bf16 v[216:219], a[100:103], a[120:123], v[216:219]\n"
  "v_mfma_f32_16x16x32_bf16 v[220:223], a[100:103], a[124:127], v[220:223]\n"
  "v_mfma_f32_16x16x32_bf16 v[224:227], a[104:107], a[112:115], v[224:227]\n"
  "buffer_load_dwordx4 a[176:179], v18, s[36:39], s72 offen offset:2048 sc1\n"
  "v_mfma_f32_16x16x32_bf16 v[228:231], a[104:107], a[116:119], v[228:231]\n"
  "v_mfma_f32_16x16x32_bf16 v[232:235], a[104:107], a[120:123], v[232:235]\n"
  "v_mfma_f32_16x16x32_bf16 v[236:239], a[104:107], a[124:127], v[236:239]\n"
  "buffer_load_dwordx4 a[180:183], v18, s[36:39], s72 offen offset:3072 sc1\n"
  "v_mfma_f32_16x16x32_bf16 v[240:243], a[108:111], a[112:115], v[240:243]\n"
  "v_mfma_f32_16x16x32_bf16 v[244:247], a[108:111], a[116:119], v[244:247]\n"
  "v_mfma_f32_16x16x32_bf16 v[248:251], a[108:111], a[120:123], v[248:251]\n"
  "v_mfma_f32_16x16x32_bf16 v[252:255], a[108:111], a[124:127], v[252:255]\n"
  "s_waitcnt vmcnt(18) lgkmcnt(0)\n"
  "s_barrier\n"
  "v_mfma_f32_16x16x32_bf16 v[64:67], a[0:3], a[48:51], v[64:67]\n"
  "ds_read_b128 a[112:115], v17 offset:0\n"
  "ds_read_b128 v[32:35], v56\n"
  "v_mfma_f32_16x16x32_bf16 v[68:71], a[0:3], a[52:55], v[68:71]\n"
  "ds_read_b128 a[116:119], v17 offset:2048\n"
  "ds_read_b128 v[36:39], v56 offset:1536\n"
  "v_mfma_f32_16x16x32_bf16 v[72:75], a[0:3], a[56:59], v[72:75]\n"
  "ds_read_b128 a[120:123], v17 offset:4096\n"
  "ds_read_b128 v[40:43], v56 offset:3072\n"
  "v_mfma_f32_16x16x32_bf16 v[76:79], a[0:3], a[60:63], v[76:79]\n"
  "ds_read_b128 a[124:127], v17 offset:6144\n"
  "ds_read_b128 v[44:47], v56 offset:4608\n"
  "v_mfma_f32_16x16x32_bf16 v[80:83], a[4:7], a[48:51], v[80:83]\n"
  "ds_read_b64_tr_b16 a[64:65], v13 offset:24576\n"
  "s_waitcnt lgkmcnt(0)\n"
  "s_nop 0\n"
  "v_mfma_f32_16x16x32_bf16 v[84:87], a[4:7], a[52:55], v[84:87]\n"
  "ds_read_b64_tr_b16 a[66:67], v13 offset:25088\n"
  "v_pk_add_f32 v[32:33], v[32:33], v[36:37]\n"
  "v_mfma_f32_16x16x32_bf16 v[88:91], a[4:7], a[56:59], v[88:91]\n"
  "ds_read_b64_tr_b16 a[68:69], v15 offset:24576\n"
  "v_pk_add_f32 v[34:35], v[34:35], v[38:39]\n"
  "v_mfma_f32_16x16x32_bf16 v[92:95], a[4:7], a[60:63], v[92:95]\n"
  "ds_read_b64_tr_b16 a[70:71], v15 offset:25088\n"
  "v_pk_add_f32 v[32:33], v[32:33], v[40:41]\n"
  "v_mfma_f32_16x16x32_bf16 v[96:99], a[8:11], a[48:51], v[96:99]\n"
  "ds_read_b64_tr_b16 a[72:73], v13 offset:28672\n"
  "v_pk_add_f32 v[34:35], v[34:35], v[42:43]\n"
  "v_mfma_f32_16x16x32_bf16 v[100:103], a[8:11], a[52:55], v[100:103]\n"
  "ds_read_b64_tr_b16 a[74:75], v13 offset:29184\n"
  "v_pk_add_f32 v[32:33], v[32:33], v[44:45]\n"
  "v_mfma_f32_16x16x32_bf16 v[104:107], a[8:11], a[56:59], v[104:107]\n"
  "ds_read_b64_tr_b16 a[76:77], v15 offset:28672\n"
  "v_pk_add_f32 v[34:35], v[34:35], v[46:47]\n"
  "v_mfma_f32_16x16x32_bf16 v[108:111], a[8:11], a[60:63], v[108:111]\n"
  "ds_read_b64_tr_b16 a[78:79], v15 offset:29184\n"
  "v_mfma_f32_16x16x32_bf16 v[112:115], a[12:15], a[48:51], v[112:115]\n"
  "ds_read_b64_tr_b16 a[80:81], v13 offset:32768\n"
  "ds_read_b128 v[36:39], v56 offset:6144\n"
  "v_mfma_f32_16x16x32_bf16 v[116:119], a[12:15], a[52:55], v[116:119]\n"
  "ds_read_b64_tr_b16 a[82:83], v13 offset:33280\n"
  "ds_read_b128 v[40:43], v56 offset:7680\n"
  "v_mfma_f32_16x16x32_bf16 v[120:123], a[12:15], a[56:59], v[120:123]\n"
  "ds_read_b64_tr_b16 a[84:85], v15 offset:32768\n"
  "ds_read_b128 v[44:47], v56 offset:9216\n"
  "v_mfma_f32_16x16x32_bf16 v[124:127], a[12:15], a[60:63], v[124:127]\n"
  "ds_read_b64_tr_b16 a[86:87], v15 offset:33280\n"
  "s_waitcnt lgkmcnt(0)\n"
  "s_nop 0\n"
  "v_mfma_f32_16x16x32_bf16 v[128:131], a[16:19], a[48:51], v[128:131]\n"
  "ds_read_b64_tr_b16 a[88:89], v13 offset:36864\n"
  "v_pk_add_f32 v[32:33], v[32:33], v[36:37]\n"
  "v_mfma_f32_16x16x32_bf16 v[132:135], a[16:19], a[52:55], v[132:135]\n"
  "ds_read_b64_tr_b16 a[90:91], v13 offset:37376\n"
  "v_pk_add_f32 v[34:35], v[34:35], v[38:39]\n"
  "v_mfma_f32_16x16x32_bf16 v[136:139], a[16:19], a[56:59], v[136:139]\n"
  "ds_read_b64_tr_b16 a[92:93], v15 offset:36864\n"
  "v_pk_add_f32 v[32:33], v[32:33], v[40:41]\n"
  "v_mfma_f32_16x16x32_bf16 v[140:143], a[16:19], a[60:63], v[140:143]\n"
  "ds_read_b64_tr_b16 a[94:95], v15 offset:37376\n"
  "v_pk_add_f32 v[34:35], v[34:35], v[42:43]\n"
  "v_mfma_f32_16x16x32_bf16 v[144:147], a[20:23], a[48:51], v[144:147]\n"
  "ds_read_b64_tr_b16 a[96:97], v13 offset:40960\n"
  "v_pk_add_f32 v[32:33], v[32:33], v[44:45]\n"
  "v_mfma_f32_16x16x32_bf16 v[148:151], a[20:23], a[52:55], v[148:151]\n"
  "ds_read_b64_tr_b16 a[98:99], v13 offset:41472\n"
  "v_pk_add_f32 v[34:35], v[34:35], v[46:47]\n"
  "v_mfma_f32_16x16x32_bf16 v[152:155], a[20:23], a[56:59], v[152:155]\n"
  "ds_read_b64_tr_b16 a[100:101], v15 offset:40960\n"
  "ds_read_b128 v[36:39], v56 offset:10752\n"
  "v_mfma_f32_16x16x32_bf16 v[156:159], a[20:23], a[60:63], v[156:159]\n"
  "ds_read_b64_tr_b16 a[102:103], v15 offset:41472\n"
  "v_mfma_f32_16x16x32_bf16 v[160:163], a[24:27], a[48:51], v[160:163]\n"
  "ds_read_b64_tr_b16 a[104:105], v13 offset:45056\n"
  "ds_read_b128 v[40:43], v56 offset:12288\n"
  "v_mfma_f32_16x16x32_bf16 v[164:167], a[24:27], a[52:55], v[164:167]\n"
  "ds_read_b64_tr_b16 a[106:107], v13 offset:45568\n"
  "ds_read_b128 v[44:47], v56 offset:13824\n"
  "v_mfma_f32_16x16x32_bf16 v[168:171], a[24:27], a[56:59], v[168:171]\n"
  "ds_read_b64_tr_b16 a[108:109], v15 offset:45056\n"
  "s_waitcnt lgkmcnt(0)\n"
  "s_nop 0\n"
  "v_mfma_f32_16x16x32_bf16 v[172:175], a[24:27], a[60:63], v[172:175]\n"
  "ds_read_b64_tr_b16 a[110:111], v15 offset:45568\n"
  "v_pk_add_f32 v[32:33], v[32:33], v[36:37]\n"
  "v_mfma_f32_16x16x32_bf16 v[176:179], a[28:31], a[48:51], v[176:179]\n"
  "v_pk_add_f32 v[34:35], v[34:35], v[38:39]\n"
  "v_mfma_f32_16x16x32_bf16 v[180:183], a[28:31], a[52:55], v[180:183]\n"
  "v_pk_add_f32 v[32:33], v[32:33], v[40:41]\n"
  "v_mfma_f32_16x16x32_bf16 v[184:187], a[28:31], a[56:59], v[184:187]\n"
  "v_pk_add_f32 v[34:35], v[34:35], v[42:43]\n"
  "v_mfma_f32_16x16x32_bf16 v[188:191], a[28:31], a[60:63], v[188:191]\n"
  "v_pk_add_f32 v[32:33], v[32:33], v[44:45]\n"
  "v_mfma_f32_16x16x32_bf16 v[192:195], a[32:35], a[48:51], v[192:195]\n"
  "v_pk_add_f32 v[34:35], v[34:35], v[46:47]\n"
  "v_mfma_f32_16x16x32_bf16 v[196:199], a[32:35], a[52:55], v[196:199]\n"
  "ds_write_b128 v57, v[32:35]\n"
  "v_mfma_f32_16x16x32_bf16 v[200:203], a[32:35], a[56:59], v[200:203]\n"
  "buffer_load_dwordx4 a[184:187], v19, s[36:39], s72 offen sc1\n"
  "v_mfma_f32_16x16x32_bf16 v[204:207], a[32:35], a[60:63], v[204:207]\n"
  "v_mfma_f32_16x16x32_bf16 v[208:211], a[36:39], a[48:51], v[208:211]\n"
  "s_add_u32 s72, s80, 15360\n"
  "v_mfma_f32_16x16x32_bf16 v[212:215], a[36:39], a[52:55], v[212:215]\n"
  "buffer_load_dwordx4 a[188:191], v18, s[36:39], s72 offen sc1\n"
  "v_mfma_f32_16x16x32_bf16 v[216:219], a[36:39], a[56:59], v[216:219]\n"
  "buffer_load_dwordx4 a[192:195], v18, s[36:39], s72 offen offset:1024 sc1\n"
  "v_mfma_f32_16x16x32_bf16 v[220:223], a[36:39], a[60:63], v[220:223]\n"
  "buffer_load_dwordx4 a[196:199], v18, s[36:39], s72 offen offset:2048 sc1\n"
  "v_mfma_f32_16x16x32_bf16 v[224:227], a[40:43], a[48:51], v[224:227]\n"
  "buffer_load_dwordx4 a[200:203], v18, s[36:39], s72 offen offset:3072 sc1\n"
  "v_mfma_f32_16x16x32_bf16 v[228:231], a[40:43], a[52:55], v[228:231]\n"
  "buffer_load_dwordx4 a[204:207], v19, s[36:39], s72 offen sc1\n"
  "v_mfma_f32_16x16x32_bf16 v[232:235], a[40:43], a[56:59], v[232:235]\n"
  "s_add_u32 s72, s80, 20480\n"
  "v_mfma_f32_16x16x32_bf16 v[236:239], a[40:43], a[60:63], v[236:239]\n"
  "buffer_load_dwordx4 a[208:211], v18, s[36:39], s72 offen sc1\n"
  "v_mfma_f32_16x16x32_bf16 v[240:243], a[44:47], a[48:51], v[240:243]\n"
  "buffer_load_dwordx4 a[212:215], v18, s[36:39], s72 offen offset:1024 sc1\n"
  "v_mfma_f32_16x16x32_bf16 v[244:247], a[44:47], a[52:55], v[244:247]\n"
  "buffer_load_dwordx4 a[216:219], v18, s[36:39], s72 offen offset:2048 sc1\n"
  "v_mfma_f32_16x16x32_bf16 v[248:251], a[44:47], a[56:59], v[248:251]\n"
  "v_mfma_f32_16x16x32_bf16 v[252:255], a[44:47], a[60:63], v[252:255]\n"
  "s_waitcnt lgkmcnt(0)\n"
  "s_barrier\n"
  "v_mfma_f32_16x16x32_bf16 v[64:67], a[64:67], a[112:115], v[64:67]\n"
  "buffer_load_dwordx4 a[220:223], v18, s[36:39], s72 offen offset:3072 sc1\n"
  "v_mfma_f32_16x16x32_bf16 v[68:71], a[64:67], a[116:119], v[68:71]\n"
  "v_mfma_f32_16x16x32_bf16 v[72:75], a[64:67], a[120:123], v[72:75]\n"
  "v_mfma_f32_16x16x32_bf16 v[76:79], a[64:67], a[124:127], v[76:79]\n"
  "buffer_load_dwordx4 a[224:227], v19, s[36:39], s72 offen sc1\n"
  "v_mfma_f32_16x16x32_bf16 v[80:83], a[68:71], a[112:115], v[80:83]\n"
  "v_mfma_f32_16x16x32_bf16 v[84:87], a[68:71], a[116:119], v[84:87]\n"
  "v_mfma_f32_16x16x32_bf16 v[88:91], a[68:71], a[120:123], v[88:91]\n"
  "s_mul_i32 s72, s81, 5\n"
  "s_nop 0\n"
  "v_mfma_f32_16x16x32_bf16 v[92:95], a[68:71], a[124:127], v[92:95]\n"
  "v_mfma_f32_16x16x32_bf16 v[96:99], a[72:75], a[112:115], v[96:99]\n"
  "v_mfma_f32_16x16x32_bf16 v[100:103], a[72:75], a[116:119], v[100:103]\n"
  "s_add_u32 s72, s72, 25600\n"
  "v_mfma_f32_16x16x32_bf16 v[104:107], a[72:75], a[120:123], v[104:107]\n"
  "v_mfma_f32_16x16x32_bf16 v[108:111], a[72:75], a[124:127], v[108:111]\n"
  "v_mfma_f32_16x16x32_bf16 v[112:115], a[76:79], a[112:115], v[112:115]\n"
  "s_add_u32 s72, s72, s80\n"
  "s_nop 0\n"
  "v_mfma_f32_16x16x32_bf16 v[116:119], a[76:79], a[116:119], v[116:119]\n"
  "v_mfma_f32_16x16x32_bf16 v[120:123], a[76:79], a[120:123], v[120:123]\n"
  "v_mfma_f32_16x16x32_bf16 v[124:127], a[76:79], a[124:127], v[124:127]\n"
  "buffer_load_dwordx4 a[228:231], v18, s[36:39], s72 offen sc1\n"
  "v_mfma_f32_16x16x32_bf16 v[128:131], a[80:83], a[112:115], v[128:131]\n"
  "v_mfma_f32_16x16x32_bf16 v[132:135], a[80:83], a[116:119], v[132:135]\n"
  "v_mfma_f32_16x16x32_bf16 v[136:139], a[80:83], a[120:123], v[136:139]\n"
  "buffer_load_dwordx4 a[232:235], v18, s[36:39], s72 offen offset:1024 sc1\n"
  "v_mfma_f32_16x16x32_bf16 v[140:143], a[80:83], a[124:127], v[140:143]\n"
  "v_mfma_f32_16x16x32_bf16 v[144:147], a[84:87], a[112:115], v[144:147]\n"
  "v_mfma_f32_16x16x32_bf16 v[148:151], a[84:87], a[116:119], v[148:151]\n"
  "buffer_load_dwordx4 a[236:239], v18, s[36:39], s72 offen offset:2048 sc1\n"
  "v_mfma_f32_16x16x32_bf16 v[152:155], a[84:87], a[120:123], v[152:155]\n"
  "v_mfma_f32_16x16x32_bf16 v[156:159], a[84:87], a[124:127], v[156:159]\n"
  "v_mfma_f32_16x16x32_bf16 v[160:163], a[88:91], a[112:115], v[160:163]\n"
  "v_mfma_f32_16x16x32_bf16 v[164:167], a[88:91], a[116:119], v[164:167]\n"
  "buffer_load_dwordx4 a[240:243], v18, s[36:39], s72 offen offset:3072 sc1\n"
  "v_mfma_f32_16x16x32_bf16 v[168:171], a[88:91], a[120:123], v[168:171]\n"
  "v_mfma_f32_16x16x32_bf16 v[172:175], a[88:91], a[124:127], v[172:175]\n"
  "v_mfma_f32_16x16x32_bf16 v[176:179], a[92:95], a[112:115], v[176:179]\n"
  "buffer_load_dwordx4 a[244:247], v19, s[36:39], s72 offen sc1\n"
  "v_mfma_f32_16x16x32_bf16 v[180:183], a[92:95], a[116:119], v[180:183]\n"
  "v_mfma_f32_16x16x32_bf16 v[184:187], a[92:95], a[120:123], v[184:187]\n"
  "v_mfma_f32_16x16x32_bf16 v[188:191], a[92:95], a[124:127], v[188:191]\n"
  "s_mul_i32 s72, s81, 5\n"
  "s_nop 0\n"
  "v_mfma_f32_16x16x32_bf16 v[192:195], a[96:99], a[112:115], v[192:195]\n"
  "v_mfma_f32_16x16x32_bf16 v[196:199], a[96:99], a[116:119], v[196:199]\n"
  "v_mfma_f32_16x16x32_bf16 v[200:203], a[96:99], a[120:123], v[200:203]\n"
  "s_add_u32 s72, s72, 30720\n"
  "v_mfma_f32_16x16x32_bf16 v[204:207], a[96:99], a[124:127], v[204:207]\n"
  "v_mfma_f32_16x16x32_bf16 v[208:211], a[100:103], a[112:115], v[208:211]\n"
  "v_mfma_f32_16x16x32_bf16 v[212:215], a[100:103], a[116:119], v[212:215]\n"
  "s_add_u32 s72, s72, s80\n"
  "s_nop 0\n"
  "v_mfma_f32_16x16x32_bf16 v[216:219], a[100:103], a[120:123], v[216:219]\n"
  "v_mfma_f32_16x16x32_bf16 v[220:223], a[100:103], a[124:127], v[220:223]\n"
  "v_mfma_f32_16x16x32_bf16 v[224:227], a[104:107], a[112:115], v[224:227]\n"
  "buffer_load_dwordx4 a[248:251], v18, s[36:39], s72 offen sc1\n"
  "v_mfma_f32_16x16x32_bf16 v[228:231], a[104:107], a[116:119], v[228:231]\n"
  "v_mfma_f32_16x16x32_bf16 v[232:235], a[104:107], a[120:123], v[232:235]\n"
  "v_mfma_f32_16x16x32_bf16 v[236:239], a[104:107], a[124:127], v[236:239]\n"
  "buffer_load_dwordx4 a[252:255], v18, s[36:39], s72 offen offset:1024 sc1\n"
  "v_mfma_f32_16x16x32_bf16 v[240:243], a[108:111], a[112:115], v[240:243]\n"
  "v_mfma_f32_16x16x32_bf16 v[244:247], a[108:111], a[116:119], v[244:247]\n"
  "v_mfma_f32_16x16x32_bf16 v[248:251], a[108:111], a[120:123], v[248:251]\n"
  "v_mfma_f32_16x16x32_bf16 v[252:255], a[108:111], a[124:127], v[252:255]\n"
  "s_waitcnt lgkmcnt(0)\n"
  "s_barrier\n"
  "s_mul_i32 s65, s46, 21504\n"
  "s_waitcnt vmcnt(32)\n"
  "v_mul_f32 v41, v25, v25\n"
  "v_mul_f32 v32, 0xc0135761, v25\n"
  "v_mul_f32 v33, v41, v25\n"
  "v_rcp_f32 v40, v41\n"
  "v_mul_f32 v33, 0xbdd2d3e8, v33\n"
  "v_mul_f32_e64 v43, v27, v27\n"
  "v_mul_f32 v34, 0xc0135761, v27\n"
  "v_mul_f32 v35, v43, v27\n"
  "v_rcp_f32 v42, v43\n"
  "v_mul_f32 v35, 0xbdd2d3e8, v35\n"
  "v_mul_f32_e64 v45, v29, v29\n"
  "v_mul_f32 v36, 0xc0135761, v29\n"
  "v_mul_f32 v37, v45, v29\n"
  "v_rcp_f32 v44, v45\n"
  "v_mul_f32 v37, 0xbdd2d3e8, v37\n"
  "v_mul_f32_e64 v47, v31, v31\n"
  "v_mul_f32 v38, 0xc0135761, v31\n"
  "v_mul_f32 v39, v47, v31\n"
  "v_rcp_f32 v46, v47\n"
  "v_mul_f32 v39, 0xbdd2d3e8, v39\n"
  "s_and_b32 s66, s46, 1\n"
  "s_nop 0\n"
  "s_mul_i32 s66, s66, 768\n"
  "s_add_u32 s66, s66, 131072\n"
  "v_lshrrev_b32_e64 v20, 4, v0\n"
  "v_lshl_add_u32 v20, v20, 4, s66\n"
  "s_lshl_b32 s66, s47, 1\n"
  "s_lshr_b32 s67, s46, 1\n"
  "s_add_u32 s66, s66, s67\n"
  "s_nop 0\n"
  "s_mul_i32 s66, s66, 192\n"
  "s_mul_i32 s67, s48, 6\n"
  "s_add_u32 s66, s66, s67\n"
  "s_and_b32 s67, s46, 1\n"
  "s_add_u32 s66, s66, s67\n"
  "s_lshl_b32 s66, s66, 10\n"
  "s_nop 0\n"
  "s_add_u32 s70, s66, 4194304\n"
  "ds_read_b128 v[2:5], v20 offset:0\n"
  "ds_read_b128 v[6:9], v20 offset:64\n"
  "ds_read_b128 v[10:13], v20 offset:128\n"
  "ds_read_b128 v[14:17], v20 offset:192\n"
  "s_waitcnt lgkmcnt(0)\n"
  "s_nop 0\n"
  "v_pk_fma_f32 v[64:65], v[24:25], v[2:3], v[64:65] op_sel_hi:[0,1,1] neg_lo:[1,0,0] neg_hi:[1,0,0]\n"
  "v_pk_fma_f32 v[66:67], v[24:25], v[4:5], v[66:67] op_sel_hi:[0,1,1] neg_lo:[1,0,0] neg_hi:[1,0,0]\n"
  "v_pk_fma_f32 v[80:81], v[24:25], v[6:7], v[80:81] op_sel_hi:[0,1,1] neg_lo:[1,0,0] neg_hi:[1,0,0]\n"
  "v_pk_fma_f32 v[82:83], v[24:25], v[8:9], v[82:83] op_sel_hi:[0,1,1] neg_lo:[1,0,0] neg_hi:[1,0,0]\n"
  "v_pk_mul_f32 v[48:49], v[64:65], v[64:65]\n"
  "v_pk_mul_f32 v[50:51], v[66:67], v[66:67]\n"
  "v_pk_mul_f32 v[52:53], v[80:81], v[80:81]\n"
  "v_pk_mul_f32 v[54:55], v[82:83], v[82:83]\n"
  "v_pk_fma_f32 v[96:97], v[24:25], v[10:11], v[96:97] op_sel_hi:[0,1,1] neg_lo:[1,0,0] neg_hi:[1,0,0]\n"
  "v_pk_fma_f32 v[98:99], v[24:25], v[12:13], v[98:99] op_sel_hi:[0,1,1] neg_lo:[1,0,0] neg_hi:[1,0,0]\n"
  "v_pk_fma_f32 v[112:113], v[24:25], v[14:15], v[112:113] op_sel_hi:[0,1,1] neg_lo:[1,0,0] neg_hi:[1,0,0]\n"
  "v_pk_fma_f32 v[114:115], v[24:25], v[16:17], v[114:115] op_sel_hi:[0,1,1] neg_lo:[1,0,0] neg_hi:[1,0,0]\n"
  "v_pk_fma_f32 v[48:49], v[48:49], v[32:33], v[32:33] op_sel:[0,1,0] op_sel_hi:[1,1,0]\n"
  "v_pk_fma_f32 v[50:51], v[50:51], v[32:33], v[32:33] op_sel:[0,1,0] op_sel_hi:[1,1,0]\n"
  "v_pk_fma_f32 v[52:53], v[52:53], v[32:33], v[32:33] op_sel:[0,1,0] op_sel_hi:[1,1,0]\n"
  "v_pk_fma_f32 v[54:55], v[54:55], v[32:33], v[32:33] op_sel:[0,1,0] op_sel_hi:[1,1,0]\n"
  "s_mul_i32 s72, s81, 5\n"
  "s_nop 0\n"
  "s_add_u32 s72, s72, 30720\n"
  "s_add_u32 s72, s72, s80\n"
  "s_nop 0\n"
  "buffer_load_dwordx4 a[0:3], v18, s[36:39], s72 offen offset:2048 sc1\n"
  "v_pk_mul_f32 v[48:49], v[48:49], v[64:65]\n"
  "v_pk_mul_f32 v[50:51], v[50:51], v[66:67]\n"
  "v_pk_mul_f32 v[52:53], v[52:53], v[80:81]\n"
  "v_pk_mul_f32 v[54:55], v[54:55], v[82:83]\n"
  "v_exp_f32 v48, v48\n"
  "v_exp_f32 v49, v49\n"
  "v_exp_f32 v50, v50\n"
  "v_exp_f32 v51, v51\n"
  "v_exp_f32 v52, v52\n"
  "v_exp_f32 v53, v53\n"
  "v_exp_f32 v54, v54\n"
  "v_exp_f32 v55, v55\n"
  "buffer_load_dwordx4 a[4:7], v18, s[36:39], s72 offen offset:3072 sc1\n"
  "v_pk_mul_f32 v[96:97], v[96:97], v[64:65]\n"
  "v_pk_mul_f32 v[98:99], v[98:99], v[66:67]\n"
  "v_pk_mul_f32 v[112:113], v[112:113], v[80:81]\n"
  "v_pk_mul_f32 v[114:115], v[114:115], v[82:83]\n"
  "v_pk_fma_f32 v[48:49], v[48:49], v[40:41], v[40:41] op_sel_hi:[1,0,0]\n"
  "v_pk_fma_f32 v[50:51], v[50:51], v[40:41], v[40:41] op_sel_hi:[1,0,0]\n"
  "v_pk_fma_f32 v[52:53], v[52:53], v[40:41], v[40:41] op_sel_hi:[1,0,0]\n"
  "v_pk_fma_f32 v[54:55], v[54:55], v[40:41], v[40:41] op_sel_hi:[1,0,0]\n"
  "v_rcp_f32 v48, v48\n"
  "v_rcp_f32 v49, v49\n"
  "v_rcp_f32 v50, v50\n"
  "v_rcp_f32 v51, v51\n"
  "v_rcp_f32 v52, v52\n"
  "v_rcp_f32 v53, v53\n"
  "v_rcp_f32 v54, v54\n"
  "v_rcp_f32 v55, v55\n"
  "buffer_load_dwordx4 a[8:11], v19, s[36:39], s72 offen sc1\n"
  "v_pk_mul_f32 v[96:97], v[96:97], v[48:49]\n"
  "v_pk_mul_f32 v[98:99], v[98:99], v[50:51]\n"
  "v_pk_mul_f32 v[112:113], v[112:113], v[52:53]\n"
  "v_pk_mul_f32 v[114:115], v[114:115], v[54:55]\n"
  "v_cvt_pk_bf16_f32 v64, v96, v97\n"
  "v_cvt_pk_bf16_f32 v65, v98, v99\n"
  "v_cvt_pk_bf16_f32 v66, v112, v113\n"
  "v_cvt_pk_bf16_f32 v67, v114, v115\n"
  "s_add_u32 s71, s70, 0\n"
  "s_nop 0\n"
  "buffer_store_dwordx4 v[64:67], v18, s[36:39], s71 offen sc1\n"
  "s_mul_i32 s72, s82, 5\n"
  "s_nop 0\n"
  "s_add_u32 s72, s72, 25600\n"
  "s_add_u32 s72, s72, s80\n"
  "s_nop 0\n"
  "buffer_load_dwordx4 a[12:15], v18, s[36:39], s72 offen sc1\n"
  "buffer_load_dwordx4 a[16:19], v18, s[36:39], s72 offen offset:1024 sc1\n"
  "v_pk_fma_f32 v[68:69], v[26:27], v[2:3], v[68:69] op_sel_hi:[0,1,1] neg_lo:[1,0,0] neg_hi:[1,0,0]\n"
  "v_pk_fma_f32 v[70:71], v[26:27], v[4:5], v[70:71] op_sel_hi:[0,1,1] neg_lo:[1,0,0] neg_hi:[1,0,0]\n"
  "v_pk_fma_f32 v[84:85], v[26:27], v[6:7], v[84:85] op_sel_hi:[0,1,1] neg_lo:[1,0,0] neg_hi:[1,0,0]\n"
  "v_pk_fma_f32 v[86:87], v[26:27], v[8:9], v[86:87] op_sel_hi:[0,1,1] neg_lo:[1,0,0] neg_hi:[1,0,0]\n"
  "v_pk_mul_f32 v[48:49], v[68:69], v[68:69]\n"
  "v_pk_mul_f32 v[50:51], v[70:71], v[70:71]\n"
  "v_pk_mul_f32 v[52:53], v[84:85], v[84:85]\n"
  "v_pk_mul_f32 v[54:55], v[86:87], v[86:87]\n"
  "v_pk_fma_f32 v[100:101], v[26:27], v[10:11], v[100:101] op_sel_hi:[0,1,1] neg_lo:[1,0,0] neg_hi:[1,0,0]\n"
  "v_pk_fma_f32 v[102:103], v[26:27], v[12:13], v[102:103] op_sel_hi:[0,1,1] neg_lo:[1,0,0] neg_hi:[1,0,0]\n"
  "v_pk_fma_f32 v[116:117], v[26:27], v[14:15], v[116:117] op_sel_hi:[0,1,1] neg_lo:[1,0,0] neg_hi:[1,0,0]\n"
  "v_pk_fma_f32 v[118:119], v[26:27], v[16:17], v[118:119] op_sel_hi:[0,1,1] neg_lo:[1,0,0] neg_hi:[1,0,0]\n"
  "v_pk_fma_f32 v[48:49], v[48:49], v[34:35], v[34:35] op_sel:[0,1,0] op_sel_hi:[1,1,0]\n"
  "v_pk_fma_f32 v[50:51], v[50:51], v[34:35], v[34:35] op_sel:[0,1,0] op_sel_hi:[1,1,0]\n"
  "v_pk_fma_f32 v[52:53], v[52:53], v[34:35], v[34:35] op_sel:[0,1,0] op_sel_hi:[1,1,0]\n"
  "v_pk_fma_f32 v[54:55], v[54:55], v[34:35], v[34:35] op_sel:[0,1,0] op_sel_hi:[1,1,0]\n"
  "buffer_load_dwordx4 a[20:23], v18, s[36:39], s72 offen offset:2048 sc1\n"
  "v_pk_mul_f32 v[48:49], v[48:49], v[68:69]\n"
  "v_pk_mul_f32 v[50:51], v[50:51], v[70:71]\n"
  "v_pk_mul_f32 v[52:53], v[52:53], v[84:85]\n"
  "v_pk_mul_f32 v[54:55], v[54:55], v[86:87]\n"
  "v_exp_f32 v48, v48\n"
  "v_exp_f32 v49, v49\n"
  "v_exp_f32 v50, v50\n"
  "v_exp_f32 v51, v51\n"
  "v_exp_f32 v52, v52\n"
  "v_exp_f32 v53, v53\n"
  "v_exp_f32 v54, v54\n"
  "v_exp_f32 v55, v55\n"
  "buffer_load_dwordx4 a[24:27], v18, s[36:39], s72 offen offset:3072 sc1\n"
  "v_pk_mul_f32 v[100:101], v[100:101], v[68:69]\n"
  "v_pk_mul_f32 v[102:103], v[102:103], v[70:71]\n"
  "v_pk_mul_f32 v[116:117], v[116:117], v[84:85]\n"
  "v_pk_mul_f32 v[118:119], v[118:119], v[86:87]\n"
  "v_pk_fma_f32 v[48:49], v[48:49], v[42:43], v[42:43] op_sel_hi:[1,0,0]\n"
  "v_pk_fma_f32 v[50:51], v[50:51], v[42:43], v[42:43] op_sel_hi:[1,0,0]\n"
  "v_pk_fma_f32 v[52:53], v[52:53], v[42:43], v[42:43] op_sel_hi:[1,0,0]\n"
  "v_pk_fma_f32 v[54:55], v[54:55], v[42:43], v[42:43] op_sel_hi:[1,0,0]\n"
  "v_rcp_f32 v48, v48\n"
  "v_rcp_f32 v49, v49\n"
  "v_rcp_f32 v50, v50\n"
  "v_rcp_f32 v51, v51\n"
  "v_rcp_f32 v52, v52\n"
  "v_rcp_f32 v53, v53\n"
  "v_rcp_f32 v54, v54\n"
  "v_rcp_f32 v55, v55\n"
  "buffer_load_dwordx4 a[28:31], v19, s[36:39], s72 offen sc1\n"
  "v_pk_mul_f32 v[100:101], v[100:101], v[48:49]\n"
  "v_pk_mul_f32 v[102:103], v[102:103], v[50:51]\n"
  "v_pk_mul_f32 v[116:117], v[116:117], v[52:53]\n"
  "v_pk_mul_f32 v[118:119], v[118:119], v[54:55]\n"
  "v_cvt_pk_bf16_f32 v68, v100, v101\n"
  "v_cvt_pk_bf16_f32 v69, v102, v103\n"
  "v_cvt_pk_bf16_f32 v70, v116, v117\n"
  "v_cvt_pk_bf16_f32 v71, v118, v119\n"
  "s_add_u32 s71, s70, 49152\n"
  "buffer_store_dwordx4 v[68:71], v18, s[36:39], s71 offen sc1\n"
  "s_mul_i32 s72, s82, 5\n"
  "s_nop 0\n"
  "s_add_u32 s72, s72, 30720\n"
  "s_add_u32 s72, s72, s80\n"
  "s_nop 0\n"
  "buffer_load_dwordx4 a[32:35], v18, s[36:39], s72 offen sc1\n"
  "v_pk_fma_f32 v[72:73], v[28:29], v[2:3], v[72:73] op_sel_hi:[0,1,1] neg_lo:[1,0,0] neg_hi:[1,0,0]\n"
  "v_pk_fma_f32 v[74:75], v[28:29], v[4:5], v[74:75] op_sel_hi:[0,1,1] neg_lo:[1,0,0] neg_hi:[1,0,0]\n"
  "v_pk_fma_f32 v[88:89], v[28:29], v[6:7], v[88:89] op_sel_hi:[0,1,1] neg_lo:[1,0,0] neg_hi:[1,0,0]\n"
  "v_pk_fma_f32 v[90:91], v[28:29], v[8:9], v[90:91] op_sel_hi:[0,1,1] neg_lo:[1,0,0] neg_hi:[1,0,0]\n"
  "v_pk_mul_f32 v[48:49], v[72:73], v[72:73]\n"
  "v_pk_mul_f32 v[50:51], v[74:75], v[74:75]\n"
  "v_pk_mul_f32 v[52:53], v[88:89], v[88:89]\n"
  "v_pk_mul_f32 v[54:55], v[90:91], v[90:91]\n"
  "v_pk_fma_f32 v[104:105], v[28:29], v[10:11], v[104:105] op_sel_hi:[0,1,1] neg_lo:[1,0,0] neg_hi:[1,0,0]\n"
  "v_pk_fma_f32 v[106:107], v[28:29], v[12:13], v[106:107] op_sel_hi:[0,1,1] neg_lo:[1,0,0] neg_hi:[1,0,0]\n"
  "v_pk_fma_f32 v[120:121], v[28:29], v[14:15], v[120:121] op_sel_hi:[0,1,1] neg_lo:[1,0,0] neg_hi:[1,0,0]\n"
  "v_pk_fma_f32 v[122:123], v[28:29], v[16:17], v[122:123] op_sel_hi:[0,1,1] neg_lo:[1,0,0] neg_hi:[1,0,0]\n"
  "v_pk_fma_f32 v[48:49], v[48:49], v[36:37], v[36:37] op_sel:[0,1,0] op_sel_hi:[1,1,0]\n"
  "v_pk_fma_f32 v[50:51], v[50:51], v[36:37], v[36:37] op_sel:[0,1,0] op_sel_hi:[1,1,0]\n"
  "v_pk_fma_f32 v[52:53], v[52:53], v[36:37], v[36:37] op_sel:[0,1,0] op_sel_hi:[1,1,0]\n"
  "v_pk_fma_f32 v[54:55], v[54:55], v[36:37], v[36:37] op_sel:[0,1,0] op_sel_hi:[1,1,0]\n"
  "buffer_load_dwordx4 a[36:39], v18, s[36:39], s72 offen offset:1024 sc1\n"
  "v_pk_mul_f32 v[48:49], v[48:49], v[72:73]\n"
  "v_pk_mul_f32 v[50:51], v[50:51], v[74:75]\n"
  "v_pk_mul_f32 v[52:53], v[52:53], v[88:89]\n"
  "v_pk_mul_f32 v[54:55], v[54:55], v[90:91]\n"
  "v_exp_f32 v48, v48\n"
  "v_exp_f32 v49, v49\n"
  "v_exp_f32 v50, v50\n"
  "v_exp_f32 v51, v51\n"
  "v_exp_f32 v52, v52\n"
  "v_exp_f32 v53, v53\n"
  "v_exp_f32 v54, v54\n"
  "v_exp_f32 v55, v55\n"
  "buffer_load_dwordx4 a[40:43], v18, s[36:39], s72 offen offset:2048 sc1\n"
  "v_pk_mul_f32 v[104:105], v[104:105], v[72:73]\n"
  "v_pk_mul_f32 v[106:107], v[106:107], v[74:75]\n"
  "v_pk_mul_f32 v[120:121], v[120:121], v[88:89]\n"
  "v_pk_mul_f32 v[122:123], v[122:123], v[90:91]\n"
  "v_pk_fma_f32 v[48:49], v[48:49], v[44:45], v[44:45] op_sel_hi:[1,0,0]\n"
  "v_pk_fma_f32 v[50:51], v[50:51], v[44:45], v[44:45] op_sel_hi:[1,0,0]\n"
  "v_pk_fma_f32 v[52:53], v[52:53], v[44:45], v[44:45] op_sel_hi:[1,0,0]\n"
  "v_pk_fma_f32 v[54:55], v[54:55], v[44:45], v[44:45] op_sel_hi:[1,0,0]\n"
  "v_rcp_f32 v48, v48\n"
  "v_rcp_f32 v49, v49\n"
  "v_rcp_f32 v50, v50\n"
  "v_rcp_f32 v51, v51\n"
  "v_rcp_f32 v52, v52\n"
  "v_rcp_f32 v53, v53\n"
  "v_rcp_f32 v54, v54\n"
  "v_rcp_f32 v55, v55\n"
  "buffer_load_dwordx4 a[44:47], v18, s[36:39], s72 offen offset:3072 sc1\n"
  "v_pk_mul_f32 v[104:105], v[104:105], v[48:49]\n"
  "v_pk_mul_f32 v[106:107], v[106:107], v[50:51]\n"
  "v_pk_mul_f32 v[120:121], v[120:121], v[52:53]\n"
  "v_pk_mul_f32 v[122:123], v[122:123], v[54:55]\n"
  "v_cvt_pk_bf16_f32 v72, v104, v105\n"
  "v_cvt_pk_bf16_f32 v73, v106, v107\n"
  "v_cvt_pk_bf16_f32 v74, v120, v121\n"
  "v_cvt_pk_bf16_f32 v75, v122, v123\n"
  "s_add_u32 s71, s70, 98304\n"
  "buffer_store_dwordx4 v[72:75], v18, s[36:39], s71 offen sc1\n"
  "buffer_load_dwordx4 a[48:51], v19, s[36:39], s72 offen sc1\n"
  "v_pk_fma_f32 v[76:77], v[30:31], v[2:3], v[76:77] op_sel_hi:[0,1,1] neg_lo:[1,0,0] neg_hi:[1,0,0]\n"
  "v_pk_fma_f32 v[78:79], v[30:31], v[4:5], v[78:79] op_sel_hi:[0,1,1] neg_lo:[1,0,0] neg_hi:[1,0,0]\n"
  "v_pk_fma_f32 v[92:93], v[30:31], v[6:7], v[92:93] op_sel_hi:[0,1,1] neg_lo:[1,0,0] neg_hi:[1,0,0]\n"
  "v_pk_fma_f32 v[94:95], v[30:31], v[8:9], v[94:95] op_sel_hi:[0,1,1] neg_lo:[1,0,0] neg_hi:[1,0,0]\n"
  "v_pk_mul_f32 v[48:49], v[76:77], v[76:77]\n"
  "v_pk_mul_f32 v[50:51], v[78:79], v[78:79]\n"
  "v_pk_mul_f32 v[52:53], v[92:93], v[92:93]\n"
  "v_pk_mul_f32 v[54:55], v[94:95], v[94:95]\n"
  "v_pk_fma_f32 v[108:109], v[30:31], v[10:11], v[108:109] op_sel_hi:[0,1,1] neg_lo:[1,0,0] neg_hi:[1,0,0]\n"
  "v_pk_fma_f32 v[110:111], v[30:31], v[12:13], v[110:111] op_sel_hi:[0,1,1] neg_lo:[1,0,0] neg_hi:[1,0,0]\n"
  "v_pk_fma_f32 v[124:125], v[30:31], v[14:15], v[124:125] op_sel_hi:[0,1,1] neg_lo:[1,0,0] neg_hi:[1,0,0]\n"
  "v_pk_fma_f32 v[126:127], v[30:31], v[16:17], v[126:127] op_sel_hi:[0,1,1] neg_lo:[1,0,0] neg_hi:[1,0,0]\n"
  "v_pk_fma_f32 v[48:49], v[48:49], v[38:39], v[38:39] op_sel:[0,1,0] op_sel_hi:[1,1,0]\n"
  "v_pk_fma_f32 v[50:51], v[50:51], v[38:39], v[38:39] op_sel:[0,1,0] op_sel_hi:[1,1,0]\n"
  "v_pk_fma_f32 v[52:53], v[52:53], v[38:39], v[38:39] op_sel:[0,1,0] op_sel_hi:[1,1,0]\n"
  "v_pk_fma_f32 v[54:55], v[54:55], v[38:39], v[38:39] op_sel:[0,1,0] op_sel_hi:[1,1,0]\n"
  "s_mul_i32 s72, s83, 5\n"
  "s_nop 0\n"
  "s_add_u32 s72, s72, 25600\n"
  "s_add_u32 s72, s72, s80\n"
  "s_nop 0\n"
  "buffer_load_dwordx4 a[52:55], v18, s[36:39], s72 offen sc1\n"
  "v_pk_mul_f32 v[48:49], v[48:49], v[76:77]\n"
  "v_pk_mul_f32 v[50:51], v[50:51], v[78:79]\n"
  "v_pk_mul_f32 v[52:53], v[52:53], v[92:93]\n"
  "v_pk_mul_f32 v[54:55], v[54:55], v[94:95]\n"
  "v_exp_f32 v48, v48\n"
  "v_exp_f32 v49, v49\n"
  "v_exp_f32 v50, v50\n"
  "v_exp_f32 v51, v51\n"
  "v_exp_f32 v52, v52\n"
  "v_exp_f32 v53, v53\n"
  "v_exp_f32 v54, v54\n"
  "v_exp_f32 v55, v55\n"
  "buffer_load_dwordx4 a[56:59], v18, s[36:39], s72 offen offset:1024 sc1\n"
  "v_pk_mul_f32 v[108:109], v[108:109], v[76:77]\n"
  "v_pk_mul_f32 v[110:111], v[110:111], v[78:79]\n"
  "v_pk_mul_f32 v[124:125], v[124:125], v[92:93]\n"
  "v_pk_mul_f32 v[126:127], v[126:127], v[94:95]\n"
  "v_pk_fma_f32 v[48:49], v[48:49], v[46:47], v[46:47] op_sel_hi:[1,0,0]\n"
  "v_pk_fma_f32 v[50:51], v[50:51], v[46:47], v[46:47] op_sel_hi:[1,0,0]\n"
  "v_pk_fma_f32 v[52:53], v[52:53], v[46:47], v[46:47] op_sel_hi:[1,0,0]\n"
  "v_pk_fma_f32 v[54:55], v[54:55], v[46:47], v[46:47] op_sel_hi:[1,0,0]\n"
  "v_rcp_f32 v48, v48\n"
  "v_rcp_f32 v49, v49\n"
  "v_rcp_f32 v50, v50\n"
  "v_rcp_f32 v51, v51\n"
  "v_rcp_f32 v52, v52\n"
  "v_rcp_f32 v53, v53\n"
  "v_rcp_f32 v54, v54\n"
  "v_rcp_f32 v55, v55\n"
  "buffer_load_dwordx4 a[60:63], v18, s[36:39], s72 offen offset:2048 sc1\n"
  "v_pk_mul_f32 v[108:109], v[108:109], v[48:49]\n"
  "v_pk_mul_f32 v[110:111], v[110:111], v[50:51]\n"
  "v_pk_mul_f32 v[124:125], v[124:125], v[52:53]\n"
  "v_pk_mul_f32 v[126:127], v[126:127], v[54:55]\n"
  "v_cvt_pk_bf16_f32 v76, v108, v109\n"
  "v_cvt_pk_bf16_f32 v77, v110, v111\n"
  "v_cvt_pk_bf16_f32 v78, v124, v125\n"
  "v_cvt_pk_bf16_f32 v79, v126, v127\n"
  "s_add_u32 s71, s70, 147456\n"
  "buffer_store_dwordx4 v[76:79], v18, s[36:39], s71 offen sc1\n"
  "buffer_load_dwordx4 a[64:67], v18, s[36:39], s72 offen offset:3072 sc1\n"
  "buffer_load_dwordx4 a[68:71], v19, s[36:39], s72 offen sc1\n"
  "ds_read_b128 v[2:5], v20 offset:256\n"
  "ds_read_b128 v[6:9], v20 offset:320\n"
  "ds_read_b128 v[10:13], v20 offset:384\n"
  "ds_read_b128 v[14:17], v20 offset:448\n"
  "s_waitcnt lgkmcnt(0)\n"
  "s_nop 0\n"
  "v_pk_fma_f32 v[128:129], v[24:25], v[2:3], v[128:129] op_sel_hi:[0,1,1] neg_lo:[1,0,0] neg_hi:[1,0,0]\n"
  "v_pk_fma_f32 v[130:131], v[24:25], v[4:5], v[130:131] op_sel_hi:[0,1,1] neg_lo:[1,0,0] neg_hi:[1,0,0]\n"
  "v_pk_fma_f32 v[144:145], v[24:25], v[6:7], v[144:145] op_sel_hi:[0,1,1] neg_lo:[1,0,0] neg_hi:[1,0,0]\n"
  "v_pk_fma_f32 v[146:147], v[24:25], v[8:9], v[146:147] op_sel_hi:[0,1,1] neg_lo:[1,0,0] neg_hi:[1,0,0]\n"
  "v_pk_mul_f32 v[48:49], v[128:129], v[128:129]\n"
  "v_pk_mul_f32 v[50:51], v[130:131], v[130:131]\n"
  "v_pk_mul_f32 v[52:53], v[144:145], v[144:145]\n"
  "v_pk_mul_f32 v[54:55], v[146:147], v[146:147]\n"
  "v_pk_fma_f32 v[160:161], v[24:25], v[10:11], v[160:161] op_sel_hi:[0,1,1] neg_lo:[1,0,0] neg_hi:[1,0,0]\n"
  "v_pk_fma_f32 v[162:163], v[24:25], v[12:13], v[162:163] op_sel_hi:[0,1,1] neg_lo:[1,0,0] neg_hi:[1,0,0]\n"
  "v_pk_fma_f32 v[176:177], v[24:25], v[14:15], v[176:177] op_sel_hi:[0,1,1] neg_lo:[1,0,0] neg_hi:[1,0,0]\n"
  "v_pk_fma_f32 v[178:179], v[24:25], v[16:17], v[178:179] op_sel_hi:[0,1,1] neg_lo:[1,0,0] neg_hi:[1,0,0]\n"
  "v_pk_fma_f32 v[48:49], v[48:49], v[32:33], v[32:33] op_sel:[0,1,0] op_sel_hi:[1,1,0]\n"
  "v_pk_fma_f32 v[50:51], v[50:51], v[32:33], v[32:33] op_sel:[0,1,0] op_sel_hi:[1,1,0]\n"
  "v_pk_fma_f32 v[52:53], v[52:53], v[32:33], v[32:33] op_sel:[0,1,0] op_sel_hi:[1,1,0]\n"
  "v_pk_fma_f32 v[54:55], v[54:55], v[32:33], v[32:33] op_sel:[0,1,0] op_sel_hi:[1,1,0]\n"
  "s_mul_i32 s72, s83, 5\n"
  "s_nop 0\n"
  "s_add_u32 s72, s72, 30720\n"
  "s_add_u32 s72, s72, s80\n"
  "s_nop 0\n"
  "buffer_load_dwordx4 a[72:75], v18, s[36:39], s72 offen sc1\n"
  "v_pk_mul_f32 v[48:49], v[48:49], v[128:129]\n"
  "v_pk_mul_f32 v[50:51], v[50:51], v[130:131]\n"
  "v_pk_mul_f32 v[52:53], v[52:53], v[144:145]\n"
  "v_pk_mul_f32 v[54:55], v[54:55], v[146:147]\n"
  "v_exp_f32 v48, v48\n"
  "v_exp_f32 v49, v49\n"
  "v_exp_f32 v50, v50\n"
  "v_exp_f32 v51, v51\n"
  "v_exp_f32 v52, v52\n"
  "v_exp_f32 v53, v53\n"
  "v_exp_f32 v54, v54\n"
  "v_exp_f32 v55, v55\n"
  "buffer_load_dwordx4 a[76:79], v18, s[36:39], s72 offen offset:1024 sc1\n"
  "v_pk_mul_f32 v[160:161], v[160:161], v[128:129]\n"
  "v_pk_mul_f32 v[162:163], v[162:163], v[130:131]\n"
  "v_pk_mul_f32 v[176:177], v[176:177], v[144:145]\n"
  "v_pk_mul_f32 v[178:179], v[178:179], v[146:147]\n"
  "v_pk_fma_f32 v[48:49], v[48:49], v[40:41], v[40:41] op_sel_hi:[1,0,0]\n"
  "v_pk_fma_f32 v[50:51], v[50:51], v[40:41], v[40:41] op_sel_hi:[1,0,0]\n"
  "v_pk_fma_f32 v[52:53], v[52:53], v[40:41], v[40:41] op_sel_hi:[1,0,0]\n"
  "v_pk_fma_f32 v[54:55], v[54:55], v[40:41], v[40:41] op_sel_hi:[1,0,0]\n"
  "v_rcp_f32 v48, v48\n"
  "v_rcp_f32 v49, v49\n"
  "v_rcp_f32 v50, v50\n"
  "v_rcp_f32 v51, v51\n"
  "v_rcp_f32 v52, v52\n"
  "v_rcp_f32 v53, v53\n"
  "v_rcp_f32 v54, v54\n"
  "v_rcp_f32 v55, v55\n"
  "buffer_load_dwordx4 a[80:83], v18, s[36:39], s72 offen offset:2048 sc1\n"
  "v_pk_mul_f32 v[160:161], v[160:161], v[48:49]\n"
  "v_pk_mul_f32 v[162:163], v[162:163], v[50:51]\n"
  "v_pk_mul_f32 v[176:177], v[176:177], v[52:53]\n"
  "v_pk_mul_f32 v[178:179], v[178:179], v[54:55]\n"
  "v_cvt_pk_bf16_f32 v128, v160, v161\n"
  "v_cvt_pk_bf16_f32 v129, v162, v163\n"
  "v_cvt_pk_bf16_f32 v130, v176, v177\n"
  "v_cvt_pk_bf16_f32 v131, v178, v179\n"
  "s_add_u32 s71, s70, 2048\n"
  "buffer_store_dwordx4 v[128:131], v18, s[36:39], s71 offen sc1\n"
  "buffer_load_dwordx4 a[84:87], v18, s[36:39], s72 offen offset:3072 sc1\n"
  "v_pk_fma_f32 v[132:133], v[26:27], v[2:3], v[132:133] op_sel_hi:[0,1,1] neg_lo:[1,0,0] neg_hi:[1,0,0]\n"
  "v_pk_fma_f32 v[134:135], v[26:27], v[4:5], v[134:135] op_sel_hi:[0,1,1] neg_lo:[1,0,0] neg_hi:[1,0,0]\n"
  "v_pk_fma_f32 v[148:149], v[26:27], v[6:7], v[148:149] op_sel_hi:[0,1,1] neg_lo:[1,0,0] neg_hi:[1,0,0]\n"
  "v_pk_fma_f32 v[150:151], v[26:27], v[8:9], v[150:151] op_sel_hi:[0,1,1] neg_lo:[1,0,0] neg_hi:[1,0,0]\n"
  "v_pk_mul_f32 v[48:49], v[132:133], v[132:133]\n"
  "v_pk_mul_f32 v[50:51], v[134:135], v[134:135]\n"
  "v_pk_mul_f32 v[52:53], v[148:149], v[148:149]\n"
  "v_pk_mul_f32 v[54:55], v[150:151], v[150:151]\n"
  "v_pk_fma_f32 v[164:165], v[26:27], v[10:11], v[164:165] op_sel_hi:[0,1,1] neg_lo:[1,0,0] neg_hi:[1,0,0]\n"
  "v_pk_fma_f32 v[166:167], v[26:27], v[12:13], v[166:167] op_sel_hi:[0,1,1] neg_lo:[1,0,0] neg_hi:[1,0,0]\n"
  "v_pk_fma_f32 v[180:181], v[26:27], v[14:15], v[180:181] op_sel_hi:[0,1,1] neg_lo:[1,0,0] neg_hi:[1,0,0]\n"
  "v_pk_fma_f32 v[182:183], v[26:27], v[16:17], v[182:183] op_sel_hi:[0,1,1] neg_lo:[1,0,0] neg_hi:[1,0,0]\n"
  "v_pk_fma_f32 v[48:49], v[48:49], v[34:35], v[34:35] op_sel:[0,1,0] op_sel_hi:[1,1,0]\n"
  "v_pk_fma_f32 v[50:51], v[50:51], v[34:35], v[34:35] op_sel:[0,1,0] op_sel_hi:[1,1,0]\n"
  "v_pk_fma_f32 v[52:53], v[52:53], v[34:35], v[34:35] op_sel:[0,1,0] op_sel_hi:[1,1,0]\n"
  "v_pk_fma_f32 v[54:55], v[54:55], v[34:35], v[34:35] op_sel:[0,1,0] op_sel_hi:[1,1,0]\n"
  "buffer_load_dwordx4 a[88:91], v19, s[36:39], s72 offen sc1\n"
  "v_pk_mul_f32 v[48:49], v[48:49], v[132:133]\n"
  "v_pk_mul_f32 v[50:51], v[50:51], v[134:135]\n"
  "v_pk_mul_f32 v[52:53], v[52:53], v[148:149]\n"
  "v_pk_mul_f32 v[54:55], v[54:55], v[150:151]\n"
  "v_exp_f32 v48, v48\n"
  "v_exp_f32 v49, v49\n"
  "v_exp_f32 v50, v50\n"
  "v_exp_f32 v51, v51\n"
  "v_exp_f32 v52, v52\n"
  "v_exp_f32 v53, v53\n"
  "v_exp_f32 v54, v54\n"
  "v_exp_f32 v55, v55\n"
  "s_mul_i32 s72, s84, 5\n"
  "s_nop 0\n"
  "s_add_u32 s72, s72, 25600\n"
  "s_add_u32 s72, s72, s80\n"
  "s_nop 0\n"
  "buffer_load_dwordx4 a[92:95], v18, s[36:39], s72 offen sc1\n"
  "v_pk_mul_f32 v[164:165], v[164:165], v[132:133]\n"
  "v_pk_mul_f32 v[166:167], v[166:167], v[134:135]\n"
  "v_pk_mul_f32 v[180:181], v[180:181], v[148:149]\n"
  "v_pk_mul_f32 v[182:183], v[182:183], v[150:151]\n"
  "v_pk_fma_f32 v[48:49], v[48:49], v[42:43], v[42:43] op_sel_hi:[1,0,0]\n"
  "v_pk_fma_f32 v[50:51], v[50:51], v[42:43], v[42:43] op_sel_hi:[1,0,0]\n"
  "v_pk_fma_f32 v[52:53], v[52:53], v[42:43], v[42:43] op_sel_hi:[1,0,0]\n"
  "v_pk_fma_f32 v[54:55], v[54:55], v[42:43], v[42:43] op_sel_hi:[1,0,0]\n"
  "v_rcp_f32 v48, v48\n"
  "v_rcp_f32 v49, v49\n"
  "v_rcp_f32 v50, v50\n"
  "v_rcp_f32 v51, v51\n"
  "v_rcp_f32 v52, v52\n"
  "v_rcp_f32 v53, v53\n"
  "v_rcp_f32 v54, v54\n"
  "v_rcp_f32 v55, v55\n"
  "buffer_load_dwordx4 a[96:99], v18, s[36:39], s72 offen offset:1024 sc1\n"
  "v_pk_mul_f32 v[164:165], v[164:165], v[48:49]\n"
  "v_pk_mul_f32 v[166:167], v[166:167], v[50:51]\n"
  "v_pk_mul_f32 v[180:181], v[180:181], v[52:53]\n"
  "v_pk_mul_f32 v[182:183], v[182:183], v[54:55]\n"
  "v_cvt_pk_bf16_f32 v132, v164, v165\n"
  "v_cvt_pk_bf16_f32 v133, v166, v167\n"
  "v_cvt_pk_bf16_f32 v134, v180, v181\n"
  "v_cvt_pk_bf16_f32 v135, v182, v183\n"
  "s_add_u32 s71, s70, 51200\n"
  "buffer_store_dwordx4 v[132:135], v18, s[36:39], s71 offen sc1\n"
  "buffer_load_dwordx4 a[100:103], v18, s[36:39], s72 offen offset:2048 sc1\n"
  "v_pk_fma_f32 v[136:137], v[28:29], v[2:3], v[136:137] op_sel_hi:[0,1,1] neg_lo:[1,0,0] neg_hi:[1,0,0]\n"
  "v_pk_fma_f32 v[138:139], v[28:29], v[4:5], v[138:139] op_sel_hi:[0,1,1] neg_lo:[1,0,0] neg_hi:[1,0,0]\n"
  "v_pk_fma_f32 v[152:153], v[28:29], v[6:7], v[152:153] op_sel_hi:[0,1,1] neg_lo:[1,0,0] neg_hi:[1,0,0]\n"
  "v_pk_fma_f32 v[154:155], v[28:29], v[8:9], v[154:155] op_sel_hi:[0,1,1] neg_lo:[1,0,0] neg_hi:[1,0,0]\n"
  "v_pk_mul_f32 v[48:49], v[136:137], v[136:137]\n"
  "v_pk_mul_f32 v[50:51], v[138:139], v[138:139]\n"
  "v_pk_mul_f32 v[52:53], v[152:153], v[152:153]\n"
  "v_pk_mul_f32 v[54:55], v[154:155], v[154:155]\n"
  "v_pk_fma_f32 v[168:169], v[28:29], v[10:11], v[168:169] op_sel_hi:[0,1,1] neg_lo:[1,0,0] neg_hi:[1,0,0]\n"
  "v_pk_fma_f32 v[170:171], v[28:29], v[12:13], v[170:171] op_sel_hi:[0,1,1] neg_lo:[1,0,0] neg_hi:[1,0,0]\n"
  "v_pk_fma_f32 v[184:185], v[28:29], v[14:15], v[184:185] op_sel_hi:[0,1,1] neg_lo:[1,0,0] neg_hi:[1,0,0]\n"
  "v_pk_fma_f32 v[186:187], v[28:29], v[16:17], v[186:187] op_sel_hi:[0,1,1] neg_lo:[1,0,0] neg_hi:[1,0,0]\n"
  "v_pk_fma_f32 v[48:49], v[48:49], v[36:37], v[36:37] op_sel:[0,1,0] op_sel_hi:[1,1,0]\n"
  "v_pk_fma_f32 v[50:51], v[50:51], v[36:37], v[36:37] op_sel:[0,1,0] op_sel_hi:[1,1,0]\n"
  "v_pk_fma_f32 v[52:53], v[52:53], v[36:37], v[36:37] op_sel:[0,1,0] op_sel_hi:[1,1,0]\n"
  "v_pk_fma_f32 v[54:55], v[54:55], v[36:37], v[36:37] op_sel:[0,1,0] op_sel_hi:[1,1,0]\n"
  "buffer_load_dwordx4 a[104:107], v18, s[36:39], s72 offen offset:3072 sc1\n"
  "v_pk_mul_f32 v[48:49], v[48:49], v[136:137]\n"
  "v_pk_mul_f32 v[50:51], v[50:51], v[138:139]\n"
  "v_pk_mul_f32 v[52:53], v[52:53], v[152:153]\n"
  "v_pk_mul_f32 v[54:55], v[54:55], v[154:155]\n"
  "v_exp_f32 v48, v48\n"
  "v_exp_f32 v49, v49\n"
  "v_exp_f32 v50, v50\n"
  "v_exp_f32 v51, v51\n"
  "v_exp_f32 v52, v52\n"
  "v_exp_f32 v53, v53\n"
  "v_exp_f32 v54, v54\n"
  "v_exp_f32 v55, v55\n"
  "buffer_load_dwordx4 a[108:111], v19, s[36:39], s72 offen sc1\n"
  "v_pk_mul_f32 v[168:169], v[168:169], v[136:137]\n"
  "v_pk_mul_f32 v[170:171], v[170:171], v[138:139]\n"
  "v_pk_mul_f32 v[184:185], v[184:185], v[152:153]\n"
  "v_pk_mul_f32 v[186:187], v[186:187], v[154:155]\n"
  "v_pk_fma_f32 v[48:49], v[48:49], v[44:45], v[44:45] op_sel_hi:[1,0,0]\n"
  "v_pk_fma_f32 v[50:51], v[50:51], v[44:45], v[44:45] op_sel_hi:[1,0,0]\n"
  "v_pk_fma_f32 v[52:53], v[52:53], v[44:45], v[44:45] op_sel_hi:[1,0,0]\n"
  "v_pk_fma_f32 v[54:55], v[54:55], v[44:45], v[44:45] op_sel_hi:[1,0,0]\n"
  "v_rcp_f32 v48, v48\n"
  "v_rcp_f32 v49, v49\n"
  "v_rcp_f32 v50, v50\n"
  "v_rcp_f32 v51, v51\n"
  "v_rcp_f32 v52, v52\n"
  "v_rcp_f32 v53, v53\n"
  "v_rcp_f32 v54, v54\n"
  "v_rcp_f32 v55, v55\n"
  "s_mul_i32 s72, s84, 5\n"
  "s_nop 0\n"
  "s_add_u32 s72, s72, 30720\n"
  "s_add_u32 s72, s72, s80\n"
  "s_nop 0\n"
  "buffer_load_dwordx4 a[112:115], v18, s[36:39], s72 offen sc1\n"
  "v_pk_mul_f32 v[168:169], v[168:169], v[48:49]\n"
  "v_pk_mul_f32 v[170:171], v[170:171], v[50:51]\n"
  "v_pk_mul_f32 v[184:185], v[184:185], v[52:53]\n"
  "v_pk_mul_f32 v[186:187], v[186:187], v[54:55]\n"
  "v_cvt_pk_bf16_f32 v136, v168, v169\n"
  "v_cvt_pk_bf16_f32 v137, v170, v171\n"
  "v_cvt_pk_bf16_f32 v138, v184, v185\n"
  "v_cvt_pk_bf16_f32 v139, v186, v187\n"
  "s_add_u32 s71, s70, 100352\n"
  "buffer_store_dwordx4 v[136:139], v18, s[36:39], s71 offen sc1\n"
  "buffer_load_dwordx4 a[116:119], v18, s[36:39], s72 offen offset:1024 sc1\n"
  "buffer_load_dwordx4 a[120:123], v18, s[36:39], s72 offen offset:2048 sc1\n"
  "v_pk_fma_f32 v[140:141], v[30:31], v[2:3], v[140:141] op_sel_hi:[0,1,1] neg_lo:[1,0,0] neg_hi:[1,0,0]\n"
  "v_pk_fma_f32 v[142:143], v[30:31], v[4:5], v[142:143] op_sel_hi:[0,1,1] neg_lo:[1,0,0] neg_hi:[1,0,0]\n"
  "v_pk_fma_f32 v[156:157], v[30:31], v[6:7], v[156:157] op_sel_hi:[0,1,1] neg_lo:[1,0,0] neg_hi:[1,0,0]\n"
  "v_pk_fma_f32 v[158:159], v[30:31], v[8:9], v[158:159] op_sel_hi:[0,1,1] neg_lo:[1,0,0] neg_hi:[1,0,0]\n"
  "v_pk_mul_f32 v[48:49], v[140:141], v[140:141]\n"
  "v_pk_mul_f32 v[50:51], v[142:143], v[142:143]\n"
  "v_pk_mul_f32 v[52:53], v[156:157], v[156:157]\n"
  "v_pk_mul_f32 v[54:55], v[158:159], v[158:159]\n"
  "v_pk_fma_f32 v[172:173], v[30:31], v[10:11], v[172:173] op_sel_hi:[0,1,1] neg_lo:[1,0,0] neg_hi:[1,0,0]\n"
  "v_pk_fma_f32 v[174:175], v[30:31], v[12:13], v[174:175] op_sel_hi:[0,1,1] neg_lo:[1,0,0] neg_hi:[1,0,0]\n"
  "v_pk_fma_f32 v[188:189], v[30:31], v[14:15], v[188:189] op_sel_hi:[0,1,1] neg_lo:[1,0,0] neg_hi:[1,0,0]\n"
  "v_pk_fma_f32 v[190:191], v[30:31], v[16:17], v[190:191] op_sel_hi:[0,1,1] neg_lo:[1,0,0] neg_hi:[1,0,0]\n"
  "v_pk_fma_f32 v[48:49], v[48:49], v[38:39], v[38:39] op_sel:[0,1,0] op_sel_hi:[1,1,0]\n"
  "v_pk_fma_f32 v[50:51], v[50:51], v[38:39], v[38:39] op_sel:[0,1,0] op_sel_hi:[1,1,0]\n"
  "v_pk_fma_f32 v[52:53], v[52:53], v[38:39], v[38:39] op_sel:[0,1,0] op_sel_hi:[1,1,0]\n"
  "v_pk_fma_f32 v[54:55], v[54:55], v[38:39], v[38:39] op_sel:[0,1,0] op_sel_hi:[1,1,0]\n"
  "buffer_load_dwordx4 a[124:127], v18, s[36:39], s72 offen offset:3072 sc1\n"
  "v_pk_mul_f32 v[48:49], v[48:49], v[140:141]\n"
  "v_pk_mul_f32 v[50:51], v[50:51], v[142:143]\n"
  "v_pk_mul_f32 v[52:53], v[52:53], v[156:157]\n"
  "v_pk_mul_f32 v[54:55], v[54:55], v[158:159]\n"
  "v_exp_f32 v48, v48\n"
  "v_exp_f32 v49, v49\n"
  "v_exp_f32 v50, v50\n"
  "v_exp_f32 v51, v51\n"
  "v_exp_f32 v52, v52\n"
  "v_exp_f32 v53, v53\n"
  "v_exp_f32 v54, v54\n"
  "v_exp_f32 v55, v55\n"
  "s_mul_i32 s72, s84, 5\n"
  "s_nop 0\n"
  "s_add_u32 s72, s72, 30720\n"
  "s_add_u32 s72, s72, s80\n"
  "s_add_u32 m0, s65, 0\n"
  "s_nop 0\n"
  "s_nop 0\n"
  "buffer_load_dwordx4 v19, s[36:39], s72 offen sc1 lds\n"
  "v_pk_mul_f32 v[172:173], v[172:173], v[140:141]\n"
  "v_pk_mul_f32 v[174:175], v[174:175], v[142:143]\n"
  "v_pk_mul_f32 v[188:189], v[188:189], v[156:157]\n"
  "v_pk_mul_f32 v[190:191], v[190:191], v[158:159]\n"
  "v_pk_fma_f32 v[48:49], v[48:49], v[46:47], v[46:47] op_sel_hi:[1,0,0]\n"
  "v_pk_fma_f32 v[50:51], v[50:51], v[46:47], v[46:47] op_sel_hi:[1,0,0]\n"
  "v_pk_fma_f32 v[52:53], v[52:53], v[46:47], v[46:47] op_sel_hi:[1,0,0]\n"
  "v_pk_fma_f32 v[54:55], v[54:55], v[46:47], v[46:47] op_sel_hi:[1,0,0]\n"
  "v_rcp_f32 v48, v48\n"
  "v_rcp_f32 v49, v49\n"
  "v_rcp_f32 v50, v50\n"
  "v_rcp_f32 v51, v51\n"
  "v_rcp_f32 v52, v52\n"
  "v_rcp_f32 v53, v53\n"
  "v_rcp_f32 v54, v54\n"
  "v_rcp_f32 v55, v55\n"
  "s_mul_i32 s72, s85, 5\n"
  "s_nop 0\n"
  "s_add_u32 s72, s72, 25600\n"
  "s_add_u32 s72, s72, s80\n"
  "s_nop 0\n"
  "s_add_u32 m0, s65, 1024\n"
  "s_nop 0\n"
  "s_nop 0\n"
  "buffer_load_dwordx4 v18, s[36:39], s72 offen sc1 lds\n"
  "v_pk_mul_f32 v[172:173], v[172:173], v[48:49]\n"
  "v_pk_mul_f32 v[174:175], v[174:175], v[50:51]\n"
  "v_pk_mul_f32 v[188:189], v[188:189], v[52:53]\n"
  "v_pk_mul_f32 v[190:191], v[190:191], v[54:55]\n"
  "v_cvt_pk_bf16_f32 v140, v172, v173\n"
  "v_cvt_pk_bf16_f32 v141, v174, v175\n"
  "v_cvt_pk_bf16_f32 v142, v188, v189\n"
  "v_cvt_pk_bf16_f32 v143, v190, v191\n"
  "s_add_u32 s71, s70, 149504\n"
  "buffer_store_dwordx4 v[140:143], v18, s[36:39], s71 offen sc1\n"
  "s_add_u32 m0, s65, 1024\n"
  "s_nop 0\n"
  "s_nop 0\n"
  "buffer_load_dwordx4 v18, s[36:39], s72 offen offset:1024 sc1 lds\n"
  "ds_read_b128 v[2:5], v20 offset:512\n"
  "ds_read_b128 v[6:9], v20 offset:576\n"
  "ds_read_b128 v[10:13], v20 offset:640\n"
  "ds_read_b128 v[14:17], v20 offset:704\n"
  "s_waitcnt lgkmcnt(0)\n"
  "s_nop 0\n"
  "v_pk_fma_f32 v[192:193], v[24:25], v[2:3], v[192:193] op_sel_hi:[0,1,1] neg_lo:[1,0,0] neg_hi:[1,0,0]\n"
  "v_pk_fma_f32 v[194:195], v[24:25], v[4:5], v[194:195] op_sel_hi:[0,1,1] neg_lo:[1,0,0] neg_hi:[1,0,0]\n"
  "v_pk_fma_f32 v[208:209], v[24:25], v[6:7], v[208:209] op_sel_hi:[0,1,1] neg_lo:[1,0,0] neg_hi:[1,0,0]\n"
  "v_pk_fma_f32 v[210:211], v[24:25], v[8:9], v[210:211] op_sel_hi:[0,1,1] neg_lo:[1,0,0] neg_hi:[1,0,0]\n"
  "v_pk_mul_f32 v[48:49], v[192:193], v[192:193]\n"
  "v_pk_mul_f32 v[50:51], v[194:195], v[194:195]\n"
  "v_pk_mul_f32 v[52:53], v[208:209], v[208:209]\n"
  "v_pk_mul_f32 v[54:55], v[210:211], v[210:211]\n"
  "v_pk_fma_f32 v[224:225], v[24:25], v[10:11], v[224:225] op_sel_hi:[0,1,1] neg_lo:[1,0,0] neg_hi:[1,0,0]\n"
  "v_pk_fma_f32 v[226:227], v[24:25], v[12:13], v[226:227] op_sel_hi:[0,1,1] neg_lo:[1,0,0] neg_hi:[1,0,0]\n"
  "v_pk_fma_f32 v[240:241], v[24:25], v[14:15], v[240:241] op_sel_hi:[0,1,1] neg_lo:[1,0,0] neg_hi:[1,0,0]\n"
  "v_pk_fma_f32 v[242:243], v[24:25], v[16:17], v[242:243] op_sel_hi:[0,1,1] neg_lo:[1,0,0] neg_hi:[1,0,0]\n"
  "v_pk_fma_f32 v[48:49], v[48:49], v[32:33], v[32:33] op_sel:[0,1,0] op_sel_hi:[1,1,0]\n"
  "v_pk_fma_f32 v[50:51], v[50:51], v[32:33], v[32:33] op_sel:[0,1,0] op_sel_hi:[1,1,0]\n"
  "v_pk_fma_f32 v[52:53], v[52:53], v[32:33], v[32:33] op_sel:[0,1,0] op_sel_hi:[1,1,0]\n"
  "v_pk_fma_f32 v[54:55], v[54:55], v[32:33], v[32:33] op_sel:[0,1,0] op_sel_hi:[1,1,0]\n"
  "s_add_u32 m0, s65, 1024\n"
  "s_nop 0\n"
  "s_nop 0\n"
  "buffer_load_dwordx4 v18, s[36:39], s72 offen offset:2048 sc1 lds\n"
  "v_pk_mul_f32 v[48:49], v[48:49], v[192:193]\n"
  "v_pk_mul_f32 v[50:51], v[50:51], v[194:195]\n"
  "v_pk_mul_f32 v[52:53], v[52:53], v[208:209]\n"
  "v_pk_mul_f32 v[54:55], v[54:55], v[210:211]\n"
  "v_exp_f32 v48, v48\n"
  "v_exp_f32 v49, v49\n"
  "v_exp_f32 v50, v50\n"
  "v_exp_f32 v51, v51\n"
  "v_exp_f32 v52, v52\n"
  "v_exp_f32 v53, v53\n"
  "v_exp_f32 v54, v54\n"
  "v_exp_f32 v55, v55\n"
  "s_add_u32 m0, s65, 1024\n"
  "s_nop 0\n"
  "s_nop 0\n"
  "buffer_load_dwordx4 v18, s[36:39], s72 offen offset:3072 sc1 lds\n"
  "v_pk_mul_f32 v[224:225], v[224:225], v[192:193]\n"
  "v_pk_mul_f32 v[226:227], v[226:227], v[194:195]\n"
  "v_pk_mul_f32 v[240:241], v[240:241], v[208:209]\n"
  "v_pk_mul_f32 v[242:243], v[242:243], v[210:211]\n"
  "v_pk_fma_f32 v[48:49], v[48:49], v[40:41], v[40:41] op_sel_hi:[1,0,0]\n"
  "v_pk_fma_f32 v[50:51], v[50:51], v[40:41], v[40:41] op_sel_hi:[1,0,0]\n"
  "v_pk_fma_f32 v[52:53], v[52:53], v[40:41], v[40:41] op_sel_hi:[1,0,0]\n"
  "v_pk_fma_f32 v[54:55], v[54:55], v[40:41], v[40:41] op_sel_hi:[1,0,0]\n"
  "v_rcp_f32 v48, v48\n"
  "v_rcp_f32 v49, v49\n"
  "v_rcp_f32 v50, v50\n"
  "v_rcp_f32 v51, v51\n"
  "v_rcp_f32 v52, v52\n"
  "v_rcp_f32 v53, v53\n"
  "v_rcp_f32 v54, v54\n"
  "v_rcp_f32 v55, v55\n"
  "s_add_u32 m0, s65, 5120\n"
  "s_nop 0\n"
  "s_nop 0\n"
  "buffer_load_dwordx4 v19, s[36:39], s72 offen sc1 lds\n"
  "v_pk_mul_f32 v[224:225], v[224:225], v[48:49]\n"
  "v_pk_mul_f32 v[226:227], v[226:227], v[50:51]\n"
  "v_pk_mul_f32 v[240:241], v[240:241], v[52:53]\n"
  "v_pk_mul_f32 v[242:243], v[242:243], v[54:55]\n"
  "v_cvt_pk_bf16_f32 v192, v224, v225\n"
  "v_cvt_pk_bf16_f32 v193, v226, v227\n"
  "v_cvt_pk_bf16_f32 v194, v240, v241\n"
  "v_cvt_pk_bf16_f32 v195, v242, v243\n"
  "s_add_u32 s71, s70, 4096\n"
  "buffer_store_dwordx4 v[192:195], v18, s[36:39], s71 offen sc1\n"
  "s_mul_i32 s72, s85, 5\n"
  "s_nop 0\n"
  "s_add_u32 s72, s72, 30720\n"
  "s_add_u32 s72, s72, s80\n"
  "s_nop 0\n"
  "s_add_u32 m0, s65, 6144\n"
  "s_nop 0\n"
  "s_nop 0\n"
  "buffer_load_dwordx4 v18, s[36:39], s72 offen sc1 lds\n"
  "v_pk_fma_f32 v[196:197], v[26:27], v[2:3], v[196:197] op_sel_hi:[0,1,1] neg_lo:[1,0,0] neg_hi:[1,0,0]\n"
  "v_pk_fma_f32 v[198:199], v[26:27], v[4:5], v[198:199] op_sel_hi:[0,1,1] neg_lo:[1,0,0] neg_hi:[1,0,0]\n"
  "v_pk_fma_f32 v[212:213], v[26:27], v[6:7], v[212:213] op_sel_hi:[0,1,1] neg_lo:[1,0,0] neg_hi:[1,0,0]\n"
  "v_pk_fma_f32 v[214:215], v[26:27], v[8:9], v[214:215] op_sel_hi:[0,1,1] neg_lo:[1,0,0] neg_hi:[1,0,0]\n"
  "v_pk_mul_f32 v[48:49], v[196:197], v[196:197]\n"
  "v_pk_mul_f32 v[50:51], v[198:199], v[198:199]\n"
  "v_pk_mul_f32 v[52:53], v[212:213], v[212:213]\n"
  "v_pk_mul_f32 v[54:55], v[214:215], v[214:215]\n"
  "v_pk_fma_f32 v[228:229], v[26:27], v[10:11], v[228:229] op_sel_hi:[0,1,1] neg_lo:[1,0,0] neg_hi:[1,0,0]\n"
  "v_pk_fma_f32 v[230:231], v[26:27], v[12:13], v[230:231] op_sel_hi:[0,1,1] neg_lo:[1,0,0] neg_hi:[1,0,0]\n"
  "v_pk_fma_f32 v[244:245], v[26:27], v[14:15], v[244:245] op_sel_hi:[0,1,1] neg_lo:[1,0,0] neg_hi:[1,0,0]\n"
  "v_pk_fma_f32 v[246:247], v[26:27], v[16:17], v[246:247] op_sel_hi:[0,1,1] neg_lo:[1,0,0] neg_hi:[1,0,0]\n"
  "v_pk_fma_f32 v[48:49], v[48:49], v[34:35], v[34:35] op_sel:[0,1,0] op_sel_hi:[1,1,0]\n"
  "v_pk_fma_f32 v[50:51], v[50:51], v[34:35], v[34:35] op_sel:[0,1,0] op_sel_hi:[1,1,0]\n"
  "v_pk_fma_f32 v[52:53], v[52:53], v[34:35], v[34:35] op_sel:[0,1,0] op_sel_hi:[1,1,0]\n"
  "v_pk_fma_f32 v[54:55], v[54:55], v[34:35], v[34:35] op_sel:[0,1,0] op_sel_hi:[1,1,0]\n"
  "s_add_u32 m0, s65, 6144\n"
  "s_nop 0\n"
  "s_nop 0\n"
  "buffer_load_dwordx4 v18, s[36:39], s72 offen offset:1024 sc1 lds\n"
  "v_pk_mul_f32 v[48:49], v[48:49], v[196:197]\n"
  "v_pk_mul_f32 v[50:51], v[50:51], v[198:199]\n"
  "v_pk_mul_f32 v[52:53], v[52:53], v[212:213]\n"
  "v_pk_mul_f32 v[54:55], v[54:55], v[214:215]\n"
  "v_exp_f32 v48, v48\n"
  "v_exp_f32 v49, v49\n"
  "v_exp_f32 v50, v50\n"
  "v_exp_f32 v51, v51\n"
  "v_exp_f32 v52, v52\n"
  "v_exp_f32 v53, v53\n"
  "v_exp_f32 v54, v54\n"
  "v_exp_f32 v55, v55\n"
  "s_add_u32 m0, s65, 6144\n"
  "s_nop 0\n"
  "s_nop 0\n"
  "buffer_load_dwordx4 v18, s[36:39], s72 offen offset:2048 sc1 lds\n"
  "v_pk_mul_f32 v[228:229], v[228:229], v[196:197]\n"
  "v_pk_mul_f32 v[230:231], v[230:231], v[198:199]\n"
  "v_pk_mul_f32 v[244:245], v[244:245], v[212:213]\n"
  "v_pk_mul_f32 v[246:247], v[246:247], v[214:215]\n"
  "v_pk_fma_f32 v[48:49], v[48:49], v[42:43], v[42:43] op_sel_hi:[1,0,0]\n"
  "v_pk_fma_f32 v[50:51], v[50:51], v[42:43], v[42:43] op_sel_hi:[1,0,0]\n"
  "v_pk_fma_f32 v[52:53], v[52:53], v[42:43], v[42:43] op_sel_hi:[1,0,0]\n"
  "v_pk_fma_f32 v[54:55], v[54:55], v[42:43], v[42:43] op_sel_hi:[1,0,0]\n"
  "v_rcp_f32 v48, v48\n"
  "v_rcp_f32 v49, v49\n"
  "v_rcp_f32 v50, v50\n"
  "v_rcp_f32 v51, v51\n"
  "v_rcp_f32 v52, v52\n"
  "v_rcp_f32 v53, v53\n"
  "v_rcp_f32 v54, v54\n"
  "v_rcp_f32 v55, v55\n"
  "s_add_u32 m0, s65, 6144\n"
  "s_nop 0\n"
  "s_nop 0\n"
  "buffer_load_dwordx4 v18, s[36:39], s72 offen offset:3072 sc1 lds\n"
  "v_pk_mul_f32 v[228:229], v[228:229], v[48:49]\n"
  "v_pk_mul_f32 v[230:231], v[230:231], v[50:51]\n"
  "v_pk_mul_f32 v[244:245], v[244:245], v[52:53]\n"
  "v_pk_mul_f32 v[246:247], v[246:247], v[54:55]\n"
  "v_cvt_pk_bf16_f32 v196, v228, v229\n"
  "v_cvt_pk_bf16_f32 v197, v230, v231\n"
  "v_cvt_pk_bf16_f32 v198, v244, v245\n"
  "v_cvt_pk_bf16_f32 v199, v246, v247\n"
  "s_add_u32 s71, s70, 53248\n"
  "buffer_store_dwordx4 v[196:199], v18, s[36:39], s71 offen sc1\n"
  "s_add_u32 m0, s65, 10240\n"
  "s_nop 0\n"
  "s_nop 0\n"
  "buffer_load_dwordx4 v19, s[36:39], s72 offen sc1 lds\n"
  "s_mul_i32 s72, s86, 5\n"
  "s_nop 0\n"
  "s_add_u32 s72, s72, 25600\n"
  "s_add_u32 s72, s72, s80\n"
  "s_nop 0\n"
  "s_add_u32 m0, s65, 11264\n"
  "s_nop 0\n"
  "s_nop 0\n"
  "buffer_load_dwordx4 v18, s[36:39], s72 offen sc1 lds\n"
  "v_pk_fma_f32 v[200:201], v[28:29], v[2:3], v[200:201] op_sel_hi:[0,1,1] neg_lo:[1,0,0] neg_hi:[1,0,0]\n"
  "v_pk_fma_f32 v[202:203], v[28:29], v[4:5], v[202:203] op_sel_hi:[0,1,1] neg_lo:[1,0,0] neg_hi:[1,0,0]\n"
  "v_pk_fma_f32 v[216:217], v[28:29], v[6:7], v[216:217] op_sel_hi:[0,1,1] neg_lo:[1,0,0] neg_hi:[1,0,0]\n"
  "v_pk_fma_f32 v[218:219], v[28:29], v[8:9], v[218:219] op_sel_hi:[0,1,1] neg_lo:[1,0,0] neg_hi:[1,0,0]\n"
  "v_pk_mul_f32 v[48:49], v[200:201], v[200:201]\n"
  "v_pk_mul_f32 v[50:51], v[202:203], v[202:203]\n"
  "v_pk_mul_f32 v[52:53], v[216:217], v[216:217]\n"
  "v_pk_mul_f32 v[54:55], v[218:219], v[218:219]\n"
  "v_pk_fma_f32 v[232:233], v[28:29], v[10:11], v[232:233] op_sel_hi:[0,1,1] neg_lo:[1,0,0] neg_hi:[1,0,0]\n"
  "v_pk_fma_f32 v[234:235], v[28:29], v[12:13], v[234:235] op_sel_hi:[0,1,1] neg_lo:[1,0,0] neg_hi:[1,0,0]\n"
  "v_pk_fma_f32 v[248:249], v[28:29], v[14:15], v[248:249] op_sel_hi:[0,1,1] neg_lo:[1,0,0] neg_hi:[1,0,0]\n"
  "v_pk_fma_f32 v[250:251], v[28:29], v[16:17], v[250:251] op_sel_hi:[0,1,1] neg_lo:[1,0,0] neg_hi:[1,0,0]\n"
  "v_pk_fma_f32 v[48:49], v[48:49], v[36:37], v[36:37] op_sel:[0,1,0] op_sel_hi:[1,1,0]\n"
  "v_pk_fma_f32 v[50:51], v[50:51], v[36:37], v[36:37] op_sel:[0,1,0] op_sel_hi:[1,1,0]\n"
  "v_pk_fma_f32 v[52:53], v[52:53], v[36:37], v[36:37] op_sel:[0,1,0] op_sel_hi:[1,1,0]\n"
  "v_pk_fma_f32 v[54:55], v[54:55], v[36:37], v[36:37] op_sel:[0,1,0] op_sel_hi:[1,1,0]\n"
  "s_add_u32 m0, s65, 11264\n"
  "s_nop 0\n"
  "s_nop 0\n"
  "buffer_load_dwordx4 v18, s[36:39], s72 offen offset:1024 sc1 lds\n"
  "v_pk_mul_f32 v[48:49], v[48:49], v[200:201]\n"
  "v_pk_mul_f32 v[50:51], v[50:51], v[202:203]\n"
  "v_pk_mul_f32 v[52:53], v[52:53], v[216:217]\n"
  "v_pk_mul_f32 v[54:55], v[54:55], v[218:219]\n"
  "v_exp_f32 v48, v48\n"
  "v_exp_f32 v49, v49\n"
  "v_exp_f32 v50, v50\n"
  "v_exp_f32 v51, v51\n"
  "v_exp_f32 v52, v52\n"
  "v_exp_f32 v53, v53\n"
  "v_exp_f32 v54, v54\n"
  "v_exp_f32 v55, v55\n"
  "s_add_u32 m0, s65, 11264\n"
  "s_nop 0\n"
  "s_nop 0\n"
  "buffer_load_dwordx4 v18, s[36:39], s72 offen offset:2048 sc1 lds\n"
  "v_pk_mul_f32 v[232:233], v[232:233], v[200:201]\n"
  "v_pk_mul_f32 v[234:235], v[234:235], v[202:203]\n"
  "v_pk_mul_f32 v[248:249], v[248:249], v[216:217]\n"
  "v_pk_mul_f32 v[250:251], v[250:251], v[218:219]\n"
  "v_pk_fma_f32 v[48:49], v[48:49], v[44:45], v[44:45] op_sel_hi:[1,0,0]\n"
  "v_pk_fma_f32 v[50:51], v[50:51], v[44:45], v[44:45] op_sel_hi:[1,0,0]\n"
  "v_pk_fma_f32 v[52:53], v[52:53], v[44:45], v[44:45] op_sel_hi:[1,0,0]\n"
  "v_pk_fma_f32 v[54:55], v[54:55], v[44:45], v[44:45] op_sel_hi:[1,0,0]\n"
  "v_rcp_f32 v48, v48\n"
  "v_rcp_f32 v49, v49\n"
  "v_rcp_f32 v50, v50\n"
  "v_rcp_f32 v51, v51\n"
  "v_rcp_f32 v52, v52\n"
  "v_rcp_f32 v53, v53\n"
  "v_rcp_f32 v54, v54\n"
  "v_rcp_f32 v55, v55\n"
  "s_add_u32 m0, s65, 11264\n"
  "s_nop 0\n"
  "s_nop 0\n"
  "buffer_load_dwordx4 v18, s[36:39], s72 offen offset:3072 sc1 lds\n"
  "v_pk_mul_f32 v[232:233], v[232:233], v[48:49]\n"
  "v_pk_mul_f32 v[234:235], v[234:235], v[50:51]\n"
  "v_pk_mul_f32 v[248:249], v[248:249], v[52:53]\n"
  "v_pk_mul_f32 v[250:251], v[250:251], v[54:55]\n"
  "v_cvt_pk_bf16_f32 v200, v232, v233\n"
  "v_cvt_pk_bf16_f32 v201, v234, v235\n"
  "v_cvt_pk_bf16_f32 v202, v248, v249\n"
  "v_cvt_pk_bf16_f32 v203, v250, v251\n"
  "s_add_u32 s71, s70, 102400\n"
  "buffer_store_dwordx4 v[200:203], v18, s[36:39], s71 offen sc1\n"
  "s_add_u32 m0, s65, 15360\n"
  "s_nop 0\n"
  "s_nop 0\n"
  "buffer_load_dwordx4 v19, s[36:39], s72 offen sc1 lds\n"
  "v_pk_fma_f32 v[204:205], v[30:31], v[2:3], v[204:205] op_sel_hi:[0,1,1] neg_lo:[1,0,0] neg_hi:[1,0,0]\n"
  "v_pk_fma_f32 v[206:207], v[30:31], v[4:5], v[206:207] op_sel_hi:[0,1,1] neg_lo:[1,0,0] neg_hi:[1,0,0]\n"
  "v_pk_fma_f32 v[220:221], v[30:31], v[6:7], v[220:221] op_sel_hi:[0,1,1] neg_lo:[1,0,0] neg_hi:[1,0,0]\n"
  "v_pk_fma_f32 v[222:223], v[30:31], v[8:9], v[222:223] op_sel_hi:[0,1,1] neg_lo:[1,0,0] neg_hi:[1,0,0]\n"
  "v_pk_mul_f32 v[48:49], v[204:205], v[204:205]\n"
  "v_pk_mul_f32 v[50:51], v[206:207], v[206:207]\n"
  "v_pk_mul_f32 v[52:53], v[220:221], v[220:221]\n"
  "v_pk_mul_f32 v[54:55], v[222:223], v[222:223]\n"
  "v_pk_fma_f32 v[236:237], v[30:31], v[10:11], v[236:237] op_sel_hi:[0,1,1] neg_lo:[1,0,0] neg_hi:[1,0,0]\n"
  "v_pk_fma_f32 v[238:239], v[30:31], v[12:13], v[238:239] op_sel_hi:[0,1,1] neg_lo:[1,0,0] neg_hi:[1,0,0]\n"
  "v_pk_fma_f32 v[252:253], v[30:31], v[14:15], v[252:253] op_sel_hi:[0,1,1] neg_lo:[1,0,0] neg_hi:[1,0,0]\n"
  "v_pk_fma_f32 v[254:255], v[30:31], v[16:17], v[254:255] op_sel_hi:[0,1,1] neg_lo:[1,0,0] neg_hi:[1,0,0]\n"
  "v_pk_fma_f32 v[48:49], v[48:49], v[38:39], v[38:39] op_sel:[0,1,0] op_sel_hi:[1,1,0]\n"
  "v_pk_fma_f32 v[50:51], v[50:51], v[38:39], v[38:39] op_sel:[0,1,0] op_sel_hi:[1,1,0]\n"
  "v_pk_fma_f32 v[52:53], v[52:53], v[38:39], v[38:39] op_sel:[0,1,0] op_sel_hi:[1,1,0]\n"
  "v_pk_fma_f32 v[54:55], v[54:55], v[38:39], v[38:39] op_sel:[0,1,0] op_sel_hi:[1,1,0]\n"
  "s_mul_i32 s72, s86, 5\n"
  "s_nop 0\n"
  "s_add_u32 s72, s72, 30720\n"
  "s_add_u32 s72, s72, s80\n"
  "s_nop 0\n"
  "s_add_u32 m0, s65, 16384\n"
  "s_nop 0\n"
  "s_nop 0\n"
  "buffer_load_dwordx4 v18, s[36:39], s72 offen sc1 lds\n"
  "v_pk_mul_f32 v[48:49], v[48:49], v[204:205]\n"
  "v_pk_mul_f32 v[50:51], v[50:51], v[206:207]\n"
  "v_pk_mul_f32 v[52:53], v[52:53], v[220:221]\n"
  "v_pk_mul_f32 v[54:55], v[54:55], v[222:223]\n"
  "v_exp_f32 v48, v48\n"
  "v_exp_f32 v49, v49\n"
  "v_exp_f32 v50, v50\n"
  "v_exp_f32 v51, v51\n"
  "v_exp_f32 v52, v52\n"
  "v_exp_f32 v53, v53\n"
  "v_exp_f32 v54, v54\n"
  "v_exp_f32 v55, v55\n"
  "s_add_u32 m0, s65, 16384\n"
  "s_nop 0\n"
  "s_nop 0\n"
  "buffer_load_dwordx4 v18, s[36:39], s72 offen offset:1024 sc1 lds\n"
  "v_pk_mul_f32 v[236:237], v[236:237], v[204:205]\n"
  "v_pk_mul_f32 v[238:239], v[238:239], v[206:207]\n"
  "v_pk_mul_f32 v[252:253], v[252:253], v[220:221]\n"
  "v_pk_mul_f32 v[254:255], v[254:255], v[222:223]\n"
  "v_pk_fma_f32 v[48:49], v[48:49], v[46:47], v[46:47] op_sel_hi:[1,0,0]\n"
  "v_pk_fma_f32 v[50:51], v[50:51], v[46:47], v[46:47] op_sel_hi:[1,0,0]\n"
  "v_pk_fma_f32 v[52:53], v[52:53], v[46:47], v[46:47] op_sel_hi:[1,0,0]\n"
  "v_pk_fma_f32 v[54:55], v[54:55], v[46:47], v[46:47] op_sel_hi:[1,0,0]\n"
  "v_rcp_f32 v48, v48\n"
  "v_rcp_f32 v49, v49\n"
  "v_rcp_f32 v50, v50\n"
  "v_rcp_f32 v51, v51\n"
  "v_rcp_f32 v52, v52\n"
  "v_rcp_f32 v53, v53\n"
  "v_rcp_f32 v54, v54\n"
  "v_rcp_f32 v55, v55\n"
  "s_add_u32 m0, s65, 16384\n"
  "s_nop 0\n"
  "s_nop 0\n"
  "buffer_load_dwordx4 v18, s[36:39], s72 offen offset:2048 sc1 lds\n"
  "v_pk_mul_f32 v[236:237], v[236:237], v[48:49]\n"
  "v_pk_mul_f32 v[238:239], v[238:239], v[50:51]\n"
  "v_pk_mul_f32 v[252:253], v[252:253], v[52:53]\n"
  "v_pk_mul_f32 v[254:255], v[254:255], v[54:55]\n"
  "v_cvt_pk_bf16_f32 v204, v236, v237\n"
  "v_cvt_pk_bf16_f32 v205, v238, v239\n"
  "v_cvt_pk_bf16_f32 v206, v252, v253\n"
  "v_cvt_pk_bf16_f32 v207, v254, v255\n"
  "s_add_u32 s71, s70, 151552\n"
  "buffer_store_dwordx4 v[204:207], v18, s[36:39], s71 offen sc1\n"
  "s_add_u32 m0, s65, 16384\n"
  "s_nop 0\n"
  "s_nop 0\n"
  "buffer_load_dwordx4 v18, s[36:39], s72 offen offset:3072 sc1 lds\n"
  "s_add_u32 m0, s65, 20480\n"
  "s_nop 0\n"
  "s_nop 0\n"
  "buffer_load_dwordx4 v19, s[36:39], s72 offen sc1 lds\n"
  "s_lshl_b32 s70, s47, 7\n"
  "s_lshl_b32 s71, s48, 3\n"
  "s_add_u32 s70, s70, s71\n"
  "s_nop 0\n"
  "s_add_u32 s70, s70, 12288\n"
  "s_waitcnt vmcnt(2)\n"
  "s_barrier\n"
  "v_mov_b32 v20, s44\n"
  "v_mov_b32 v21, s45\n"
  "s_mov_b64 exec, s[88:89]\n"
  "s_nop 0\n"
  "buffer_store_dwordx2 v[20:21], off, s[36:39], s70 sc1\n"
  "s_mov_b64 exec, -1\n"
  "s_branch .LP2\n"
  ".LQ:\n"
  "v_lshrrev_b32 v20, 3, v0\n"
  "v_and_b32 v21, 7, v0\n"
  "v_lshrrev_b32 v22, 4, v0\n"
  "v_and_b32 v23, 15, v0\n"
  "s_lshl_b32 s66, s46, 6\n"
  "s_add_u32 s66, s66, 0\n"
  "v_add_u32 v24, s66, v0\n"
  "s_movk_i32 s67, 2185\n"
  "v_mul_u32_u24 v25, s67, v24\n"
  "v_lshrrev_b32 v25, 16, v25\n"
  "v_mul_u32_u24 v26, 30, v25\n"
  "v_sub_u32 v26, v24, v26\n"
  "v_mul_u32_u24 v27, 26, v26\n"
  "v_lshrrev_b32 v27, 8, v27\n"
  "v_mul_u32_u24 v28, 10, v27\n"
  "v_sub_u32 v28, v26, v28\n"
  "v_lshlrev_b32 v28, 4, v28\n"
  "s_movk_i32 s67, 1280\n"
  "v_mad_u32_u24 v28, v27, s67, v28\n"
  "v_and_b32 v29, 0x13, v25\n"
  "v_and_b32 v30, 4, v25\n"
  "v_lshl_or_b32 v29, v30, 1, v29\n"
  "v_and_b32 v30, 8, v25\n"
  "v_lshrrev_b32 v30, 1, v30\n"
  "v_or_b32 v29, v29, v30\n"
  "s_movk_i32 s67, 3840\n"
  "v_mad_u32_u24 v2, v29, s67, v28\n"
  "s_lshl_b32 s66, s46, 6\n"
  "s_nop 0\n"
  "s_add_u32 s66, s66, 256\n"
  "v_add_u32 v24, s66, v0\n"
  "s_movk_i32 s67, 2185\n"
  "v_mul_u32_u24 v25, s67, v24\n"
  "v_lshrrev_b32 v25, 16, v25\n"
  "v_mul_u32_u24 v26, 30, v25\n"
  "v_sub_u32 v26, v24, v26\n"
  "v_mul_u32_u24 v27, 26, v26\n"
  "v_lshrrev_b32 v27, 8, v27\n"
  "v_mul_u32_u24 v28, 10, v27\n"
  "v_sub_u32 v28, v26, v28\n"
  "v_lshlrev_b32 v28, 4, v28\n"
  "s_movk_i32 s67, 1280\n"
  "v_mad_u32_u24 v28, v27, s67, v28\n"
  "v_and_b32 v29, 0x13, v25\n"
  "v_and_b32 v30, 4, v25\n"
  "v_lshl_or_b32 v29, v30, 1, v29\n"
  "v_and_b32 v30, 8, v25\n"
  "v_lshrrev_b32 v30, 1, v30\n"
  "v_or_b32 v29, v29, v30\n"
  "s_movk_i32 s67, 3840\n"
  "v_mad_u32_u24 v3, v29, s67, v28\n"
  "s_lshl_b32 s66, s46, 6\n"
  "s_nop 0\n"
  "s_add_u32 s66, s66, 512\n"
  "v_add_u32 v24, s66, v0\n"
  "s_movk_i32 s67, 2185\n"
  "v_mul_u32_u24 v25, s67, v24\n"
  "v_lshrrev_b32 v25, 16, v25\n"
  "v_mul_u32_u24 v26, 30, v25\n"
  "v_sub_u32 v26, v24, v26\n"
  "v_mul_u32_u24 v27, 26, v26\n"
  "v_lshrrev_b32 v27, 8, v27\n"
  "v_mul_u32_u24 v28, 10, v27\n"
  "v_sub_u32 v28, v26, v28\n"
  "v_lshlrev_b32 v28, 4, v28\n"
  "s_movk_i32 s67, 1280\n"
  "v_mad_u32_u24 v28, v27, s67, v28\n"
  "v_and_b32 v29, 0x13, v25\n"
  "v_and_b32 v30, 4, v25\n"
  "v_lshl_or_b32 v29, v30, 1, v29\n"
  "v_and_b32 v30, 8, v25\n"
  "v_lshrrev_b32 v30, 1, v30\n"
  "v_or_b32 v29, v29, v30\n"
  "s_movk_i32 s67, 3840\n"
  "v_mad_u32_u24 v4, v29, s67, v28\n"
  "s_lshl_b32 s66, s46, 6\n"
  "s_nop 0\n"
  "s_add_u32 s66, s66, 768\n"
  "v_add_u32 v24, s66, v0\n"
  "s_movk_i32 s67, 2185\n"
  "v_mul_u32_u24 v25, s67, v24\n"
  "v_lshrrev_b32 v25, 16, v25\n"
  "v_mul_u32_u24 v26, 30, v25\n"
  "v_sub_u32 v26, v24, v26\n"
  "v_mul_u32_u24 v27, 26, v26\n"
  "v_lshrrev_b32 v27, 8, v27\n"
  "v_mul_u32_u24 v28, 10, v27\n"
  "v_sub_u32 v28, v26, v28\n"
  "v_lshlrev_b32 v28, 4, v28\n"
  "s_movk_i32 s67, 1280\n"
  "v_mad_u32_u24 v28, v27, s67, v28\n"
  "v_and_b32 v29, 0x13, v25\n"
  "v_and_b32 v30, 4, v25\n"
  "v_lshl_or_b32 v29, v30, 1, v29\n"
  "v_and_b32 v30, 8, v25\n"
  "v_lshrrev_b32 v30, 1, v30\n"
  "v_or_b32 v29, v29, v30\n"
  "s_movk_i32 s67, 3840\n"
  "v_mad_u32_u24 v5, v29, s67, v28\n"
  "v_cmp_gt_u32 vcc, 32, v25\n"
  "s_nop 0\n"
  "v_mov_b32 v30, 0x7fff0000\n"
  "s_nop 0\n"
  "v_cndmask_b32 v5, v30, v5, vcc\n"
  "s_lshl_b32 s68, s47, 7\n"
  "s_and_b32 s69, s46, 1\n"
  "s_lshr_b32 s70, s46, 1\n"
  "s_lshl_b32 s71, s69, 3\n"
  "v_add_u32 v24, s71, v20\n"
  "s_lshl_b32 s71, s69, 2\n"
  "v_xor_b32 v25, s71, v22\n"
  "v_xor_b32 v25, v25, v21\n"
  "v_lshlrev_b32 v25, 4, v25\n"
  "s_movk_i32 s67, 1280\n"
  "s_add_u32 s66, s68, s70\n"
  "s_add_u32 s66, s66, 0\n"
  "v_lshlrev_b32 v26, 1, v24\n"
  "v_add_u32 v26, s66, v26\n"
  "v_mad_u32_u24 v6, v26, s67, v25\n"
  "s_add_u32 s66, s68, s70\n"
  "s_add_u32 s66, s66, 32\n"
  "v_lshlrev_b32 v26, 1, v24\n"
  "v_add_u32 v26, s66, v26\n"
  "v_mad_u32_u24 v7, v26, s67, v25\n"
  "s_add_u32 s66, s68, s70\n"
  "s_add_u32 s66, s66, 64\n"
  "v_lshlrev_b32 v26, 1, v24\n"
  "v_add_u32 v26, s66, v26\n"
  "v_mad_u32_u24 v8, v26, s67, v25\n"
  "s_add_u32 s66, s68, s70\n"
  "s_nop 0\n"
  "s_add_u32 s66, s66, 96\n"
  "v_lshlrev_b32 v26, 1, v24\n"
  "v_add_u32 v26, s66, v26\n"
  "v_mad_u32_u24 v9, v26, s67, v25\n"
  "s_mul_i32 s66, s69, 17\n"
  "s_add_u32 s66, s66, 15\n"
  "s_sub_i32 s66, s68, s66\n"
  "v_lshlrev_b32 v26, 1, v24\n"
  "v_add_u32 v26, s66, v26\n"
  "v_max_i32 v26, 0, v26\n"
  "v_mad_u32_u24 v10, v26, s67, v25\n"
  "s_cmp_lt_u32 s46, 2\n"
  "s_nop 0\n"
  "s_cselect_b32 s74, 0xffffff00, 0\n"
  "s_cselect_b32 s75, -1, 0\n"
  "s_lshl_b32 s66, s50, 3\n"
  "s_lshl_b32 s67, s46, 1\n"
  "s_add_u32 s66, s66, s67\n"
  "v_lshrrev_b32 v46, 5, v0\n"
  "v_add_u32 v46, s66, v46\n"
  "v_and_b32 v44, 31, v0\n"
  "v_cmp_gt_u32 vcc, 16, v44\n"
  "v_lshlrev_b32 v44, 4, v44\n"
  "s_movk_i32 s67, 1280\n"
  "v_mad_u32_u24 v44, v46, s67, v44\n"
  "v_mov_b32 v45, 0x7fff0000\n"
  "v_cndmask_b32 v45, v45, v44, vcc\n"
  "s_waitcnt lgkmcnt(0)\n"
  "s_load_dwordx2 s[44:45], s[22:23], 0x0\n"
  "s_mov_b32 s24, s8\n"
  "s_nop 0\n"
  "s_and_b32 s25, s9, 0xffff\n"
  "s_mov_b32 s26, 2621440\n"
  "s_mov_b32 s27, 0x20000\n"
  "buffer_load_dwordx4 v[32:35], v44, s[24:27], 0 offen\n"
  "buffer_load_dwordx4 v[36:39], v44, s[24:27], 0 offen offset:512\n"
  "buffer_load_dwordx4 v[40:43], v45, s[24:27], 0 offen offset:1024\n"
  "s_mul_i32 s66, s48, 160\n"
  "s_mov_b32 s28, s10\n"
  "s_nop 0\n"
  "s_and_b32 s29, s11, 0xffff\n"
  "s_mov_b32 s30, 2457600\n"
  "s_mov_b32 s31, 0x20000\n"
  "s_add_u32 s28, s28, s66\n"
  "s_addc_u32 s29, s29, 0\n"
  "s_mul_i32 s62, s52, 1920\n"
  "s_add_u32 m0, s64, 0\n"
  "s_nop 0\n"
  "buffer_load_dwordx4 v2, s[28:31], s62 offen lds\n"
  "s_add_u32 m0, s64, 4096\n"
  "s_nop 0\n"
  "s_nop 0\n"
  "buffer_load_dwordx4 v3, s[28:31], s62 offen lds\n"
  "s_add_u32 m0, s64, 8192\n"
  "s_nop 0\n"
  "s_nop 0\n"
  "buffer_load_dwordx4 v4, s[28:31], s62 offen lds\n"
  "s_add_u32 m0, s64, 12288\n"
  "s_nop 0\n"
  "s_nop 0\n"
  "buffer_load_dwordx4 v5, s[28:31], s62 offen lds\n"
  "s_add_u32 m0, s64, 81920\n"
  "s_nop 0\n"
  "s_nop 0\n"
  "buffer_load_dwordx4 v6, s[24:27], s52 offen lds\n"
  "s_add_u32 m0, s64, 86016\n"
  "s_nop 0\n"
  "s_nop 0\n"
  "buffer_load_dwordx4 v7, s[24:27], s52 offen lds\n"
  "s_add_u32 m0, s64, 90112\n"
  "s_nop 0\n"
  "s_nop 0\n"
  "buffer_load_dwordx4 v8, s[24:27], s52 offen lds\n"
  "s_add_u32 m0, s64, 94208\n"
  "s_nop 0\n"
  "s_nop 0\n"
  "buffer_load_dwordx4 v9, s[24:27], s52 offen lds\n"
  "s_add_u32 m0, s64, 98304\n"
  "s_mov_b64 exec, s[74:75]\n"
  "s_nop 0\n"
  "buffer_load_dwordx4 v10, s[24:27], s52 offen lds\n"
  "s_mov_b64 exec, -1\n"
  "s_nop 0\n"
  "s_add_u32 s63, s62, 122880\n"
  "s_add_u32 m0, s64, 16384\n"
  "s_nop 0\n"
  "s_nop 0\n"
  "buffer_load_dwordx4 v2, s[28:31], s63 offen lds\n"
  "s_add_u32 m0, s64, 20480\n"
  "s_nop 0\n"
  "s_nop 0\n"
  "buffer_load_dwordx4 v3, s[28:31], s63 offen lds\n"
  "s_add_u32 m0, s64, 24576\n"
  "s_nop 0\n"
  "s_nop 0\n"
  "buffer_load_dwordx4 v4, s[28:31], s63 offen lds\n"
  "s_add_u32 m0, s64, 28672\n"
  "s_nop 0\n"
  "s_nop 0\n"
  "buffer_load_dwordx4 v5, s[28:31], s63 offen lds\n"
  "s_mov_b32 s36, s22\n"
  "s_nop 0\n"
  "s_and_b32 s37, s23, 0xffff\n"
  "s_mov_b32 s38, 16777216\n"
  "s_mov_b32 s39, 0x20000\n"
  "s_waitcnt vmcnt(13)\n"
  "v_mov_b32 v24, 0\n"
  "v_mov_b32 v25, 0\n"
  "v_mov_b32 v26, 0\n"
  "v_mov_b32_e64 v27, 0\n"
  "v_mov_b32 v28, 0x3f803f80\n"
  "v_dot2c_f32_bf16 v24, v28, v32\n"
  "v_dot2c_f32_bf16 v26, v32, v32\n"
  "v_dot2c_f32_bf16 v25, v28, v33\n"
  "v_dot2c_f32_bf16 v27, v33, v33\n"
  "v_dot2c_f32_bf16 v24, v28, v34\n"
  "v_dot2c_f32_bf16 v26, v34, v34\n"
  "v_dot2c_f32_bf16 v25, v28, v35\n"
  "v_dot2c_f32_bf16 v27, v35, v35\n"
  "v_dot2c_f32_bf16 v24, v28, v36\n"
  "v_dot2c_f32_bf16 v26, v36, v36\n"
  "v_dot2c_f32_bf16 v25, v28, v37\n"
  "v_dot2c_f32_bf16 v27, v37, v37\n"
  "v_dot2c_f32_bf16 v24, v28, v38\n"
  "v_dot2c_f32_bf16 v26, v38, v38\n"
  "v_dot2c_f32_bf16 v25, v28, v39\n"
  "v_dot2c_f32_bf16 v27, v39, v39\n"
  "v_dot2c_f32_bf16 v24, v28, v40\n"
  "v_dot2c_f32_bf16 v26, v40, v40\n"
  "v_dot2c_f32_bf16 v25, v28, v41\n"
  "v_dot2c_f32_bf16 v27, v41, v41\n"
  "v_dot2c_f32_bf16 v24, v28, v42\n"
  "v_dot2c_f32_bf16 v26, v42, v42\n"
  "v_dot2c_f32_bf16 v25, v28, v43\n"
  "v_dot2c_f32_bf16 v27, v43, v43\n"
  "s_nop 1\n"
  "v_add_f32 v24, v24, v25\n"
  "v_add_f32 v26, v26, v27\n"
  "s_nop 0\n"
  "v_add_f32_dpp v24, v24, v24 row_ror:8 row_mask:0xf bank_mask:0xf\n"
  "v_add_f32_dpp v26, v26, v26 row_ror:8 row_mask:0xf bank_mask:0xf\n"
  "s_nop 0\n"
  "s_nop 0\n"
  "v_add_f32_dpp v24, v24, v24 row_ror:4 row_mask:0xf bank_mask:0xf\n"
  "v_add_f32_dpp v26, v26, v26 row_ror:4 row_mask:0xf bank_mask:0xf\n"
  "s_nop 0\n"
  "s_nop 0\n"
  "v_add_f32_dpp v24, v24, v24 row_ror:2 row_mask:0xf bank_mask:0xf\n"
  "v_add_f32_dpp v26, v26, v26 row_ror:2 row_mask:0xf bank_mask:0xf\n"
  "s_nop 0\n"
  "s_nop 0\n"
  "v_add_f32_dpp v24, v24, v24 row_ror:1 row_mask:0xf bank_mask:0xf\n"
  "v_add_f32_dpp v26, v26, v26 row_ror:1 row_mask:0xf bank_mask:0xf\n"
  "v_mov_b32 v25, v24\n"
  "v_mov_b32 v27, v26\n"
  "s_nop 0\n"
  "v_permlane16_swap_b32 v24, v25\n"
  "v_permlane16_swap_b32 v26, v27\n"
  "v_add_f32 v24, v24, v25\n"
  "v_add_f32_e64 v26, v26, v27\n"
  "v_mul_f32 v24, 0x3acccccd, v24\n"
  "v_mul_f32 v26, 0x3acccccd, v26\n"
  "v_fma_f32 v26, -v24, v24, v26\n"
  "v_max_f32_e64 v26, 0, v26\n"
  "v_add_f32 v26, 0x3727c5ac, v26\n"
  "v_rsq_f32_e64 v25, v26\n"
  "v_and_b32 v27, 0xffffffe0, v46\n"
  "v_and_b32_e64 v28, 1, v46\n"
  "v_lshl_add_u32 v27, v28, 4, v27\n"
  "v_bfe_u32 v28, v46, 1, 4\n"
  "v_add_u32 v27, v27, v28\n"
  "v_lshlrev_b32 v27, 3, v27\n"
  "s_mov_b32 s66, 32768\n"
  "s_mov_b32 exec_lo, 1\n"
  "s_mov_b32 exec_hi, 1\n"
  "buffer_store_dwordx2 v[24:25], v27, s[36:39], s66 offen sc1\n"
  "s_mov_b64 exec, -1\n"
  "s_nop 0\n"
  "s_add_u32 m0, s64, 102400\n"
  "s_nop 0\n"
  "s_nop 0\n"
  "buffer_load_dwordx4 v6, s[24:27], s53 offen lds\n"
  "s_add_u32 m0, s64, 106496\n"
  "s_nop 0\n"
  "s_nop 0\n"
  "buffer_load_dwordx4 v7, s[24:27], s53 offen lds\n"
  "s_add_u32 m0, s64, 110592\n"
  "s_nop 0\n"
  "s_nop 0\n"
  "buffer_load_dwordx4 v8, s[24:27], s53 offen lds\n"
  "s_add_u32 m0, s64, 114688\n"
  "s_nop 0\n"
  "s_nop 0\n"
  "buffer_load_dwordx4 v9, s[24:27], s53 offen lds\n"
  "s_add_u32 m0, s64, 118784\n"
  "s_mov_b64 exec, s[74:75]\n"
  "s_nop 0\n"
  "buffer_load_dwordx4 v10, s[24:27], s53 offen lds\n"
  "s_mov_b64 exec, -1\n"
  "s_nop 0\n"
  "s_mul_i32 s62, s53, 1920\n"
  "s_add_u32 m0, s64, 32768\n"
  "s_nop 0\n"
  "s_nop 0\n"
  "buffer_load_dwordx4 v2, s[28:31], s62 offen lds\n"
  "s_add_u32 m0, s64, 36864\n"
  "s_nop 0\n"
  "s_nop 0\n"
  "buffer_load_dwordx4 v3, s[28:31], s62 offen lds\n"
  "s_add_u32 m0, s64, 40960\n"
  "s_nop 0\n"
  "s_nop 0\n"
  "buffer_load_dwordx4 v4, s[28:31], s62 offen lds\n"
  "s_add_u32 m0, s64, 45056\n"
  "s_nop 0\n"
  "s_nop 0\n"
  "buffer_load_dwordx4 v5, s[28:31], s62 offen lds\n"
  "s_add_u32 s63, s62, 122880\n"
  "s_add_u32 m0, s64, 49152\n"
  "s_nop 0\n"
  "s_nop 0\n"
  "buffer_load_dwordx4 v2, s[28:31], s63 offen lds\n"
  "s_add_u32 m0, s64, 53248\n"
  "s_nop 0\n"
  "s_nop 0\n"
  "buffer_load_dwordx4 v3, s[28:31], s63 offen lds\n"
  "s_add_u32 m0, s64, 57344\n"
  "s_nop 0\n"
  "s_nop 0\n"
  "buffer_load_dwordx4 v4, s[28:31], s63 offen lds\n"
  "s_add_u32 m0, s64, 61440\n"
  "s_nop 0\n"
  "s_nop 0\n"
  "buffer_load_dwordx4 v5, s[28:31], s63 offen lds\n"
  "s_add_u32 m0, s64, 122880\n"
  "s_nop 0\n"
  "s_nop 0\n"
  "buffer_load_dwordx4 v6, s[24:27], s54 offen lds\n"
  "s_add_u32 m0, s64, 126976\n"
  "s_nop 0\n"
  "s_nop 0\n"
  "buffer_load_dwordx4 v7, s[24:27], s54 offen lds\n"
  "s_mul_i32 s62, s54, 1920\n"
  "s_add_u32 m0, s64, 65536\n"
  "s_nop 0\n"
  "s_nop 0\n"
  "buffer_load_dwordx4 v2, s[28:31], s62 offen lds\n"
  "s_add_u32 m0, s64, 69632\n"
  "s_nop 0\n"
  "s_nop 0\n"
  "buffer_load_dwordx4 v3, s[28:31], s62 offen lds\n"
  "s_add_u32 m0, s64, 73728\n"
  "s_nop 0\n"
  "s_nop 0\n"
  "buffer_load_dwordx4 v4, s[28:31], s62 offen lds\n"
  "s_add_u32 m0, s64, 77824\n"
  "s_nop 0\n"
  "s_nop 0\n"
  "buffer_load_dwordx4 v5, s[28:31], s62 offen lds\n"
  "s_lshr_b32 s66, s50, 4\n"
  "s_lshl_b32 s66, s66, 3\n"
  "s_and_b32 s67, s50, 7\n"
  "s_or_b32 s66, s66, s67\n"
  "s_lshl_b32 s67, s46, 7\n"
  "s_add_u32 s66, s66, s67\n"
  "s_add_u32 s66, s66, 384\n"
  "s_cmp_lt_u32 s66, 544\n"
  "s_cselect_b32 s90, -1, 0\n"
  "s_cselect_b32 s91, -1, 0\n"
  "s_cselect_b32 s66, s66, 0\n"
  "s_and_b32 s67, s66, 7\n"
  "s_lshr_b32 s68, s66, 3\n"
  "s_nop 0\n"
  "s_mul_i32 s69, s68, 241\n"
  "s_lshr_b32 s69, s69, 12\n"
  "s_mul_i32 s70, s69, 17\n"
  "s_sub_u32 s70, s68, s70\n"
  "s_nop 0\n"
  "s_mul_i32 s76, s67, 68\n"
  "s_add_u32 s76, s76, s68\n"
  "s_nop 0\n"
  "s_mul_i32 s76, s76, 5120\n"
  "s_add_u32 s76, s76, 262144\n"
  "s_mul_i32 s2, s69, 12\n"
  "s_add_u32 s2, s2, s70\n"
  "s_sub_u32 s2, s2, 5\n"
  "s_lshl_b32 s2, s2, 5\n"
  "s_mov_b32 s3, 16\n"
  "s_mov_b32 s6, 8\n"
  "s_mov_b32 s7, 4\n"
  "s_nop 0\n"
  "s_mul_i32 s4, s69, 160\n"
  "s_mul_i32 s5, s70, 40\n"
  "s_add_u32 s5, s5, s4\n"
  "s_add_u32 s4, s4, 16\n"
  "s_cmp_lt_u32 s70, 4\n"
  "s_cselect_b32 s2, s5, s2\n"
  "s_cselect_b32 s3, 4, s3\n"
  "s_cselect_b32 s6, 20, s6\n"
  "s_cselect_b32 s7, 8, s7\n"
  "s_cmp_eq_u32 s70, 4\n"
  "s_cselect_b32 s2, s4, s2\n"
  "s_cselect_b32 s3, 80, s3\n"
  "s_cselect_b32 s6, 40, s6\n"
  "s_cselect_b32 s7, 20, s7\n"
  "s_mov_b32 s42, 1966080\n"
  "s_cmp_lt_u32 s70, 5\n"
  "s_cselect_b32 s40, s12, s18\n"
  "s_cselect_b32 s41, s13, s19\n"
  "s_nop 0\n"
  "s_cselect_b32 s42, 819200, s42\n"
  "s_and_b32 s41, s41, 0xffff\n"
  "s_mov_b32 s43, 0x20000\n"
  "s_mul_i32 s67, s67, 160\n"
  "s_movk_i32 s68, 1280\n"
  "s_movk_i32 s69, 6554\n"
  "s_mul_i32 s71, s46, 5120\n"
  "s_add_u32 s71, s71, 143360\n"
  "v_add_u32 v24, 0, v0\n"
  "v_mul_u32_u24 v25, s69, v24\n"
  "v_lshrrev_b32 v25, 16, v25\n"
  "v_mul_u32_u24 v26, 10, v25\n"
  "v_sub_u32 v26, v24, v26\n"
  "v_and_b32 v27, 3, v25\n"
  "v_add_u32_e64 v27, s2, v27\n"
  "v_bfe_u32 v28, v25, 2, 1\n"
  "v_mad_u32_u24 v27, v28, s3, v27\n"
  "v_bfe_u32 v28, v25, 4, 1\n"
  "v_mad_u32_u24 v27, v28, s6, v27\n"
  "v_bfe_u32 v28, v25, 3, 1\n"
  "v_mad_u32_u24 v27, v28, s7, v27\n"
  "v_lshlrev_b32_e64 v26, 4, v26\n"
  "v_mad_u32_u24 v27, v27, s68, v26\n"
  "s_add_u32 m0, s71, 0\n"
  "s_mov_b64 exec, s[90:91]\n"
  "buffer_load_dwordx4 v27, s[40:43], s67 offen lds\n"
  "s_mov_b64 exec, -1\n"
  "v_add_u32 v24, 64, v0\n"
  "v_mul_u32_u24 v25, s69, v24\n"
  "v_lshrrev_b32 v25, 16, v25\n"
  "v_mul_u32_u24 v26, 10, v25\n"
  "v_sub_u32 v26, v24, v26\n"
  "v_and_b32 v27, 3, v25\n"
  "v_add_u32 v27, s2, v27\n"
  "v_bfe_u32 v28, v25, 2, 1\n"
  "v_mad_u32_u24 v27, v28, s3, v27\n"
  "v_bfe_u32 v28, v25, 4, 1\n"
  "v_mad_u32_u24 v27, v28, s6, v27\n"
  "v_bfe_u32 v28, v25, 3, 1\n"
  "v_mad_u32_u24 v27, v28, s7, v27\n"
  "v_lshlrev_b32_e64 v26, 4, v26\n"
  "v_mad_u32_u24 v27, v27, s68, v26\n"
  "s_add_u32 m0, s71, 1024\n"
  "s_mov_b64 exec, s[90:91]\n"
  "s_nop 0\n"
  "buffer_load_dwordx4 v27, s[40:43], s67 offen lds\n"
  "s_mov_b64 exec, -1\n"
  "s_nop 0\n"
  "v_add_u32 v24, 128, v0\n"
  "v_mul_u32_u24 v25, s69, v24\n"
  "v_lshrrev_b32 v25, 16, v25\n"
  "v_mul_u32_u24 v26, 10, v25\n"
  "v_sub_u32 v26, v24, v26\n"
  "v_and_b32 v27, 3, v25\n"
  "v_add_u32 v27, s2, v27\n"
  "v_bfe_u32 v28, v25, 2, 1\n"
  "v_mad_u32_u24 v27, v28, s3, v27\n"
  "v_bfe_u32 v28, v25, 4, 1\n"
  "v_mad_u32_u24 v27, v28, s6, v27\n"
  "v_bfe_u32 v28, v25, 3, 1\n"
  "v_mad_u32_u24 v27, v28, s7, v27\n"
  "v_lshlrev_b32_e64 v26, 4, v26\n"
  "v_mad_u32_u24 v27, v27, s68, v26\n"
  "s_add_u32 m0, s71, 2048\n"
  "s_mov_b64 exec, s[90:91]\n"
  "s_nop 0\n"
  "buffer_load_dwordx4 v27, s[40:43], s67 offen lds\n"
  "s_mov_b64 exec, -1\n"
  "s_nop 0\n"
  "v_add_u32 v24, 192, v0\n"
  "v_mul_u32_u24 v25, s69, v24\n"
  "v_lshrrev_b32 v25, 16, v25\n"
  "v_mul_u32_u24 v26, 10, v25\n"
  "v_sub_u32 v26, v24, v26\n"
  "v_and_b32 v27, 3, v25\n"
  "v_add_u32 v27, s2, v27\n"
  "v_bfe_u32 v28, v25, 2, 1\n"
  "v_mad_u32_u24 v27, v28, s3, v27\n"
  "v_bfe_u32 v28, v25, 4, 1\n"
  "v_mad_u32_u24 v27, v28, s6, v27\n"
  "v_bfe_u32 v28, v25, 3, 1\n"
  "v_mad_u32_u24 v27, v28, s7, v27\n"
  "v_lshlrev_b32_e64 v26, 4, v26\n"
  "v_mad_u32_u24 v27, v27, s68, v26\n"
  "s_add_u32 m0, s71, 3072\n"
  "s_mov_b64 exec, s[90:91]\n"
  "s_nop 0\n"
  "buffer_load_dwordx4 v27, s[40:43], s67 offen lds\n"
  "s_mov_b64 exec, -1\n"
  "s_nop 0\n"
  "v_add_u32 v24, 256, v0\n"
  "v_mul_u32_u24 v25, s69, v24\n"
  "v_lshrrev_b32 v25, 16, v25\n"
  "v_mul_u32_u24 v26, 10, v25\n"
  "v_sub_u32 v26, v24, v26\n"
  "v_and_b32 v27, 3, v25\n"
  "v_add_u32 v27, s2, v27\n"
  "v_bfe_u32 v28, v25, 2, 1\n"
  "v_mad_u32_u24 v27, v28, s3, v27\n"
  "v_bfe_u32 v28, v25, 4, 1\n"
  "v_mad_u32_u24 v27, v28, s6, v27\n"
  "v_bfe_u32 v28, v25, 3, 1\n"
  "v_mad_u32_u24 v27, v28, s7, v27\n"
  "v_lshlrev_b32_e64 v26, 4, v26\n"
  "v_mad_u32_u24 v27, v27, s68, v26\n"
  "s_add_u32 m0, s71, 4096\n"
  "s_mov_b64 exec, s[90:91]\n"
  "s_nop 0\n"
  "buffer_load_dwordx4 v27, s[40:43], s67 offen lds\n"
  "s_mov_b64 exec, -1\n"
  "s_nop 0\n"
  "v_bfe_u32 v24, v0, 2, 2\n"
  "v_and_b32 v25, 3, v0\n"
  "v_lshlrev_b32 v18, 4, v0\n"
  "v_add_u32 v19, 4096, v18\n"
  "v_mov_b32 v26, 0x7fff0000\n"
  "v_cndmask_b32_e64 v48, v26, v18, s[90:91]\n"
  "s_min_u32 s71, s71, 153600\n"
  "v_lshlrev_b32 v26, 3, v22\n"
  "v_add_u32 v26, v26, v24\n"
  "s_movk_i32 s66, 160\n"
  "v_mul_u32_u24 v26, s66, v26\n"
  "v_mul_u32_u24 v27, 40, v25\n"
  "s_nop 0\n"
  "v_add3_u32 v49, v26, v27, s71\n"
  "v_mov_b32 v52, 0x3f803f80\n"
  "v_mov_b32 v53, 0x3f803f80\n"
  "v_mov_b32 v54, 0x3f803f80\n"
  "v_mov_b32 v55, 0x3f803f80\n"
  "v_and_b32_e64 v26, 1, v22\n"
  "v_lshl_or_b32 v26, v26, 2, v24\n"
  "v_lshrrev_b32_e64 v27, 1, v22\n"
  "v_lshl_or_b32 v26, v27, 4, v26\n"
  "s_movk_i32 s67, 480\n"
  "s_mul_i32 s66, s46, 40\n"
  "v_mul_u32_u24 v26, s67, v26\n"
  "v_add_u32 v26, s66, v26\n"
  "v_lshl_add_u32 v12, v25, 3, v26\n"
  "v_add_u32 v13, 0x10000, v12\n"
  "v_min_u32 v27, 2, v25\n"
  "s_movk_i32 s67, 160\n"
  "v_mad_u32_u24 v14, v27, s67, v26\n"
  "v_add_u32_e64 v14, 32, v14\n"
  "v_add_u32 v15, 0x10000, v14\n"
  "s_mov_b32 s66, 81920\n"
  "v_min_u32 v27, 2, v22\n"
  "s_movk_i32 s67, 20480\n"
  "v_mul_u32_u24 v27, s67, v27\n"
  "s_nop 0\n"
  "v_bfe_u32 v28, v0, 3, 1\n"
  "v_lshl_add_u32 v27, v28, 10, v27\n"
  "v_lshl_add_u32 v27, v21, 4, v27\n"
  "v_add_u32 v27, 98304, v27\n"
  "ds_write_b128 v27, v[52:55]\n"
  "v_lshrrev_b32 v26, 1, v23\n"
  "v_xor_b32 v26, v26, v22\n"
  "v_lshlrev_b32_e64 v26, 4, v26\n"
  "v_lshl_add_u32 v26, v23, 7, v26\n"
  "v_add_u32 v16, s66, v26\n"
  "v_xor_b32 v26, 64, v26\n"
  "v_add_u32 v17, s66, v26\n"
  "s_waitcnt lgkmcnt(0)\n"
  "s_add_u32 s44, s44, 0x7f4a7c15\n"
  "s_addc_u32 s45, s45, 0x9e3779b9\n"
  "s_waitcnt vmcnt(25) lgkmcnt(0)\n"
  "s_barrier\n"
  "ds_read_b128 a[16:19], v16 offset:0\n"
  "ds_read_b128 a[20:23], v16 offset:2048\n"
  "ds_read_b128 a[24:27], v16 offset:4096\n"
  "ds_read_b128 a[28:31], v16 offset:6144\n"
  "ds_read_b128 a[32:35], v16 offset:8192\n"
  "ds_read_b128 a[36:39], v16 offset:10240\n"
  "ds_read_b128 a[40:43], v16 offset:12288\n"
  "ds_read_b128 a[44:47], v16 offset:14336\n"
  "ds_read_b128 a[48:51], v16 offset:16384\n"
  "ds_read_b64_tr_b16 a[0:1], v12 offset:0\n"
  "ds_read_b64_tr_b16 a[2:3], v12 offset:3840\n"
  "ds_read_b64_tr_b16 a[4:5], v12 offset:160\n"
  "ds_read_b64_tr_b16 a[6:7], v12 offset:4000\n"
  "ds_read_b64_tr_b16 a[8:9], v12 offset:320\n"
  "ds_read_b64_tr_b16 a[10:11], v12 offset:4160\n"
  "ds_read_b64_tr_b16 a[12:13], v14 offset:0\n"
  "ds_read_b64_tr_b16 a[14:15], v14 offset:3840\n"
  "s_waitcnt lgkmcnt(0)\n"
  "s_nop 0\n"
  "v_mfma_f32_16x16x32_bf16 v[112:115], a[0:3], a[16:19], 0\n"
  "s_add_u32 s63, s62, 122880\n"
  "s_add_u32 m0, s64, 131072\n"
  "v_mfma_f32_16x16x32_bf16 v[116:119], a[0:3], a[20:23], 0\n"
  "buffer_load_dwordx4 v8, s[24:27], s54 offen lds\n"
  "v_mfma_f32_16x16x32_bf16 v[120:123], a[0:3], a[24:27], 0\n"
  "ds_read_b128 a[68:71], v17 offset:0\n"
  "v_mfma_f32_16x16x32_bf16 v[124:127], a[0:3], a[28:31], 0\n"
  "ds_read_b128 a[72:75], v17 offset:2048\n"
  "v_mfma_f32_16x16x32_bf16 v[128:131], a[0:3], a[32:35], 0\n"
  "ds_read_b128 a[76:79], v17 offset:4096\n"
  "v_mfma_f32_16x16x32_bf16 v[132:135], a[0:3], a[36:39], 0\n"
  "s_add_u32 m0, s64, 135168\n"
  "v_mfma_f32_16x16x32_bf16 v[136:139], a[0:3], a[40:43], 0\n"
  "buffer_load_dwordx4 v9, s[24:27], s54 offen lds\n"
  "v_mfma_f32_16x16x32_bf16 v[140:143], a[0:3], a[44:47], 0\n"
  "ds_read_b128 a[80:83], v17 offset:6144\n"
  "v_mfma_f32_16x16x32_bf16 v[144:147], a[0:3], a[48:51], 0\n"
  "ds_read_b128 a[84:87], v17 offset:8192\n"
  "v_mfma_f32_16x16x32_bf16 v[148:151], a[4:7], a[16:19], 0\n"
  "ds_read_b128 a[88:91], v17 offset:10240\n"
  "v_mfma_f32_16x16x32_bf16 v[152:155], a[4:7], a[20:23], 0\n"
  "s_add_u32 m0, s64, 139264\n"
  "v_mfma_f32_16x16x32_bf16 v[156:159], a[4:7], a[24:27], 0\n"
  "s_mov_b64 exec, s[74:75]\n"
  "s_nop 0\n"
  "buffer_load_dwordx4 v10, s[24:27], s54 offen lds\n"
  "s_mov_b64 exec, -1\n"
  "s_nop 0\n"
  "v_mfma_f32_16x16x32_bf16 v[160:163], a[4:7], a[28:31], 0\n"
  "ds_read_b128 a[92:95], v17 offset:12288\n"
  "v_mfma_f32_16x16x32_bf16 v[164:167], a[4:7], a[32:35], 0\n"
  "ds_read_b128 a[96:99], v17 offset:14336\n"
  "v_mfma_f32_16x16x32_bf16 v[168:171], a[4:7], a[36:39], 0\n"
  "ds_read_b128 a[100:103], v17 offset:16384\n"
  "v_mfma_f32_16x16x32_bf16 v[172:175], a[4:7], a[40:43], 0\n"
  "s_add_u32 m0, s64, 0\n"
  "s_nop 0\n"
  "v_mfma_f32_16x16x32_bf16 v[176:179], a[4:7], a[44:47], 0\n"
  "buffer_load_dwordx4 v2, s[28:31], s63 offen lds\n"
  "v_mfma_f32_16x16x32_bf16 v[180:183], a[4:7], a[48:51], 0\n"
  "ds_read_b64_tr_b16 a[52:53], v12 offset:16384\n"
  "v_mfma_f32_16x16x32_bf16 v[184:187], a[8:11], a[16:19], 0\n"
  "ds_read_b64_tr_b16 a[54:55], v12 offset:20224\n"
  "v_mfma_f32_16x16x32_bf16 v[188:191], a[8:11], a[20:23], 0\n"
  "ds_read_b64_tr_b16 a[56:57], v12 offset:16544\n"
  "v_mfma_f32_16x16x32_bf16 v[192:195], a[8:11], a[24:27], 0\n"
  "s_add_u32 m0, s64, 4096\n"
  "v_mfma_f32_16x16x32_bf16 v[196:199], a[8:11], a[28:31], 0\n"
  "buffer_load_dwordx4 v3, s[28:31], s63 offen lds\n"
  "v_mfma_f32_16x16x32_bf16 v[200:203], a[8:11], a[32:35], 0\n"
  "ds_read_b64_tr_b16 a[58:59], v12 offset:20384\n"
  "v_mfma_f32_16x16x32_bf16 v[204:207], a[8:11], a[36:39], 0\n"
  "ds_read_b64_tr_b16 a[60:61], v12 offset:16704\n"
  "v_mfma_f32_16x16x32_bf16 v[208:211], a[8:11], a[40:43], 0\n"
  "ds_read_b64_tr_b16 a[62:63], v12 offset:20544\n"
  "v_mfma_f32_16x16x32_bf16 v[212:215], a[8:11], a[44:47], 0\n"
  "s_add_u32 m0, s64, 8192\n"
  "v_mfma_f32_16x16x32_bf16 v[216:219], a[8:11], a[48:51], 0\n"
  "buffer_load_dwordx4 v4, s[28:31], s63 offen lds\n"
  "v_mfma_f32_16x16x32_bf16 v[220:223], a[12:15], a[16:19], 0\n"
  "ds_read_b64_tr_b16 a[64:65], v14 offset:16384\n"
  "v_mfma_f32_16x16x32_bf16 v[224:227], a[12:15], a[20:23], 0\n"
  "ds_read_b64_tr_b16 a[66:67], v14 offset:20224\n"
  "v_mfma_f32_16x16x32_bf16 v[228:231], a[12:15], a[24:27], 0\n"
  "v_mfma_f32_16x16x32_bf16 v[232:235], a[12:15], a[28:31], 0\n"
  "s_add_u32 m0, s64, 12288\n"
  "v_mfma_f32_16x16x32_bf16 v[236:239], a[12:15], a[32:35], 0\n"
  "buffer_load_dwordx4 v5, s[28:31], s63 offen lds\n"
  "v_mfma_f32_16x16x32_bf16 v[240:243], a[12:15], a[36:39], 0\n"
  "v_mfma_f32_16x16x32_bf16 v[244:247], a[12:15], a[40:43], 0\n"
  "v_mfma_f32_16x16x32_bf16 v[248:251], a[12:15], a[44:47], 0\n"
  "v_mfma_f32_16x16x32_bf16 v[252:255], a[12:15], a[48:51], 0\n"
  "s_waitcnt vmcnt(22) lgkmcnt(0)\n"
  "s_barrier\n"
  "v_mfma_f32_16x16x32_bf16 v[112:115], a[52:55], a[68:71], v[112:115]\n"
  "s_mul_i32 s62, s55, 1920\n"
  "s_add_u32 m0, s64, 81920\n"
  "v_mfma_f32_16x16x32_bf16 v[116:119], a[52:55], a[72:75], v[116:119]\n"
  "buffer_load_dwordx4 v6, s[24:27], s55 offen lds\n"
  "v_mfma_f32_16x16x32_bf16 v[120:123], a[52:55], a[76:79], v[120:123]\n"
  "ds_read_b128 a[16:19], v16 offset:20480\n"
  "v_mfma_f32_16x16x32_bf16 v[124:127], a[52:55], a[80:83], v[124:127]\n"
  "ds_read_b128 a[20:23], v16 offset:22528\n"
  "v_mfma_f32_16x16x32_bf16 v[128:131], a[52:55], a[84:87], v[128:131]\n"
  "ds_read_b128 a[24:27], v16 offset:24576\n"
  "v_mfma_f32_16x16x32_bf16 v[132:135], a[52:55], a[88:91], v[132:135]\n"
  "s_add_u32 m0, s64, 86016\n"
  "v_mfma_f32_16x16x32_bf16 v[136:139], a[52:55], a[92:95], v[136:139]\n"
  "buffer_load_dwordx4 v7, s[24:27], s55 offen lds\n"
  "v_mfma_f32_16x16x32_bf16 v[140:143], a[52:55], a[96:99], v[140:143]\n"
  "ds_read_b128 a[28:31], v16 offset:26624\n"
  "v_mfma_f32_16x16x32_bf16 v[144:147], a[52:55], a[100:103], v[144:147]\n"
  "ds_read_b128 a[32:35], v16 offset:28672\n"
  "v_mfma_f32_16x16x32_bf16 v[148:151], a[56:59], a[68:71], v[148:151]\n"
  "ds_read_b128 a[36:39], v16 offset:30720\n"
  "v_mfma_f32_16x16x32_bf16 v[152:155], a[56:59], a[72:75], v[152:155]\n"
  "s_add_u32 m0, s64, 16384\n"
  "v_mfma_f32_16x16x32_bf16 v[156:159], a[56:59], a[76:79], v[156:159]\n"
  "buffer_load_dwordx4 v2, s[28:31], s62 offen lds\n"
  "v_mfma_f32_16x16x32_bf16 v[160:163], a[56:59], a[80:83], v[160:163]\n"
  "ds_read_b128 a[40:43], v16 offset:32768\n"
  "v_mfma_f32_16x16x32_bf16 v[164:167], a[56:59], a[84:87], v[164:167]\n"
  "ds_read_b128 a[44:47], v16 offset:34816\n"
  "v_mfma_f32_16x16x32_bf16 v[168:171], a[56:59], a[88:91], v[168:171]\n"
  "ds_read_b128 a[48:51], v16 offset:36864\n"
  "v_mfma_f32_16x16x32_bf16 v[172:175], a[56:59], a[92:95], v[172:175]\n"
  "s_add_u32 m0, s64, 20480\n"
  "v_mfma_f32_16x16x32_bf16 v[176:179], a[56:59], a[96:99], v[176:179]\n"
  "buffer_load_dwordx4 v3, s[28:31], s62 offen lds\n"
  "v_mfma_f32_16x16x32_bf16 v[180:183], a[56:59], a[100:103], v[180:183]\n"
  "ds_read_b64_tr_b16 a[0:1], v12 offset:32768\n"
  "v_mfma_f32_16x16x32_bf16 v[184:187], a[60:63], a[68:71], v[184:187]\n"
  "ds_read_b64_tr_b16 a[2:3], v12 offset:36608\n"
  "v_mfma_f32_16x16x32_bf16 v[188:191], a[60:63], a[72:75], v[188:191]\n"
  "ds_read_b64_tr_b16 a[4:5], v12 offset:32928\n"
  "v_mfma_f32_16x16x32_bf16 v[192:195], a[60:63], a[76:79], v[192:195]\n"
  "s_add_u32 m0, s64, 24576\n"
  "v_mfma_f32_16x16x32_bf16 v[196:199], a[60:63], a[80:83], v[196:199]\n"
  "buffer_load_dwordx4 v4, s[28:31], s62 offen lds\n"
  "v_mfma_f32_16x16x32_bf16 v[200:203], a[60:63], a[84:87], v[200:203]\n"
  "ds_read_b64_tr_b16 a[6:7], v12 offset:36768\n"
  "v_mfma_f32_16x16x32_bf16 v[204:207], a[60:63], a[88:91], v[204:207]\n"
  "ds_read_b64_tr_b16 a[8:9], v12 offset:33088\n"
  "v_mfma_f32_16x16x32_bf16 v[208:211], a[60:63], a[92:95], v[208:211]\n"
  "ds_read_b64_tr_b16 a[10:11], v12 offset:36928\n"
  "v_mfma_f32_16x16x32_bf16 v[212:215], a[60:63], a[96:99], v[212:215]\n"
  "s_add_u32 m0, s64, 28672\n"
  "v_mfma_f32_16x16x32_bf16 v[216:219], a[60:63], a[100:103], v[216:219]\n"
  "buffer_load_dwordx4 v5, s[28:31], s62 offen lds\n"
  "v_mfma_f32_16x16x32_bf16 v[220:223], a[64:67], a[68:71], v[220:223]\n"
  "ds_read_b64_tr_b16 a[12:13], v14 offset:32768\n"
  "v_mfma_f32_16x16x32_bf16 v[224:227], a[64:67], a[72:75], v[224:227]\n"
  "ds_read_b64_tr_b16 a[14:15], v14 offset:36608\n"
  "v_mfma_f32_16x16x32_bf16 v[228:231], a[64:67], a[76:79], v[228:231]\n"
  "v_mfma_f32_16x16x32_bf16 v[232:235], a[64:67], a[80:83], v[232:235]\n"
  "v_mfma_f32_16x16x32_bf16 v[236:239], a[64:67], a[84:87], v[236:239]\n"
  "v_mfma_f32_16x16x32_bf16 v[240:243], a[64:67], a[88:91], v[240:243]\n"
  "v_mfma_f32_16x16x32_bf16 v[244:247], a[64:67], a[92:95], v[244:247]\n"
  "v_mfma_f32_16x16x32_bf16 v[248:251], a[64:67], a[96:99], v[248:251]\n"
  "v_mfma_f32_16x16x32_bf16 v[252:255], a[64:67], a[100:103], v[252:255]\n"
  "s_waitcnt vmcnt(24) lgkmcnt(0)\n"
  "s_barrier\n"
  "v_mfma_f32_16x16x32_bf16 v[112:115], a[0:3], a[16:19], v[112:115]\n"
  "s_add_u32 s63, s62, 122880\n"
  "s_add_u32 m0, s64, 90112\n"
  "v_mfma_f32_16x16x32_bf16 v[116:119], a[0:3], a[20:23], v[116:119]\n"
  "buffer_load_dwordx4 v8, s[24:27], s55 offen lds\n"
  "v_mfma_f32_16x16x32_bf16 v[120:123], a[0:3], a[24:27], v[120:123]\n"
  "ds_read_b128 a[68:71], v17 offset:20480\n"
  "v_mfma_f32_16x16x32_bf16 v[124:127], a[0:3], a[28:31], v[124:127]\n"
  "ds_read_b128 a[72:75], v17 offset:22528\n"
  "v_mfma_f32_16x16x32_bf16 v[128:131], a[0:3], a[32:35], v[128:131]\n"
  "ds_read_b128 a[76:79], v17 offset:24576\n"
  "v_mfma_f32_16x16x32_bf16 v[132:135], a[0:3], a[36:39], v[132:135]\n"
  "s_add_u32 m0, s64, 94208\n"
  "v_mfma_f32_16x16x32_bf16 v[136:139], a[0:3], a[40:43], v[136:139]\n"
  "buffer_load_dwordx4 v9, s[24:27], s55 offen lds\n"
  "v_mfma_f32_16x16x32_bf16 v[140:143], a[0:3], a[44:47], v[140:143]\n"
  "ds_read_b128 a[80:83], v17 offset:26624\n"
  "v_mfma_f32_16x16x32_bf16 v[144:147], a[0:3], a[48:51], v[144:147]\n"
  "ds_read_b128 a[84:87], v17 offset:28672\n"
  "v_mfma_f32_16x16x32_bf16 v[148:151], a[4:7], a[16:19], v[148:151]\n"
  "ds_read_b128 a[88:91], v17 offset:30720\n"
  "v_mfma_f32_16x16x32_bf16 v[152:155], a[4:7], a[20:23], v[152:155]\n"
  "s_add_u32 m0, s64, 98304\n"
  "v_mfma_f32_16x16x32_bf16 v[156:159], a[4:7], a[24:27], v[156:159]\n"
  "s_mov_b64 exec, s[74:75]\n"
  "s_nop 0\n"
  "buffer_load_dwordx4 v10, s[24:27], s55 offen lds\n"
  "s_mov_b64 exec, -1\n"
  "s_nop 0\n"
  "v_mfma_f32_16x16x32_bf16 v[160:163], a[4:7], a[28:31], v[160:163]\n"
  "ds_read_b128 a[92:95], v17 offset:32768\n"
  "v_mfma_f32_16x16x32_bf16 v[164:167], a[4:7], a[32:35], v[164:167]\n"
  "ds_read_b128 a[96:99], v17 offset:34816\n"
  "v_mfma_f32_16x16x32_bf16 v[168:171], a[4:7], a[36:39], v[168:171]\n"
  "ds_read_b128 a[100:103], v17 offset:36864\n"
  "v_mfma_f32_16x16x32_bf16 v[172:175], a[4:7], a[40:43], v[172:175]\n"
  "s_add_u32 m0, s64, 32768\n"
  "v_mfma_f32_16x16x32_bf16 v[176:179], a[4:7], a[44:47], v[176:179]\n"
  "buffer_load_dwordx4 v2, s[28:31], s63 offen lds\n"
  "v_mfma_f32_16x16x32_bf16 v[180:183], a[4:7], a[48:51], v[180:183]\n"
  "ds_read_b64_tr_b16 a[52:53], v12 offset:49152\n"
  "v_mfma_f32_16x16x32_bf16 v[184:187], a[8:11], a[16:19], v[184:187]\n"
  "ds_read_b64_tr_b16 a[54:55], v12 offset:52992\n"
  "v_mfma_f32_16x16x32_bf16 v[188:191], a[8:11], a[20:23], v[188:191]\n"
  "ds_read_b64_tr_b16 a[56:57], v12 offset:49312\n"
  "v_mfma_f32_16x16x32_bf16 v[192:195], a[8:11], a[24:27], v[192:195]\n"
  "s_add_u32 m0, s64, 36864\n"
  "v_mfma_f32_16x16x32_bf16 v[196:199], a[8:11], a[28:31], v[196:199]\n"
  "buffer_load_dwordx4 v3, s[28:31], s63 offen lds\n"
  "v_mfma_f32_16x16x32_bf16 v[200:203], a[8:11], a[32:35], v[200:203]\n"
  "ds_read_b64_tr_b16 a[58:59], v12 offset:53152\n"
  "v_mfma_f32_16x16x32_bf16 v[204:207], a[8:11], a[36:39], v[204:207]\n"
  "ds_read_b64_tr_b16 a[60:61], v12 offset:49472\n"
  "v_mfma_f32_16x16x32_bf16 v[208:211], a[8:11], a[40:43], v[208:211]\n"
  "ds_read_b64_tr_b16 a[62:63], v12 offset:53312\n"
  "v_mfma_f32_16x16x32_bf16 v[212:215], a[8:11], a[44:47], v[212:215]\n"
  "s_add_u32 m0, s64, 40960\n"
  "v_mfma_f32_16x16x32_bf16 v[216:219], a[8:11], a[48:51], v[216:219]\n"
  "buffer_load_dwordx4 v4, s[28:31], s63 offen lds\n"
  "v_mfma_f32_16x16x32_bf16 v[220:223], a[12:15], a[16:19], v[220:223]\n"
  "ds_read_b64_tr_b16 a[64:65], v14 offset:49152\n"
  "v_mfma_f32_16x16x32_bf16 v[224:227], a[12:15], a[20:23], v[224:227]\n"
  "ds_read_b64_tr_b16 a[66:67], v14 offset:52992\n"
  "v_mfma_f32_16x16x32_bf16 v[228:231], a[12:15], a[24:27], v[228:231]\n"
  "v_mfma_f32_16x16x32_bf16 v[232:235], a[12:15], a[28:31], v[232:235]\n"
  "s_add_u32 m0, s64, 45056\n"
  "v_mfma_f32_16x16x32_bf16 v[236:239], a[12:15], a[32:35], v[236:239]\n"
  "buffer_load_dwordx4 v5, s[28:31], s63 offen lds\n"
  "v_mfma_f32_16x16x32_bf16 v[240:243], a[12:15], a[36:39], v[240:243]\n"
  "v_mfma_f32_16x16x32_bf16 v[244:247], a[12:15], a[40:43], v[244:247]\n"
  "v_mfma_f32_16x16x32_bf16 v[248:251], a[12:15], a[44:47], v[248:251]\n"
  "v_mfma_f32_16x16x32_bf16 v[252:255], a[12:15], a[48:51], v[252:255]\n"
  "s_waitcnt vmcnt(17) lgkmcnt(0)\n"
  "s_barrier\n"
  "v_mfma_f32_16x16x32_bf16 v[112:115], a[52:55], a[68:71], v[112:115]\n"
  "s_mul_i32 s62, s56, 1920\n"
  "s_add_u32 m0, s64, 102400\n"
  "ds_read_b64_tr_b16 a[128:129], v49 offset:0\n"
  "v_mfma_f32_16x16x32_bf16 v[116:119], a[52:55], a[72:75], v[116:119]\n"
  "buffer_load_dwordx4 v6, s[24:27], s56 offen lds\n"
  "v_mfma_f32_16x16x32_bf16 v[120:123], a[52:55], a[76:79], v[120:123]\n"
  "ds_read_b128 a[16:19], v16 offset:40960\n"
  "v_mfma_f32_16x16x32_bf16 v[124:127], a[52:55], a[80:83], v[124:127]\n"
  "ds_read_b128 a[20:23], v16 offset:43008\n"
  "ds_read_b64_tr_b16 a[130:131], v49 offset:640\n"
  "v_mfma_f32_16x16x32_bf16 v[128:131], a[52:55], a[84:87], v[128:131]\n"
  "ds_read_b128 a[24:27], v16 offset:45056\n"
  "v_mfma_f32_16x16x32_bf16 v[132:135], a[52:55], a[88:91], v[132:135]\n"
  "s_add_u32 m0, s64, 106496\n"
  "v_mfma_f32_16x16x32_bf16 v[136:139], a[52:55], a[92:95], v[136:139]\n"
  "buffer_load_dwordx4 v7, s[24:27], s56 offen lds\n"
  "v_mfma_f32_16x16x32_bf16 v[140:143], a[52:55], a[96:99], v[140:143]\n"
  "ds_read_b128 a[28:31], v16 offset:47104\n"
  "ds_read_b64_tr_b16 a[132:133], v49 offset:8\n"
  "v_mfma_f32_16x16x32_bf16 v[144:147], a[52:55], a[100:103], v[144:147]\n"
  "ds_read_b128 a[32:35], v16 offset:49152\n"
  "v_mfma_f32_16x16x32_bf16 v[148:151], a[56:59], a[68:71], v[148:151]\n"
  "ds_read_b128 a[36:39], v16 offset:51200\n"
  "v_mfma_f32_16x16x32_bf16 v[152:155], a[56:59], a[72:75], v[152:155]\n"
  "s_add_u32 m0, s64, 49152\n"
  "ds_read_b64_tr_b16 a[134:135], v49 offset:648\n"
  "v_mfma_f32_16x16x32_bf16 v[156:159], a[56:59], a[76:79], v[156:159]\n"
  "buffer_load_dwordx4 v2, s[28:31], s62 offen lds\n"
  "v_mfma_f32_16x16x32_bf16 v[160:163], a[56:59], a[80:83], v[160:163]\n"
  "ds_read_b128 a[40:43], v16 offset:53248\n"
  "v_mfma_f32_16x16x32_bf16 v[164:167], a[56:59], a[84:87], v[164:167]\n"
  "ds_read_b128 a[44:47], v16 offset:55296\n"
  "v_mfma_f32_16x16x32_bf16 v[168:171], a[56:59], a[88:91], v[168:171]\n"
  "ds_read_b128 a[48:51], v16 offset:57344\n"
  "ds_read_b64_tr_b16 a[136:137], v49 offset:16\n"
  "v_mfma_f32_16x16x32_bf16 v[172:175], a[56:59], a[92:95], v[172:175]\n"
  "s_add_u32 m0, s64, 53248\n"
  "v_mfma_f32_16x16x32_bf16 v[176:179], a[56:59], a[96:99], v[176:179]\n"
  "buffer_load_dwordx4 v3, s[28:31], s62 offen lds\n"
  "v_mfma_f32_16x16x32_bf16 v[180:183], a[56:59], a[100:103], v[180:183]\n"
  "ds_read_b64_tr_b16 a[0:1], v13 offset:0\n"
  "ds_read_b64_tr_b16 a[138:139], v49 offset:656\n"
  "v_mfma_f32_16x16x32_bf16 v[184:187], a[60:63], a[68:71], v[184:187]\n"
  "ds_read_b64_tr_b16 a[2:3], v13 offset:3840\n"
  "v_mfma_f32_16x16x32_bf16 v[188:191], a[60:63], a[72:75], v[188:191]\n"
  "ds_read_b64_tr_b16 a[4:5], v13 offset:160\n"
  "v_mfma_f32_16x16x32_bf16 v[192:195], a[60:63], a[76:79], v[192:195]\n"
  "s_add_u32 m0, s64, 57344\n"
  "v_mfma_f32_16x16x32_bf16 v[196:199], a[60:63], a[80:83], v[196:199]\n"
  "buffer_load_dwordx4 v4, s[28:31], s62 offen lds\n"
  "ds_read_b64_tr_b16 a[140:141], v49 offset:24\n"
  "v_mfma_f32_16x16x32_bf16 v[200:203], a[60:63], a[84:87], v[200:203]\n"
  "ds_read_b64_tr_b16 a[6:7], v13 offset:4000\n"
  "v_mfma_f32_16x16x32_bf16 v[204:207], a[60:63], a[88:91], v[204:207]\n"
  "ds_read_b64_tr_b16 a[8:9], v13 offset:320\n"
  "v_mfma_f32_16x16x32_bf16 v[208:211], a[60:63], a[92:95], v[208:211]\n"
  "ds_read_b64_tr_b16 a[10:11], v13 offset:4160\n"
  "ds_read_b64_tr_b16 a[142:143], v49 offset:664\n"
  "v_mfma_f32_16x16x32_bf16 v[212:215], a[60:63], a[96:99], v[212:215]\n"
  "s_add_u32 m0, s64, 61440\n"
  "v_mfma_f32_16x16x32_bf16 v[216:219], a[60:63], a[100:103], v[216:219]\n"
  "buffer_load_dwordx4 v5, s[28:31], s62 offen lds\n"
  "v_mfma_f32_16x16x32_bf16 v[220:223], a[64:67], a[68:71], v[220:223]\n"
  "ds_read_b64_tr_b16 a[12:13], v15 offset:0\n"
  "v_mfma_f32_16x16x32_bf16 v[224:227], a[64:67], a[72:75], v[224:227]\n"
  "ds_read_b64_tr_b16 a[14:15], v15 offset:3840\n"
  "ds_read_b64_tr_b16 a[144:145], v49 offset:32\n"
  "v_mfma_f32_16x16x32_bf16 v[228:231], a[64:67], a[76:79], v[228:231]\n"
  "v_mfma_f32_16x16x32_bf16 v[232:235], a[64:67], a[80:83], v[232:235]\n"
  "v_mfma_f32_16x16x32_bf16 v[236:239], a[64:67], a[84:87], v[236:239]\n"
  "ds_read_b64_tr_b16 a[146:147], v49 offset:672\n"
  "v_mfma_f32_16x16x32_bf16 v[240:243], a[64:67], a[88:91], v[240:243]\n"
  "v_mfma_f32_16x16x32_bf16 v[244:247], a[64:67], a[92:95], v[244:247]\n"
  "v_mfma_f32_16x16x32_bf16 v[248:251], a[64:67], a[96:99], v[248:251]\n"
  "v_mfma_f32_16x16x32_bf16 v[252:255], a[64:67], a[100:103], v[252:255]\n"
  "s_waitcnt vmcnt(19) lgkmcnt(0)\n"
  "s_barrier\n"
  "v_mfma_f32_16x16x32_bf16 v[112:115], a[0:3], a[16:19], v[112:115]\n"
  "s_add_u32 s63, s62, 122880\n"
  "s_add_u32 m0, s64, 110592\n"
  "buffer_store_dwordx4 a[128:131], v48, s[36:39], s76 offen sc1\n"
  "v_mfma_f32_16x16x32_bf16 v[116:119], a[0:3], a[20:23], v[116:119]\n"
  "buffer_load_dwordx4 v8, s[24:27], s56 offen lds\n"
  "v_mfma_f32_16x16x32_bf16 v[120:123], a[0:3], a[24:27], v[120:123]\n"
  "ds_read_b128 a[68:71], v17 offset:40960\n"
  "v_mfma_f32_16x16x32_bf16 v[124:127], a[0:3], a[28:31], v[124:127]\n"
  "ds_read_b128 a[72:75], v17 offset:43008\n"
  "v_mfma_f32_16x16x32_bf16 v[128:131], a[0:3], a[32:35], v[128:131]\n"
  "ds_read_b128 a[76:79], v17 offset:45056\n"
  "v_mfma_f32_16x16x32_bf16 v[132:135], a[0:3], a[36:39], v[132:135]\n"
  "s_add_u32 m0, s64, 114688\n"
  "buffer_store_dwordx4 a[132:135], v48, s[36:39], s76 offen offset:1024 sc1\n"
  "v_mfma_f32_16x16x32_bf16 v[136:139], a[0:3], a[40:43], v[136:139]\n"
  "buffer_load_dwordx4 v9, s[24:27], s56 offen lds\n"
  "v_mfma_f32_16x16x32_bf16 v[140:143], a[0:3], a[44:47], v[140:143]\n"
  "ds_read_b128 a[80:83], v17 offset:47104\n"
  "v_mfma_f32_16x16x32_bf16 v[144:147], a[0:3], a[48:51], v[144:147]\n"
  "ds_read_b128 a[84:87], v17 offset:49152\n"
  "v_mfma_f32_16x16x32_bf16 v[148:151], a[4:7], a[16:19], v[148:151]\n"
  "ds_read_b128 a[88:91], v17 offset:51200\n"
  "v_mfma_f32_16x16x32_bf16 v[152:155], a[4:7], a[20:23], v[152:155]\n"
  "s_add_u32 m0, s64, 118784\n"
  "v_mfma_f32_16x16x32_bf16 v[156:159], a[4:7], a[24:27], v[156:159]\n"
  "s_mov_b64 exec, s[74:75]\n"
  "s_nop 0\n"
  "buffer_load_dwordx4 v10, s[24:27], s56 offen lds\n"
  "s_mov_b64 exec, -1\n"
  "s_nop 0\n"
  "buffer_store_dwordx4 a[136:139], v48, s[36:39], s76 offen offset:2048 sc1\n"
  "v_mfma_f32_16x16x32_bf16 v[160:163], a[4:7], a[28:31], v[160:163]\n"
  "ds_read_b128 a[92:95], v17 offset:53248\n"
  "v_mfma_f32_16x16x32_bf16 v[164:167], a[4:7], a[32:35], v[164:167]\n"
  "ds_read_b128 a[96:99], v17 offset:55296\n"
  "v_mfma_f32_16x16x32_bf16 v[168:171], a[4:7], a[36:39], v[168:171]\n"
  "ds_read_b128 a[100:103], v17 offset:57344\n"
  "v_mfma_f32_16x16x32_bf16 v[172:175], a[4:7], a[40:43], v[172:175]\n"
  "s_add_u32 m0, s64, 65536\n"
  "v_mfma_f32_16x16x32_bf16 v[176:179], a[4:7], a[44:47], v[176:179]\n"
  "buffer_load_dwordx4 v2, s[28:31], s63 offen lds\n"
  "v_mfma_f32_16x16x32_bf16 v[180:183], a[4:7], a[48:51], v[180:183]\n"
  "ds_read_b64_tr_b16 a[52:53], v12 offset:0\n"
  "buffer_store_dwordx4 a[140:143], v48, s[36:39], s76 offen offset:3072 sc1\n"
  "v_mfma_f32_16x16x32_bf16 v[184:187], a[8:11], a[16:19], v[184:187]\n"
  "ds_read_b64_tr_b16 a[54:55], v12 offset:3840\n"
  "v_mfma_f32_16x16x32_bf16 v[188:191], a[8:11], a[20:23], v[188:191]\n"
  "ds_read_b64_tr_b16 a[56:57], v12 offset:160\n"
  "v_mfma_f32_16x16x32_bf16 v[192:195], a[8:11], a[24:27], v[192:195]\n"
  "s_add_u32 m0, s64, 69632\n"
  "v_mfma_f32_16x16x32_bf16 v[196:199], a[8:11], a[28:31], v[196:199]\n"
  "buffer_load_dwordx4 v3, s[28:31], s63 offen lds\n"
  "v_mfma_f32_16x16x32_bf16 v[200:203], a[8:11], a[32:35], v[200:203]\n"
  "ds_read_b64_tr_b16 a[58:59], v12 offset:4000\n"
  "v_mfma_f32_16x16x32_bf16 v[204:207], a[8:11], a[36:39], v[204:207]\n"
  "ds_read_b64_tr_b16 a[60:61], v12 offset:320\n"
  "s_add_u32 s76, s76, 4096\n"
  "v_mfma_f32_16x16x32_bf16 v[208:211], a[8:11], a[40:43], v[208:211]\n"
  "ds_read_b64_tr_b16 a[62:63], v12 offset:4160\n"
  "v_mfma_f32_16x16x32_bf16 v[212:215], a[8:11], a[44:47], v[212:215]\n"
  "s_add_u32 m0, s64, 73728\n"
  "v_mfma_f32_16x16x32_bf16 v[216:219], a[8:11], a[48:51], v[216:219]\n"
  "buffer_load_dwordx4 v4, s[28:31], s63 offen lds\n"
  "v_mfma_f32_16x16x32_bf16 v[220:223], a[12:15], a[16:19], v[220:223]\n"
  "ds_read_b64_tr_b16 a[64:65], v14 offset:0\n"
  "v_mfma_f32_16x16x32_bf16 v[224:227], a[12:15], a[20:23], v[224:227]\n"
  "ds_read_b64_tr_b16 a[66:67], v14 offset:3840\n"
  "v_mfma_f32_16x16x32_bf16 v[228:231], a[12:15], a[24:27], v[228:231]\n"
  "buffer_store_dwordx4 a[144:147], v48, s[36:39], s76 offen sc1\n"
  "v_mfma_f32_16x16x32_bf16 v[232:235], a[12:15], a[28:31], v[232:235]\n"
  "s_add_u32 m0, s64, 77824\n"
  "v_mfma_f32_16x16x32_bf16 v[236:239], a[12:15], a[32:35], v[236:239]\n"
  "buffer_load_dwordx4 v5, s[28:31], s63 offen lds\n"
  "v_mfma_f32_16x16x32_bf16 v[240:243], a[12:15], a[36:39], v[240:243]\n"
  "v_mfma_f32_16x16x32_bf16 v[244:247], a[12:15], a[40:43], v[244:247]\n"
  "v_mfma_f32_16x16x32_bf16 v[248:251], a[12:15], a[44:47], v[248:251]\n"
  "v_mfma_f32_16x16x32_bf16 v[252:255], a[12:15], a[48:51], v[252:255]\n"
  "s_waitcnt vmcnt(22) lgkmcnt(0)\n"
  "s_barrier\n"
  "v_mfma_f32_16x16x32_bf16 v[112:115], a[52:55], a[68:71], v[112:115]\n"
  "s_mul_i32 s62, s57, 1920\n"
  "s_add_u32 m0, s64, 122880\n"
  "v_mfma_f32_16x16x32_bf16 v[116:119], a[52:55], a[72:75], v[116:119]\n"
  "buffer_load_dwordx4 v6, s[24:27], s57 offen lds\n"
  "v_mfma_f32_16x16x32_bf16 v[120:123], a[52:55], a[76:79], v[120:123]\n"
  "ds_read_b128 a[16:19], v16 offset:0\n"
  "v_mfma_f32_16x16x32_bf16 v[124:127], a[52:55], a[80:83], v[124:127]\n"
  "ds_read_b128 a[20:23], v16 offset:2048\n"
  "v_mfma_f32_16x16x32_bf16 v[128:131], a[52:55], a[84:87], v[128:131]\n"
  "ds_read_b128 a[24:27], v16 offset:4096\n"
  "v_mfma_f32_16x16x32_bf16 v[132:135], a[52:55], a[88:91], v[132:135]\n"
  "s_add_u32 m0, s64, 126976\n"
  "v_mfma_f32_16x16x32_bf16 v[136:139], a[52:55], a[92:95], v[136:139]\n"
  "buffer_load_dwordx4 v7, s[24:27], s57 offen lds\n"
  "v_mfma_f32_16x16x32_bf16 v[140:143], a[52:55], a[96:99], v[140:143]\n"
  "ds_read_b128 a[28:31], v16 offset:6144\n"
  "v_mfma_f32_16x16x32_bf16 v[144:147], a[52:55], a[100:103], v[144:147]\n"
  "ds_read_b128 a[32:35], v16 offset:8192\n"
  "v_mfma_f32_16x16x32_bf16 v[148:151], a[56:59], a[68:71], v[148:151]\n"
  "ds_read_b128 a[36:39], v16 offset:10240\n"
  "v_mfma_f32_16x16x32_bf16 v[152:155], a[56:59], a[72:75], v[152:155]\n"
  "s_add_u32 m0, s64, 0\n"
  "s_nop 0\n"
  "v_mfma_f32_16x16x32_bf16 v[156:159], a[56:59], a[76:79], v[156:159]\n"
  "buffer_load_dwordx4 v2, s[28:31], s62 offen lds\n"
  "v_mfma_f32_16x16x32_bf16 v[160:163], a[56:59], a[80:83], v[160:163]\n"
  "ds_read_b128 a[40:43], v16 offset:12288\n"
  "v_mfma_f32_16x16x32_bf16 v[164:167], a[56:59], a[84:87], v[164:167]\n"
  "ds_read_b128 a[44:47], v16 offset:14336\n"
  "v_mfma_f32_16x16x32_bf16 v[168:171], a[56:59], a[88:91], v[168:171]\n"
  "ds_read_b128 a[48:51], v16 offset:16384\n"
  "v_mfma_f32_16x16x32_bf16 v[172:175], a[56:59], a[92:95], v[172:175]\n"
  "s_add_u32 m0, s64, 4096\n"
  "v_mfma_f32_16x16x32_bf16 v[176:179], a[56:59], a[96:99], v[176:179]\n"
  "buffer_load_dwordx4 v3, s[28:31], s62 offen lds\n"
  "v_mfma_f32_16x16x32_bf16 v[180:183], a[56:59], a[100:103], v[180:183]\n"
  "ds_read_b64_tr_b16 a[0:1], v12 offset:16384\n"
  "v_mfma_f32_16x16x32_bf16 v[184:187], a[60:63], a[68:71], v[184:187]\n"
  "ds_read_b64_tr_b16 a[2:3], v12 offset:20224\n"
  "v_mfma_f32_16x16x32_bf16 v[188:191], a[60:63], a[72:75], v[188:191]\n"
  "ds_read_b64_tr_b16 a[4:5], v12 offset:16544\n"
  "v_mfma_f32_16x16x32_bf16 v[192:195], a[60:63], a[76:79], v[192:195]\n"
  "s_add_u32 m0, s64, 8192\n"
  "v_mfma_f32_16x16x32_bf16 v[196:199], a[60:63], a[80:83], v[196:199]\n"
  "buffer_load_dwordx4 v4, s[28:31], s62 offen lds\n"
  "v_mfma_f32_16x16x32_bf16 v[200:203], a[60:63], a[84:87], v[200:203]\n"
  "ds_read_b64_tr_b16 a[6:7], v12 offset:20384\n"
  "v_mfma_f32_16x16x32_bf16 v[204:207], a[60:63], a[88:91], v[204:207]\n"
  "ds_read_b64_tr_b16 a[8:9], v12 offset:16704\n"
  "v_mfma_f32_16x16x32_bf16 v[208:211], a[60:63], a[92:95], v[208:211]\n"
  "ds_read_b64_tr_b16 a[10:11], v12 offset:20544\n"
  "v_mfma_f32_16x16x32_bf16 v[212:215], a[60:63], a[96:99], v[212:215]\n"
  "s_add_u32 m0, s64, 12288\n"
  "v_mfma_f32_16x16x32_bf16 v[216:219], a[60:63], a[100:103], v[216:219]\n"
  "buffer_load_dwordx4 v5, s[28:31], s62 offen lds\n"
  "v_mfma_f32_16x16x32_bf16 v[220:223], a[64:67], a[68:71], v[220:223]\n"
  "ds_read_b64_tr_b16 a[12:13], v14 offset:16384\n"
  "v_mfma_f32_16x16x32_bf16 v[224:227], a[64:67], a[72:75], v[224:227]\n"
  "ds_read_b64_tr_b16 a[14:15], v14 offset:20224\n"
  "v_mfma_f32_16x16x32_bf16 v[228:231], a[64:67], a[76:79], v[228:231]\n"
  "v_mfma_f32_16x16x32_bf16 v[232:235], a[64:67], a[80:83], v[232:235]\n"
  "v_mfma_f32_16x16x32_bf16 v[236:239], a[64:67], a[84:87], v[236:239]\n"
  "v_mfma_f32_16x16x32_bf16 v[240:243], a[64:67], a[88:91], v[240:243]\n"
  "v_mfma_f32_16x16x32_bf16 v[244:247], a[64:67], a[92:95], v[244:247]\n"
  "v_mfma_f32_16x16x32_bf16 v[248:251], a[64:67], a[96:99], v[248:251]\n"
  "v_mfma_f32_16x16x32_bf16 v[252:255], a[64:67], a[100:103], v[252:255]\n"
  "s_waitcnt vmcnt(24) lgkmcnt(0)\n"
  "s_barrier\n"
  "v_mfma_f32_16x16x32_bf16 v[112:115], a[0:3], a[16:19], v[112:115]\n"
  "s_add_u32 s63, s62, 122880\n"
  "s_add_u32 m0, s64, 131072\n"
  "v_mfma_f32_16x16x32_bf16 v[116:119], a[0:3], a[20:23], v[116:119]\n"
  "buffer_load_dwordx4 v8, s[24:27], s57 offen lds\n"
  "v_mfma_f32_16x16x32_bf16 v[120:123], a[0:3], a[24:27], v[120:123]\n"
  "ds_read_b128 a[68:71], v17 offset:0\n"
  "v_mfma_f32_16x16x32_bf16 v[124:127], a[0:3], a[28:31], v[124:127]\n"
  "ds_read_b128 a[72:75], v17 offset:2048\n"
  "v_mfma_f32_16x16x32_bf16 v[128:131], a[0:3], a[32:35], v[128:131]\n"
  "ds_read_b128 a[76:79], v17 offset:4096\n"
  "v_mfma_f32_16x16x32_bf16 v[132:135], a[0:3], a[36:39], v[132:135]\n"
  "s_add_u32 m0, s64, 135168\n"
  "v_mfma_f32_16x16x32_bf16 v[136:139], a[0:3], a[40:43], v[136:139]\n"
  "buffer_load_dwordx4 v9, s[24:27], s57 offen lds\n"
  "v_mfma_f32_16x16x32_bf16 v[140:143], a[0:3], a[44:47], v[140:143]\n"
  "ds_read_b128 a[80:83], v17 offset:6144\n"
  "v_mfma_f32_16x16x32_bf16 v[144:147], a[0:3], a[48:51], v[144:147]\n"
  "ds_read_b128 a[84:87], v17 offset:8192\n"
  "v_mfma_f32_16x16x32_bf16 v[148:151], a[4:7], a[16:19], v[148:151]\n"
  "ds_read_b128 a[88:91], v17 offset:10240\n"
  "v_mfma_f32_16x16x32_bf16 v[152:155], a[4:7], a[20:23], v[152:155]\n"
  "s_add_u32 m0, s64, 139264\n"
  "v_mfma_f32_16x16x32_bf16 v[156:159], a[4:7], a[24:27], v[156:159]\n"
  "s_mov_b64 exec, s[74:75]\n"
  "s_nop 0\n"
  "buffer_load_dwordx4 v10, s[24:27], s57 offen lds\n"
  "s_mov_b64 exec, -1\n"
  "s_nop 0\n"
  "v_mfma_f32_16x16x32_bf16 v[160:163], a[4:7], a[28:31], v[160:163]\n"
  "ds_read_b128 a[92:95], v17 offset:12288\n"
  "v_mfma_f32_16x16x32_bf16 v[164:167], a[4:7], a[32:35], v[164:167]\n"
  "ds_read_b128 a[96:99], v17 offset:14336\n"
  "v_mfma_f32_16x16x32_bf16 v[168:171], a[4:7], a[36:39], v[168:171]\n"
  "ds_read_b128 a[100:103], v17 offset:16384\n"
  "v_mfma_f32_16x16x32_bf16 v[172:175], a[4:7], a[40:43], v[172:175]\n"
  "s_add_u32 m0, s64, 16384\n"
  "v_mfma_f32_16x16x32_bf16 v[176:179], a[4:7], a[44:47], v[176:179]\n"
  "buffer_load_dwordx4 v2, s[28:31], s63 offen lds\n"
  "v_mfma_f32_16x16x32_bf16 v[180:183], a[4:7], a[48:51], v[180:183]\n"
  "ds_read_b64_tr_b16 a[52:53], v12 offset:32768\n"
  "v_mfma_f32_16x16x32_bf16 v[184:187], a[8:11], a[16:19], v[184:187]\n"
  "ds_read_b64_tr_b16 a[54:55], v12 offset:36608\n"
  "v_mfma_f32_16x16x32_bf16 v[188:191], a[8:11], a[20:23], v[188:191]\n"
  "ds_read_b64_tr_b16 a[56:57], v12 offset:32928\n"
  "v_mfma_f32_16x16x32_bf16 v[192:195], a[8:11], a[24:27], v[192:195]\n"
  "s_add_u32 m0, s64, 20480\n"
  "v_mfma_f32_16x16x32_bf16 v[196:199], a[8:11], a[28:31], v[196:199]\n"
  "buffer_load_dwordx4 v3, s[28:31], s63 offen lds\n"
  "v_mfma_f32_16x16x32_bf16 v[200:203], a[8:11], a[32:35], v[200:203]\n"
  "ds_read_b64_tr_b16 a[58:59], v12 offset:36768\n"
  "v_mfma_f32_16x16x32_bf16 v[204:207], a[8:11], a[36:39], v[204:207]\n"
  "ds_read_b64_tr_b16 a[60:61], v12 offset:33088\n"
  "v_mfma_f32_16x16x32_bf16 v[208:211], a[8:11], a[40:43], v[208:211]\n"
  "ds_read_b64_tr_b16 a[62:63], v12 offset:36928\n"
  "v_mfma_f32_16x16x32_bf16 v[212:215], a[8:11], a[44:47], v[212:215]\n"
  "s_add_u32 m0, s64, 24576\n"
  "v_mfma_f32_16x16x32_bf16 v[216:219], a[8:11], a[48:51], v[216:219]\n"
  "buffer_load_dwordx4 v4, s[28:31], s63 offen lds\n"
  "v_mfma_f32_16x16x32_bf16 v[220:223], a[12:15], a[16:19], v[220:223]\n"
  "ds_read_b64_tr_b16 a[64:65], v14 offset:32768\n"
  "v_mfma_f32_16x16x32_bf16 v[224:227], a[12:15], a[20:23], v[224:227]\n"
  "ds_read_b64_tr_b16 a[66:67], v14 offset:36608\n"
  "v_mfma_f32_16x16x32_bf16 v[228:231], a[12:15], a[24:27], v[228:231]\n"
  "v_mfma_f32_16x16x32_bf16 v[232:235], a[12:15], a[28:31], v[232:235]\n"
  "s_add_u32 m0, s64, 28672\n"
  "v_mfma_f32_16x16x32_bf16 v[236:239], a[12:15], a[32:35], v[236:239]\n"
  "buffer_load_dwordx4 v5, s[28:31], s63 offen lds\n"
  "v_mfma_f32_16x16x32_bf16 v[240:243], a[12:15], a[36:39], v[240:243]\n"
  "v_mfma_f32_16x16x32_bf16 v[244:247], a[12:15], a[40:43], v[244:247]\n"
  "v_mfma_f32_16x16x32_bf16 v[248:251], a[12:15], a[44:47], v[248:251]\n"
  "v_mfma_f32_16x16x32_bf16 v[252:255], a[12:15], a[48:51], v[252:255]\n"
  "s_waitcnt vmcnt(14) lgkmcnt(0)\n"
  "s_barrier\n"
  "v_mfma_f32_16x16x32_bf16 v[112:115], a[52:55], a[68:71], v[112:115]\n"
  "s_mul_i32 s62, s58, 1920\n"
  "s_add_u32 m0, s64, 81920\n"
  "s_lshl_b32 s70, s50, 3\n"
  "s_nop 0\n"
  "v_mfma_f32_16x16x32_bf16 v[116:119], a[52:55], a[72:75], v[116:119]\n"
  "buffer_load_dwordx4 v6, s[24:27], s58 offen lds\n"
  "s_add_u32 s70, s70, 4096\n"
  "v_mfma_f32_16x16x32_bf16 v[120:123], a[52:55], a[76:79], v[120:123]\n"
  "ds_read_b128 a[16:19], v16 offset:20480\n"
  "v_mov_b32_e64 v24, s44\n"
  "v_mfma_f32_16x16x32_bf16 v[124:127], a[52:55], a[80:83], v[124:127]\n"
  "ds_read_b128 a[20:23], v16 offset:22528\n"
  "v_mov_b32_e64 v25, s45\n"
  "v_mfma_f32_16x16x32_bf16 v[128:131], a[52:55], a[84:87], v[128:131]\n"
  "ds_read_b128 a[24:27], v16 offset:24576\n"
  "s_mov_b64 exec, s[88:89]\n"
  "s_nop 0\n"
  "buffer_store_dwordx2 v[24:25], off, s[36:39], s70 sc1\n"
  "s_mov_b64 exec, -1\n"
  "s_nop 0\n"
  "v_mfma_f32_16x16x32_bf16 v[132:135], a[52:55], a[88:91], v[132:135]\n"
  "s_add_u32 m0, s64, 86016\n"
  "v_mfma_f32_16x16x32_bf16 v[136:139], a[52:55], a[92:95], v[136:139]\n"
  "buffer_load_dwordx4 v7, s[24:27], s58 offen lds\n"
  "v_mfma_f32_16x16x32_bf16 v[140:143], a[52:55], a[96:99], v[140:143]\n"
  "ds_read_b128 a[28:31], v16 offset:26624\n"
  "v_mfma_f32_16x16x32_bf16 v[144:147], a[52:55], a[100:103], v[144:147]\n"
  "ds_read_b128 a[32:35], v16 offset:28672\n"
  "v_mfma_f32_16x16x32_bf16 v[148:151], a[56:59], a[68:71], v[148:151]\n"
  "ds_read_b128 a[36:39], v16 offset:30720\n"
  "v_mfma_f32_16x16x32_bf16 v[152:155], a[56:59], a[72:75], v[152:155]\n"
  "s_add_u32 m0, s64, 32768\n"
  "v_mfma_f32_16x16x32_bf16 v[156:159], a[56:59], a[76:79], v[156:159]\n"
  "buffer_load_dwordx4 v2, s[28:31], s62 offen lds\n"
  "v_mfma_f32_16x16x32_bf16 v[160:163], a[56:59], a[80:83], v[160:163]\n"
  "ds_read_b128 a[40:43], v16 offset:32768\n"
  "v_mfma_f32_16x16x32_bf16 v[164:167], a[56:59], a[84:87], v[164:167]\n"
  "ds_read_b128 a[44:47], v16 offset:34816\n"
  "v_mfma_f32_16x16x32_bf16 v[168:171], a[56:59], a[88:91], v[168:171]\n"
  "ds_read_b128 a[48:51], v16 offset:36864\n"
  "v_mfma_f32_16x16x32_bf16 v[172:175], a[56:59], a[92:95], v[172:175]\n"
  "s_add_u32 m0, s64, 36864\n"
  "v_mfma_f32_16x16x32_bf16 v[176:179], a[56:59], a[96:99], v[176:179]\n"
  "buffer_load_dwordx4 v3, s[28:31], s62 offen lds\n"
  "v_mfma_f32_16x16x32_bf16 v[180:183], a[56:59], a[100:103], v[180:183]\n"
  "ds_read_b64_tr_b16 a[0:1], v12 offset:49152\n"
  "v_mfma_f32_16x16x32_bf16 v[184:187], a[60:63], a[68:71], v[184:187]\n"
  "ds_read_b64_tr_b16 a[2:3], v12 offset:52992\n"
  "v_mfma_f32_16x16x32_bf16 v[188:191], a[60:63], a[72:75], v[188:191]\n"
  "ds_read_b64_tr_b16 a[4:5], v12 offset:49312\n"
  "v_mfma_f32_16x16x32_bf16 v[192:195], a[60:63], a[76:79], v[192:195]\n"
  "s_add_u32 m0, s64, 40960\n"
  "v_mfma_f32_16x16x32_bf16 v[196:199], a[60:63], a[80:83], v[196:199]\n"
  "buffer_load_dwordx4 v4, s[28:31], s62 offen lds\n"
  "v_mfma_f32_16x16x32_bf16 v[200:203], a[60:63], a[84:87], v[200:203]\n"
  "ds_read_b64_tr_b16 a[6:7], v12 offset:53152\n"
  "v_mfma_f32_16x16x32_bf16 v[204:207], a[60:63], a[88:91], v[204:207]\n"
  "ds_read_b64_tr_b16 a[8:9], v12 offset:49472\n"
  "v_mfma_f32_16x16x32_bf16 v[208:211], a[60:63], a[92:95], v[208:211]\n"
  "ds_read_b64_tr_b16 a[10:11], v12 offset:53312\n"
  "v_mfma_f32_16x16x32_bf16 v[212:215], a[60:63], a[96:99], v[212:215]\n"
  "s_add_u32 m0, s64, 45056\n"
  "v_mfma_f32_16x16x32_bf16 v[216:219], a[60:63], a[100:103], v[216:219]\n"
  "buffer_load_dwordx4 v5, s[28:31], s62 offen lds\n"
  "v_mfma_f32_16x16x32_bf16 v[220:223], a[64:67], a[68:71], v[220:223]\n"
  "ds_read_b64_tr_b16 a[12:13], v14 offset:49152\n"
  "v_mfma_f32_16x16x32_bf16 v[224:227], a[64:67], a[72:75], v[224:227]\n"
  "ds_read_b64_tr_b16 a[14:15], v14 offset:52992\n"
  "v_mfma_f32_16x16x32_bf16 v[228:231], a[64:67], a[76:79], v[228:231]\n"
  "v_mfma_f32_16x16x32_bf16 v[232:235], a[64:67], a[80:83], v[232:235]\n"
  "v_mfma_f32_16x16x32_bf16 v[236:239], a[64:67], a[84:87], v[236:239]\n"
  "v_mfma_f32_16x16x32_bf16 v[240:243], a[64:67], a[88:91], v[240:243]\n"
  "v_mfma_f32_16x16x32_bf16 v[244:247], a[64:67], a[92:95], v[244:247]\n"
  "v_mfma_f32_16x16x32_bf16 v[248:251], a[64:67], a[96:99], v[248:251]\n"
  "v_mfma_f32_16x16x32_bf16 v[252:255], a[64:67], a[100:103], v[252:255]\n"
  "s_waitcnt vmcnt(20) lgkmcnt(0)\n"
  "s_barrier\n"
  "v_mfma_f32_16x16x32_bf16 v[112:115], a[0:3], a[16:19], v[112:115]\n"
  "s_add_u32 s63, s62, 122880\n"
  "s_add_u32 m0, s64, 90112\n"
  "v_mfma_f32_16x16x32_bf16 v[116:119], a[0:3], a[20:23], v[116:119]\n"
  "buffer_load_dwordx4 v8, s[24:27], s58 offen lds\n"
  "v_mfma_f32_16x16x32_bf16 v[120:123], a[0:3], a[24:27], v[120:123]\n"
  "ds_read_b128 a[68:71], v17 offset:20480\n"
  "v_mfma_f32_16x16x32_bf16 v[124:127], a[0:3], a[28:31], v[124:127]\n"
  "ds_read_b128 a[72:75], v17 offset:22528\n"
  "v_mfma_f32_16x16x32_bf16 v[128:131], a[0:3], a[32:35], v[128:131]\n"
  "ds_read_b128 a[76:79], v17 offset:24576\n"
  "v_mfma_f32_16x16x32_bf16 v[132:135], a[0:3], a[36:39], v[132:135]\n"
  "s_add_u32 m0, s64, 94208\n"
  "v_mfma_f32_16x16x32_bf16 v[136:139], a[0:3], a[40:43], v[136:139]\n"
  "buffer_load_dwordx4 v9, s[24:27], s58 offen lds\n"
  "v_mfma_f32_16x16x32_bf16 v[140:143], a[0:3], a[44:47], v[140:143]\n"
  "ds_read_b128 a[80:83], v17 offset:26624\n"
  "v_mfma_f32_16x16x32_bf16 v[144:147], a[0:3], a[48:51], v[144:147]\n"
  "ds_read_b128 a[84:87], v17 offset:28672\n"
  "v_mfma_f32_16x16x32_bf16 v[148:151], a[4:7], a[16:19], v[148:151]\n"
  "ds_read_b128 a[88:91], v17 offset:30720\n"
  "v_mfma_f32_16x16x32_bf16 v[152:155], a[4:7], a[20:23], v[152:155]\n"
  "s_add_u32 m0, s64, 98304\n"
  "v_mfma_f32_16x16x32_bf16 v[156:159], a[4:7], a[24:27], v[156:159]\n"
  "s_mov_b64 exec, s[74:75]\n"
  "s_nop 0\n"
  "buffer_load_dwordx4 v10, s[24:27], s58 offen lds\n"
  "s_mov_b64 exec, -1\n"
  "s_nop 0\n"
  "v_mfma_f32_16x16x32_bf16 v[160:163], a[4:7], a[28:31], v[160:163]\n"
  "ds_read_b128 a[92:95], v17 offset:32768\n"
  "v_mfma_f32_16x16x32_bf16 v[164:167], a[4:7], a[32:35], v[164:167]\n"
  "ds_read_b128 a[96:99], v17 offset:34816\n"
  "v_mfma_f32_16x16x32_bf16 v[168:171], a[4:7], a[36:39], v[168:171]\n"
  "ds_read_b128 a[100:103], v17 offset:36864\n"
  "v_mfma_f32_16x16x32_bf16 v[172:175], a[4:7], a[40:43], v[172:175]\n"
  "s_add_u32 m0, s64, 49152\n"
  "v_mfma_f32_16x16x32_bf16 v[176:179], a[4:7], a[44:47], v[176:179]\n"
  "buffer_load_dwordx4 v2, s[28:31], s63 offen lds\n"
  "v_mfma_f32_16x16x32_bf16 v[180:183], a[4:7], a[48:51], v[180:183]\n"
  "ds_read_b64_tr_b16 a[52:53], v13 offset:0\n"
  "v_mfma_f32_16x16x32_bf16 v[184:187], a[8:11], a[16:19], v[184:187]\n"
  "ds_read_b64_tr_b16 a[54:55], v13 offset:3840\n"
  "v_mfma_f32_16x16x32_bf16 v[188:191], a[8:11], a[20:23], v[188:191]\n"
  "ds_read_b64_tr_b16 a[56:57], v13 offset:160\n"
  "v_mfma_f32_16x16x32_bf16 v[192:195], a[8:11], a[24:27], v[192:195]\n"
  "s_add_u32 m0, s64, 53248\n"
  "v_mfma_f32_16x16x32_bf16 v[196:199], a[8:11], a[28:31], v[196:199]\n"
  "buffer_load_dwordx4 v3, s[28:31], s63 offen lds\n"
  "v_mfma_f32_16x16x32_bf16 v[200:203], a[8:11], a[32:35], v[200:203]\n"
  "ds_read_b64_tr_b16 a[58:59], v13 offset:4000\n"
  "v_mfma_f32_16x16x32_bf16 v[204:207], a[8:11], a[36:39], v[204:207]\n"
  "ds_read_b64_tr_b16 a[60:61], v13 offset:320\n"
  "v_mfma_f32_16x16x32_bf16 v[208:211], a[8:11], a[40:43], v[208:211]\n"
  "ds_read_b64_tr_b16 a[62:63], v13 offset:4160\n"
  "v_mfma_f32_16x16x32_bf16 v[212:215], a[8:11], a[44:47], v[212:215]\n"
  "s_add_u32 m0, s64, 57344\n"
  "v_mfma_f32_16x16x32_bf16 v[216:219], a[8:11], a[48:51], v[216:219]\n"
  "buffer_load_dwordx4 v4, s[28:31], s63 offen lds\n"
  "v_mfma_f32_16x16x32_bf16 v[220:223], a[12:15], a[16:19], v[220:223]\n"
  "ds_read_b64_tr_b16 a[64:65], v15 offset:0\n"
  "v_mfma_f32_16x16x32_bf16 v[224:227], a[12:15], a[20:23], v[224:227]\n"
  "ds_read_b64_tr_b16 a[66:67], v15 offset:3840\n"
  "v_mfma_f32_16x16x32_bf16 v[228:231], a[12:15], a[24:27], v[228:231]\n"
  "v_mfma_f32_16x16x32_bf16 v[232:235], a[12:15], a[28:31], v[232:235]\n"
  "s_add_u32 m0, s64, 61440\n"
  "v_mfma_f32_16x16x32_bf16 v[236:239], a[12:15], a[32:35], v[236:239]\n"
  "buffer_load_dwordx4 v5, s[28:31], s63 offen lds\n"
  "v_mfma_f32_16x16x32_bf16 v[240:243], a[12:15], a[36:39], v[240:243]\n"
  "v_mfma_f32_16x16x32_bf16 v[244:247], a[12:15], a[40:43], v[244:247]\n"
  "v_mfma_f32_16x16x32_bf16 v[248:251], a[12:15], a[44:47], v[248:251]\n"
  "v_mfma_f32_16x16x32_bf16 v[252:255], a[12:15], a[48:51], v[252:255]\n"
  "s_waitcnt vmcnt(18) lgkmcnt(0)\n"
  "s_barrier\n"
  "v_mfma_f32_16x16x32_bf16 v[112:115], a[52:55], a[68:71], v[112:115]\n"
  "s_mul_i32 s62, s59, 1920\n"
  "s_add_u32 m0, s64, 102400\n"
  "v_mfma_f32_16x16x32_bf16 v[116:119], a[52:55], a[72:75], v[116:119]\n"
  "buffer_load_dwordx4 v6, s[24:27], s59 offen lds\n"
  "v_mfma_f32_16x16x32_bf16 v[120:123], a[52:55], a[76:79], v[120:123]\n"
  "ds_read_b128 a[16:19], v16 offset:40960\n"
  "v_mfma_f32_16x16x32_bf16 v[124:127], a[52:55], a[80:83], v[124:127]\n"
  "ds_read_b128 a[20:23], v16 offset:43008\n"
  "v_mfma_f32_16x16x32_bf16 v[128:131], a[52:55], a[84:87], v[128:131]\n"
  "ds_read_b128 a[24:27], v16 offset:45056\n"
  "v_mfma_f32_16x16x32_bf16 v[132:135], a[52:55], a[88:91], v[132:135]\n"
  "s_add_u32 m0, s64, 106496\n"
  "v_mfma_f32_16x16x32_bf16 v[136:139], a[52:55], a[92:95], v[136:139]\n"
  "buffer_load_dwordx4 v7, s[24:27], s59 offen lds\n"
  "v_mfma_f32_16x16x32_bf16 v[140:143], a[52:55], a[96:99], v[140:143]\n"
  "ds_read_b128 a[28:31], v16 offset:47104\n"
  "v_mfma_f32_16x16x32_bf16 v[144:147], a[52:55], a[100:103], v[144:147]\n"
  "ds_read_b128 a[32:35], v16 offset:49152\n"
  "v_mfma_f32_16x16x32_bf16 v[148:151], a[56:59], a[68:71], v[148:151]\n"
  "ds_read_b128 a[36:39], v16 offset:51200\n"
  "v_mfma_f32_16x16x32_bf16 v[152:155], a[56:59], a[72:75], v[152:155]\n"
  "s_add_u32 m0, s64, 65536\n"
  "v_mfma_f32_16x16x32_bf16 v[156:159], a[56:59], a[76:79], v[156:159]\n"
  "buffer_load_dwordx4 v2, s[28:31], s62 offen lds\n"
  "v_mfma_f32_16x16x32_bf16 v[160:163], a[56:59], a[80:83], v[160:163]\n"
  "ds_read_b128 a[40:43], v16 offset:53248\n"
  "v_mfma_f32_16x16x32_bf16 v[164:167], a[56:59], a[84:87], v[164:167]\n"
  "ds_read_b128 a[44:47], v16 offset:55296\n"
  "v_mfma_f32_16x16x32_bf16 v[168:171], a[56:59], a[88:91], v[168:171]\n"
  "ds_read_b128 a[48:51], v16 offset:57344\n"
  "v_mfma_f32_16x16x32_bf16 v[172:175], a[56:59], a[92:95], v[172:175]\n"
  "s_add_u32 m0, s64, 69632\n"
  "v_mfma_f32_16x16x32_bf16 v[176:179], a[56:59], a[96:99], v[176:179]\n"
  "buffer_load_dwordx4 v3, s[28:31], s62 offen lds\n"
  "v_mfma_f32_16x16x32_bf16 v[180:183], a[56:59], a[100:103], v[180:183]\n"
  "ds_read_b64_tr_b16 a[0:1], v12 offset:0\n"
  "v_mfma_f32_16x16x32_bf16 v[184:187], a[60:63], a[68:71], v[184:187]\n"
  "ds_read_b64_tr_b16 a[2:3], v12 offset:3840\n"
  "v_mfma_f32_16x16x32_bf16 v[188:191], a[60:63], a[72:75], v[188:191]\n"
  "ds_read_b64_tr_b16 a[4:5], v12 offset:160\n"
  "v_mfma_f32_16x16x32_bf16 v[192:195], a[60:63], a[76:79], v[192:195]\n"
  "s_add_u32 m0, s64, 73728\n"
  "v_mfma_f32_16x16x32_bf16 v[196:199], a[60:63], a[80:83], v[196:199]\n"
  "buffer_load_dwordx4 v4, s[28:31], s62 offen lds\n"
  "v_mfma_f32_16x16x32_bf16 v[200:203], a[60:63], a[84:87], v[200:203]\n"
  "ds_read_b64_tr_b16 a[6:7], v12 offset:4000\n"
  "v_mfma_f32_16x16x32_bf16 v[204:207], a[60:63], a[88:91], v[204:207]\n"
  "ds_read_b64_tr_b16 a[8:9], v12 offset:320\n"
  "v_mfma_f32_16x16x32_bf16 v[208:211], a[60:63], a[92:95], v[208:211]\n"
  "ds_read_b64_tr_b16 a[10:11], v12 offset:4160\n"
  "v_mfma_f32_16x16x32_bf16 v[212:215], a[60:63], a[96:99], v[212:215]\n"
  "s_add_u32 m0, s64, 77824\n"
  "v_mfma_f32_16x16x32_bf16 v[216:219], a[60:63], a[100:103], v[216:219]\n"
  "buffer_load_dwordx4 v5, s[28:31], s62 offen lds\n"
  "v_mfma_f32_16x16x32_bf16 v[220:223], a[64:67], a[68:71], v[220:223]\n"
  "ds_read_b64_tr_b16 a[12:13], v14 offset:0\n"
  "v_mfma_f32_16x16x32_bf16 v[224:227], a[64:67], a[72:75], v[224:227]\n"
  "ds_read_b64_tr_b16 a[14:15], v14 offset:3840\n"
  "v_mfma_f32_16x16x32_bf16 v[228:231], a[64:67], a[76:79], v[228:231]\n"
  "v_mfma_f32_16x16x32_bf16 v[232:235], a[64:67], a[80:83], v[232:235]\n"
  "v_mfma_f32_16x16x32_bf16 v[236:239], a[64:67], a[84:87], v[236:239]\n"
  "v_mfma_f32_16x16x32_bf16 v[240:243], a[64:67], a[88:91], v[240:243]\n"
  "v_mfma_f32_16x16x32_bf16 v[244:247], a[64:67], a[92:95], v[244:247]\n"
  "v_mfma_f32_16x16x32_bf16 v[248:251], a[64:67], a[96:99], v[248:251]\n"
  "v_mfma_f32_16x16x32_bf16 v[252:255], a[64:67], a[100:103], v[252:255]\n"
  "s_waitcnt vmcnt(20) lgkmcnt(0)\n"
  "s_barrier\n"
  "v_mfma_f32_16x16x32_bf16 v[112:115], a[0:3], a[16:19], v[112:115]\n"
  "s_add_u32 s63, s62, 122880\n"
  "s_add_u32 m0, s64, 110592\n"
  "v_mfma_f32_16x16x32_bf16 v[116:119], a[0:3], a[20:23], v[116:119]\n"
  "buffer_load_dwordx4 v8, s[24:27], s59 offen lds\n"
  "v_mfma_f32_16x16x32_bf16 v[120:123], a[0:3], a[24:27], v[120:123]\n"
  "ds_read_b128 a[68:71], v17 offset:40960\n"
  "v_mfma_f32_16x16x32_bf16 v[124:127], a[0:3], a[28:31], v[124:127]\n"
  "ds_read_b128 a[72:75], v17 offset:43008\n"
  "v_mfma_f32_16x16x32_bf16 v[128:131], a[0:3], a[32:35], v[128:131]\n"
  "ds_read_b128 a[76:79], v17 offset:45056\n"
  "v_mfma_f32_16x16x32_bf16 v[132:135], a[0:3], a[36:39], v[132:135]\n"
  "s_add_u32 m0, s64, 114688\n"
  "v_mfma_f32_16x16x32_bf16 v[136:139], a[0:3], a[40:43], v[136:139]\n"
  "buffer_load_dwordx4 v9, s[24:27], s59 offen lds\n"
  "v_mfma_f32_16x16x32_bf16 v[140:143], a[0:3], a[44:47], v[140:143]\n"
  "ds_read_b128 a[80:83], v17 offset:47104\n"
  "v_mfma_f32_16x16x32_bf16 v[144:147], a[0:3], a[48:51], v[144:147]\n"
  "ds_read_b128 a[84:87], v17 offset:49152\n"
  "v_mfma_f32_16x16x32_bf16 v[148:151], a[4:7], a[16:19], v[148:151]\n"
  "ds_read_b128 a[88:91], v17 offset:51200\n"
  "v_mfma_f32_16x16x32_bf16 v[152:155], a[4:7], a[20:23], v[152:155]\n"
  "s_add_u32 m0, s64, 118784\n"
  "v_mfma_f32_16x16x32_bf16 v[156:159], a[4:7], a[24:27], v[156:159]\n"
  "s_mov_b64 exec, s[74:75]\n"
  "s_nop 0\n"
  "buffer_load_dwordx4 v10, s[24:27], s59 offen lds\n"
  "s_mov_b64 exec, -1\n"
  "s_nop 0\n"
  "v_mfma_f32_16x16x32_bf16 v[160:163], a[4:7], a[28:31], v[160:163]\n"
  "ds_read_b128 a[92:95], v17 offset:53248\n"
  "v_mfma_f32_16x16x32_bf16 v[164:167], a[4:7], a[32:35], v[164:167]\n"
  "ds_read_b128 a[96:99], v17 offset:55296\n"
  "v_mfma_f32_16x16x32_bf16 v[168:171], a[4:7], a[36:39], v[168:171]\n"
  "ds_read_b128 a[100:103], v17 offset:57344\n"
  "v_mfma_f32_16x16x32_bf16 v[172:175], a[4:7], a[40:43], v[172:175]\n"
  "s_add_u32 m0, s64, 0\n"
  "s_nop 0\n"
  "v_mfma_f32_16x16x32_bf16 v[176:179], a[4:7], a[44:47], v[176:179]\n"
  "buffer_load_dwordx4 v2, s[28:31], s63 offen lds\n"
  "v_mfma_f32_16x16x32_bf16 v[180:183], a[4:7], a[48:51], v[180:183]\n"
  "ds_read_b64_tr_b16 a[52:53], v12 offset:16384\n"
  "v_mfma_f32_16x16x32_bf16 v[184:187], a[8:11], a[16:19], v[184:187]\n"
  "ds_read_b64_tr_b16 a[54:55], v12 offset:20224\n"
  "v_mfma_f32_16x16x32_bf16 v[188:191], a[8:11], a[20:23], v[188:191]\n"
  "ds_read_b64_tr_b16 a[56:57], v12 offset:16544\n"
  "v_mfma_f32_16x16x32_bf16 v[192:195], a[8:11], a[24:27], v[192:195]\n"
  "s_add_u32 m0, s64, 4096\n"
  "v_mfma_f32_16x16x32_bf16 v[196:199], a[8:11], a[28:31], v[196:199]\n"
  "buffer_load_dwordx4 v3, s[28:31], s63 offen lds\n"
  "v_mfma_f32_16x16x32_bf16 v[200:203], a[8:11], a[32:35], v[200:203]\n"
  "ds_read_b64_tr_b16 a[58:59], v12 offset:20384\n"
  "v_mfma_f32_16x16x32_bf16 v[204:207], a[8:11], a[36:39], v[204:207]\n"
  "ds_read_b64_tr_b16 a[60:61], v12 offset:16704\n"
  "v_mfma_f32_16x16x32_bf16 v[208:211], a[8:11], a[40:43], v[208:211]\n"
  "ds_read_b64_tr_b16 a[62:63], v12 offset:20544\n"
  "v_mfma_f32_16x16x32_bf16 v[212:215], a[8:11], a[44:47], v[212:215]\n"
  "s_add_u32 m0, s64, 8192\n"
  "v_mfma_f32_16x16x32_bf16 v[216:219], a[8:11], a[48:51], v[216:219]\n"
  "buffer_load_dwordx4 v4, s[28:31], s63 offen lds\n"
  "v_mfma_f32_16x16x32_bf16 v[220:223], a[12:15], a[16:19], v[220:223]\n"
  "ds_read_b64_tr_b16 a[64:65], v14 offset:16384\n"
  "v_mfma_f32_16x16x32_bf16 v[224:227], a[12:15], a[20:23], v[224:227]\n"
  "ds_read_b64_tr_b16 a[66:67], v14 offset:20224\n"
  "v_mfma_f32_16x16x32_bf16 v[228:231], a[12:15], a[24:27], v[228:231]\n"
  "v_mfma_f32_16x16x32_bf16 v[232:235], a[12:15], a[28:31], v[232:235]\n"
  "s_add_u32 m0, s64, 12288\n"
  "v_mfma_f32_16x16x32_bf16 v[236:239], a[12:15], a[32:35], v[236:239]\n"
  "buffer_load_dwordx4 v5, s[28:31], s63 offen lds\n"
  "v_mfma_f32_16x16x32_bf16 v[240:243], a[12:15], a[36:39], v[240:243]\n"
  "v_mfma_f32_16x16x32_bf16 v[244:247], a[12:15], a[40:43], v[244:247]\n"
  "v_mfma_f32_16x16x32_bf16 v[248:251], a[12:15], a[44:47], v[248:251]\n"
  "v_mfma_f32_16x16x32_bf16 v[252:255], a[12:15], a[48:51], v[252:255]\n"
  "s_waitcnt vmcnt(17) lgkmcnt(0)\n"
  "s_barrier\n"
  "v_mfma_f32_16x16x32_bf16 v[112:115], a[52:55], a[68:71], v[112:115]\n"
  "s_mul_i32 s62, s60, 1920\n"
  "s_add_u32 m0, s64, 122880\n"
  "v_mfma_f32_16x16x32_bf16 v[116:119], a[52:55], a[72:75], v[116:119]\n"
  "buffer_load_dwordx4 v6, s[24:27], s60 offen lds\n"
  "v_mfma_f32_16x16x32_bf16 v[120:123], a[52:55], a[76:79], v[120:123]\n"
  "ds_read_b128 a[16:19], v16 offset:0\n"
  "v_mfma_f32_16x16x32_bf16 v[124:127], a[52:55], a[80:83], v[124:127]\n"
  "ds_read_b128 a[20:23], v16 offset:2048\n"
  "v_mfma_f32_16x16x32_bf16 v[128:131], a[52:55], a[84:87], v[128:131]\n"
  "ds_read_b128 a[24:27], v16 offset:4096\n"
  "v_mfma_f32_16x16x32_bf16 v[132:135], a[52:55], a[88:91], v[132:135]\n"
  "s_add_u32 m0, s64, 126976\n"
  "v_mfma_f32_16x16x32_bf16 v[136:139], a[52:55], a[92:95], v[136:139]\n"
  "buffer_load_dwordx4 v7, s[24:27], s60 offen lds\n"
  "v_mfma_f32_16x16x32_bf16 v[140:143], a[52:55], a[96:99], v[140:143]\n"
  "ds_read_b128 a[28:31], v16 offset:6144\n"
  "v_mfma_f32_16x16x32_bf16 v[144:147], a[52:55], a[100:103], v[144:147]\n"
  "ds_read_b128 a[32:35], v16 offset:8192\n"
  "v_mfma_f32_16x16x32_bf16 v[148:151], a[56:59], a[68:71], v[148:151]\n"
  "ds_read_b128 a[36:39], v16 offset:10240\n"
  "v_mfma_f32_16x16x32_bf16 v[152:155], a[56:59], a[72:75], v[152:155]\n"
  "s_add_u32 m0, s64, 16384\n"
  "v_mfma_f32_16x16x32_bf16 v[156:159], a[56:59], a[76:79], v[156:159]\n"
  "buffer_load_dwordx4 v2, s[28:31], s62 offen lds\n"
  "v_mfma_f32_16x16x32_bf16 v[160:163], a[56:59], a[80:83], v[160:163]\n"
  "ds_read_b128 a[40:43], v16 offset:12288\n"
  "v_mfma_f32_16x16x32_bf16 v[164:167], a[56:59], a[84:87], v[164:167]\n"
  "ds_read_b128 a[44:47], v16 offset:14336\n"
  "v_mfma_f32_16x16x32_bf16 v[168:171], a[56:59], a[88:91], v[168:171]\n"
  "ds_read_b128 a[48:51], v16 offset:16384\n"
  "v_mfma_f32_16x16x32_bf16 v[172:175], a[56:59], a[92:95], v[172:175]\n"
  "s_add_u32 m0, s64, 20480\n"
  "v_mfma_f32_16x16x32_bf16 v[176:179], a[56:59], a[96:99], v[176:179]\n"
  "buffer_load_dwordx4 v3, s[28:31], s62 offen lds\n"
  "v_mfma_f32_16x16x32_bf16 v[180:183], a[56:59], a[100:103], v[180:183]\n"
  "ds_read_b64_tr_b16 a[0:1], v12 offset:32768\n"
  "v_mfma_f32_16x16x32_bf16 v[184:187], a[60:63], a[68:71], v[184:187]\n"
  "ds_read_b64_tr_b16 a[2:3], v12 offset:36608\n"
  "v_mfma_f32_16x16x32_bf16 v[188:191], a[60:63], a[72:75], v[188:191]\n"
  "ds_read_b64_tr_b16 a[4:5], v12 offset:32928\n"
  "v_mfma_f32_16x16x32_bf16 v[192:195], a[60:63], a[76:79], v[192:195]\n"
  "s_add_u32 m0, s64, 24576\n"
  "v_mfma_f32_16x16x32_bf16 v[196:199], a[60:63], a[80:83], v[196:199]\n"
  "buffer_load_dwordx4 v4, s[28:31], s62 offen lds\n"
  "v_mfma_f32_16x16x32_bf16 v[200:203], a[60:63], a[84:87], v[200:203]\n"
  "ds_read_b64_tr_b16 a[6:7], v12 offset:36768\n"
  "v_mfma_f32_16x16x32_bf16 v[204:207], a[60:63], a[88:91], v[204:207]\n"
  "ds_read_b64_tr_b16 a[8:9], v12 offset:33088\n"
  "v_mfma_f32_16x16x32_bf16 v[208:211], a[60:63], a[92:95], v[208:211]\n"
  "ds_read_b64_tr_b16 a[10:11], v12 offset:36928\n"
  "v_mfma_f32_16x16x32_bf16 v[212:215], a[60:63], a[96:99], v[212:215]\n"
  "s_add_u32 m0, s64, 28672\n"
  "v_mfma_f32_16x16x32_bf16 v[216:219], a[60:63], a[100:103], v[216:219]\n"
  "buffer_load_dwordx4 v5, s[28:31], s62 offen lds\n"
  "v_mfma_f32_16x16x32_bf16 v[220:223], a[64:67], a[68:71], v[220:223]\n"
  "ds_read_b64_tr_b16 a[12:13], v14 offset:32768\n"
  "v_mfma_f32_16x16x32_bf16 v[224:227], a[64:67], a[72:75], v[224:227]\n"
  "ds_read_b64_tr_b16 a[14:15], v14 offset:36608\n"
  "v_mfma_f32_16x16x32_bf16 v[228:231], a[64:67], a[76:79], v[228:231]\n"
  "v_mfma_f32_16x16x32_bf16 v[232:235], a[64:67], a[80:83], v[232:235]\n"
  "v_mfma_f32_16x16x32_bf16 v[236:239], a[64:67], a[84:87], v[236:239]\n"
  "v_mfma_f32_16x16x32_bf16 v[240:243], a[64:67], a[88:91], v[240:243]\n"
  "v_mfma_f32_16x16x32_bf16 v[244:247], a[64:67], a[92:95], v[244:247]\n"
  "v_mfma_f32_16x16x32_bf16 v[248:251], a[64:67], a[96:99], v[248:251]\n"
  "v_mfma_f32_16x16x32_bf16 v[252:255], a[64:67], a[100:103], v[252:255]\n"
  "s_waitcnt vmcnt(19) lgkmcnt(0)\n"
  "s_barrier\n"
  "v_mfma_f32_16x16x32_bf16 v[112:115], a[0:3], a[16:19], v[112:115]\n"
  "s_add_u32 s63, s62, 122880\n"
  "s_add_u32 m0, s64, 131072\n"
  "v_mfma_f32_16x16x32_bf16 v[116:119], a[0:3], a[20:23], v[116:119]\n"
  "buffer_load_dwordx4 v8, s[24:27], s60 offen lds\n"
  "v_mfma_f32_16x16x32_bf16 v[120:123], a[0:3], a[24:27], v[120:123]\n"
  "ds_read_b128 a[68:71], v17 offset:0\n"
  "v_mfma_f32_16x16x32_bf16 v[124:127], a[0:3], a[28:31], v[124:127]\n"
  "ds_read_b128 a[72:75], v17 offset:2048\n"
  "v_mfma_f32_16x16x32_bf16 v[128:131], a[0:3], a[32:35], v[128:131]\n"
  "ds_read_b128 a[76:79], v17 offset:4096\n"
  "v_mfma_f32_16x16x32_bf16 v[132:135], a[0:3], a[36:39], v[132:135]\n"
  "s_add_u32 m0, s64, 135168\n"
  "v_mfma_f32_16x16x32_bf16 v[136:139], a[0:3], a[40:43], v[136:139]\n"
  "buffer_load_dwordx4 v9, s[24:27], s60 offen lds\n"
  "v_mfma_f32_16x16x32_bf16 v[140:143], a[0:3], a[44:47], v[140:143]\n"
  "ds_read_b128 a[80:83], v17 offset:6144\n"
  "v_mfma_f32_16x16x32_bf16 v[144:147], a[0:3], a[48:51], v[144:147]\n"
  "ds_read_b128 a[84:87], v17 offset:8192\n"
  "v_mfma_f32_16x16x32_bf16 v[148:151], a[4:7], a[16:19], v[148:151]\n"
  "ds_read_b128 a[88:91], v17 offset:10240\n"
  "v_mfma_f32_16x16x32_bf16 v[152:155], a[4:7], a[20:23], v[152:155]\n"
  "s_add_u32 m0, s64, 139264\n"
  "v_mfma_f32_16x16x32_bf16 v[156:159], a[4:7], a[24:27], v[156:159]\n"
  "s_mov_b64 exec, s[74:75]\n"
  "s_nop 0\n"
  "buffer_load_dwordx4 v10, s[24:27], s60 offen lds\n"
  "s_mov_b64 exec, -1\n"
  "s_nop 0\n"
  "v_mfma_f32_16x16x32_bf16 v[160:163], a[4:7], a[28:31], v[160:163]\n"
  "ds_read_b128 a[92:95], v17 offset:12288\n"
  "v_mfma_f32_16x16x32_bf16 v[164:167], a[4:7], a[32:35], v[164:167]\n"
  "ds_read_b128 a[96:99], v17 offset:14336\n"
  "v_mfma_f32_16x16x32_bf16 v[168:171], a[4:7], a[36:39], v[168:171]\n"
  "ds_read_b128 a[100:103], v17 offset:16384\n"
  "v_mfma_f32_16x16x32_bf16 v[172:175], a[4:7], a[40:43], v[172:175]\n"
  "s_add_u32 m0, s64, 32768\n"
  "v_mfma_f32_16x16x32_bf16 v[176:179], a[4:7], a[44:47], v[176:179]\n"
  "buffer_load_dwordx4 v2, s[28:31], s63 offen lds\n"
  "v_mfma_f32_16x16x32_bf16 v[180:183], a[4:7], a[48:51], v[180:183]\n"
  "ds_read_b64_tr_b16 a[52:53], v12 offset:49152\n"
  "v_mfma_f32_16x16x32_bf16 v[184:187], a[8:11], a[16:19], v[184:187]\n"
  "ds_read_b64_tr_b16 a[54:55], v12 offset:52992\n"
  "v_mfma_f32_16x16x32_bf16 v[188:191], a[8:11], a[20:23], v[188:191]\n"
  "ds_read_b64_tr_b16 a[56:57], v12 offset:49312\n"
  "v_mfma_f32_16x16x32_bf16 v[192:195], a[8:11], a[24:27], v[192:195]\n"
  "s_add_u32 m0, s64, 36864\n"
  "v_mfma_f32_16x16x32_bf16 v[196:199], a[8:11], a[28:31], v[196:199]\n"
  "buffer_load_dwordx4 v3, s[28:31], s63 offen lds\n"
  "v_mfma_f32_16x16x32_bf16 v[200:203], a[8:11], a[32:35], v[200:203]\n"
  "ds_read_b64_tr_b16 a[58:59], v12 offset:53152\n"
  "v_mfma_f32_16x16x32_bf16 v[204:207], a[8:11], a[36:39], v[204:207]\n"
  "ds_read_b64_tr_b16 a[60:61], v12 offset:49472\n"
  "v_mfma_f32_16x16x32_bf16 v[208:211], a[8:11], a[40:43], v[208:211]\n"
  "ds_read_b64_tr_b16 a[62:63], v12 offset:53312\n"
  "v_mfma_f32_16x16x32_bf16 v[212:215], a[8:11], a[44:47], v[212:215]\n"
  "s_add_u32 m0, s64, 40960\n"
  "v_mfma_f32_16x16x32_bf16 v[216:219], a[8:11], a[48:51], v[216:219]\n"
  "buffer_load_dwordx4 v4, s[28:31], s63 offen lds\n"
  "v_mfma_f32_16x16x32_bf16 v[220:223], a[12:15], a[16:19], v[220:223]\n"
  "ds_read_b64_tr_b16 a[64:65], v14 offset:49152\n"
  "v_mfma_f32_16x16x32_bf16 v[224:227], a[12:15], a[20:23], v[224:227]\n"
  "ds_read_b64_tr_b16 a[66:67], v14 offset:52992\n"
  "v_mfma_f32_16x16x32_bf16 v[228:231], a[12:15], a[24:27], v[228:231]\n"
  "v_mfma_f32_16x16x32_bf16 v[232:235], a[12:15], a[28:31], v[232:235]\n"
  "s_add_u32 m0, s64, 45056\n"
  "v_mfma_f32_16x16x32_bf16 v[236:239], a[12:15], a[32:35], v[236:239]\n"
  "buffer_load_dwordx4 v5, s[28:31], s63 offen lds\n"
  "v_mfma_f32_16x16x32_bf16 v[240:243], a[12:15], a[36:39], v[240:243]\n"
  "v_mfma_f32_16x16x32_bf16 v[244:247], a[12:15], a[40:43], v[244:247]\n"
  "v_mfma_f32_16x16x32_bf16 v[248:251], a[12:15], a[44:47], v[248:251]\n"
  "v_mfma_f32_16x16x32_bf16 v[252:255], a[12:15], a[48:51], v[252:255]\n"
  "s_waitcnt vmcnt(17) lgkmcnt(0)\n"
  "s_barrier\n"
  "v_mfma_f32_16x16x32_bf16 v[112:115], a[52:55], a[68:71], v[112:115]\n"
  "s_mul_i32 s62, s61, 1920\n"
  "s_add_u32 m0, s64, 81920\n"
  "s_movk_i32 s70, 4096\n"
  "s_nop 0\n"
  "v_mfma_f32_16x16x32_bf16 v[116:119], a[52:55], a[72:75], v[116:119]\n"
  "buffer_load_dwordx4 v6, s[24:27], s61 offen lds\n"
  "buffer_load_dwordx4 v[40:43], v18, s[36:39], s70 offen sc1\n"
  "v_mfma_f32_16x16x32_bf16 v[120:123], a[52:55], a[76:79], v[120:123]\n"
  "ds_read_b128 a[16:19], v16 offset:20480\n"
  "buffer_load_dwordx4 v[44:47], v18, s[36:39], s70 offen offset:1024 sc1\n"
  "v_mfma_f32_16x16x32_bf16 v[124:127], a[52:55], a[80:83], v[124:127]\n"
  "ds_read_b128 a[20:23], v16 offset:22528\n"
  "v_mfma_f32_16x16x32_bf16 v[128:131], a[52:55], a[84:87], v[128:131]\n"
  "ds_read_b128 a[24:27], v16 offset:24576\n"
  "v_mfma_f32_16x16x32_bf16 v[132:135], a[52:55], a[88:91], v[132:135]\n"
  "s_add_u32 m0, s64, 86016\n"
  "v_mfma_f32_16x16x32_bf16 v[136:139], a[52:55], a[92:95], v[136:139]\n"
  "buffer_load_dwordx4 v7, s[24:27], s61 offen lds\n"
  "v_mfma_f32_16x16x32_bf16 v[140:143], a[52:55], a[96:99], v[140:143]\n"
  "ds_read_b128 a[28:31], v16 offset:26624\n"
  "v_mfma_f32_16x16x32_bf16 v[144:147], a[52:55], a[100:103], v[144:147]\n"
  "ds_read_b128 a[32:35], v16 offset:28672\n"
  "v_mfma_f32_16x16x32_bf16 v[148:151], a[56:59], a[68:71], v[148:151]\n"
  "ds_read_b128 a[36:39], v16 offset:30720\n"
  "v_mfma_f32_16x16x32_bf16 v[152:155], a[56:59], a[72:75], v[152:155]\n"
  "s_add_u32 m0, s64, 49152\n"
  "v_mfma_f32_16x16x32_bf16 v[156:159], a[56:59], a[76:79], v[156:159]\n"
  "buffer_load_dwordx4 v2, s[28:31], s62 offen lds\n"
  "v_mfma_f32_16x16x32_bf16 v[160:163], a[56:59], a[80:83], v[160:163]\n"
  "ds_read_b128 a[40:43], v16 offset:32768\n"
  "v_mfma_f32_16x16x32_bf16 v[164:167], a[56:59], a[84:87], v[164:167]\n"
  "ds_read_b128 a[44:47], v16 offset:34816\n"
  "v_mfma_f32_16x16x32_bf16 v[168:171], a[56:59], a[88:91], v[168:171]\n"
  "ds_read_b128 a[48:51], v16 offset:36864\n"
  "v_mfma_f32_16x16x32_bf16 v[172:175], a[56:59], a[92:95], v[172:175]\n"
  "s_add_u32 m0, s64, 53248\n"
  "v_mfma_f32_16x16x32_bf16 v[176:179], a[56:59], a[96:99], v[176:179]\n"
  "buffer_load_dwordx4 v3, s[28:31], s62 offen lds\n"
  "v_mfma_f32_16x16x32_bf16 v[180:183], a[56:59], a[100:103], v[180:183]\n"
  "ds_read_b64_tr_b16 a[0:1], v13 offset:0\n"
  "v_mfma_f32_16x16x32_bf16 v[184:187], a[60:63], a[68:71], v[184:187]\n"
  "ds_read_b64_tr_b16 a[2:3], v13 offset:3840\n"
  "v_mfma_f32_16x16x32_bf16 v[188:191], a[60:63], a[72:75], v[188:191]\n"
  "ds_read_b64_tr_b16 a[4:5], v13 offset:160\n"
  "v_mfma_f32_16x16x32_bf16 v[192:195], a[60:63], a[76:79], v[192:195]\n"
  "s_add_u32 m0, s64, 57344\n"
  "v_mfma_f32_16x16x32_bf16 v[196:199], a[60:63], a[80:83], v[196:199]\n"
  "buffer_load_dwordx4 v4, s[28:31], s62 offen lds\n"
  "v_mfma_f32_16x16x32_bf16 v[200:203], a[60:63], a[84:87], v[200:203]\n"
  "ds_read_b64_tr_b16 a[6:7], v13 offset:4000\n"
  "v_mfma_f32_16x16x32_bf16 v[204:207], a[60:63], a[88:91], v[204:207]\n"
  "ds_read_b64_tr_b16 a[8:9], v13 offset:320\n"
  "v_mfma_f32_16x16x32_bf16 v[208:211], a[60:63], a[92:95], v[208:211]\n"
  "ds_read_b64_tr_b16 a[10:11], v13 offset:4160\n"
  "v_mfma_f32_16x16x32_bf16 v[212:215], a[60:63], a[96:99], v[212:215]\n"
  "s_add_u32 m0, s64, 61440\n"
  "v_mfma_f32_16x16x32_bf16 v[216:219], a[60:63], a[100:103], v[216:219]\n"
  "buffer_load_dwordx4 v5, s[28:31], s62 offen lds\n"
  "v_mfma_f32_16x16x32_bf16 v[220:223], a[64:67], a[68:71], v[220:223]\n"
  "ds_read_b64_tr_b16 a[12:13], v15 offset:0\n"
  "v_mfma_f32_16x16x32_bf16 v[224:227], a[64:67], a[72:75], v[224:227]\n"
  "ds_read_b64_tr_b16 a[14:15], v15 offset:3840\n"
  "v_mfma_f32_16x16x32_bf16 v[228:231], a[64:67], a[76:79], v[228:231]\n"
  "v_mfma_f32_16x16x32_bf16 v[232:235], a[64:67], a[80:83], v[232:235]\n"
  "v_mfma_f32_16x16x32_bf16 v[236:239], a[64:67], a[84:87], v[236:239]\n"
  "v_mfma_f32_16x16x32_bf16 v[240:243], a[64:67], a[88:91], v[240:243]\n"
  "v_mfma_f32_16x16x32_bf16 v[244:247], a[64:67], a[92:95], v[244:247]\n"
  "v_mfma_f32_16x16x32_bf16 v[248:251], a[64:67], a[96:99], v[248:251]\n"
  "v_mfma_f32_16x16x32_bf16 v[252:255], a[64:67], a[100:103], v[252:255]\n"
  "s_waitcnt vmcnt(21) lgkmcnt(0)\n"
  "s_barrier\n"
  "v_mfma_f32_16x16x32_bf16 v[112:115], a[0:3], a[16:19], v[112:115]\n"
  "s_add_u32 s63, s62, 122880\n"
  "s_add_u32 m0, s64, 90112\n"
  "v_mfma_f32_16x16x32_bf16 v[116:119], a[0:3], a[20:23], v[116:119]\n"
  "buffer_load_dwordx4 v8, s[24:27], s61 offen lds\n"
  "v_mfma_f32_16x16x32_bf16 v[120:123], a[0:3], a[24:27], v[120:123]\n"
  "ds_read_b128 a[68:71], v17 offset:20480\n"
  "v_mfma_f32_16x16x32_bf16 v[124:127], a[0:3], a[28:31], v[124:127]\n"
  "ds_read_b128 a[72:75], v17 offset:22528\n"
  "v_mfma_f32_16x16x32_bf16 v[128:131], a[0:3], a[32:35], v[128:131]\n"
  "ds_read_b128 a[76:79], v17 offset:24576\n"
  "v_mfma_f32_16x16x32_bf16 v[132:135], a[0:3], a[36:39], v[132:135]\n"
  "s_add_u32 m0, s64, 94208\n"
  "v_mfma_f32_16x16x32_bf16 v[136:139], a[0:3], a[40:43], v[136:139]\n"
  "buffer_load_dwordx4 v9, s[24:27], s61 offen lds\n"
  "v_mfma_f32_16x16x32_bf16 v[140:143], a[0:3], a[44:47], v[140:143]\n"
  "ds_read_b128 a[80:83], v17 offset:26624\n"
  "v_mfma_f32_16x16x32_bf16 v[144:147], a[0:3], a[48:51], v[144:147]\n"
  "ds_read_b128 a[84:87], v17 offset:28672\n"
  "v_mfma_f32_16x16x32_bf16 v[148:151], a[4:7], a[16:19], v[148:151]\n"
  "ds_read_b128 a[88:91], v17 offset:30720\n"
  "v_mfma_f32_16x16x32_bf16 v[152:155], a[4:7], a[20:23], v[152:155]\n"
  "s_add_u32 m0, s64, 98304\n"
  "v_mfma_f32_16x16x32_bf16 v[156:159], a[4:7], a[24:27], v[156:159]\n"
  "s_mov_b64 exec, s[74:75]\n"
  "s_nop 0\n"
  "buffer_load_dwordx4 v10, s[24:27], s61 offen lds\n"
  "s_mov_b64 exec, -1\n"
  "s_nop 0\n"
  "v_mfma_f32_16x16x32_bf16 v[160:163], a[4:7], a[28:31], v[160:163]\n"
  "ds_read_b128 a[92:95], v17 offset:32768\n"
  "v_mfma_f32_16x16x32_bf16 v[164:167], a[4:7], a[32:35], v[164:167]\n"
  "ds_read_b128 a[96:99], v17 offset:34816\n"
  "v_mfma_f32_16x16x32_bf16 v[168:171], a[4:7], a[36:39], v[168:171]\n"
  "ds_read_b128 a[100:103], v17 offset:36864\n"
  "v_mfma_f32_16x16x32_bf16 v[172:175], a[4:7], a[40:43], v[172:175]\n"
  "s_add_u32 m0, s64, 65536\n"
  "v_mfma_f32_16x16x32_bf16 v[176:179], a[4:7], a[44:47], v[176:179]\n"
  "buffer_load_dwordx4 v2, s[28:31], s63 offen lds\n"
  "v_mfma_f32_16x16x32_bf16 v[180:183], a[4:7], a[48:51], v[180:183]\n"
  "ds_read_b64_tr_b16 a[52:53], v12 offset:0\n"
  "v_mfma_f32_16x16x32_bf16 v[184:187], a[8:11], a[16:19], v[184:187]\n"
  "ds_read_b64_tr_b16 a[54:55], v12 offset:3840\n"
  "v_mfma_f32_16x16x32_bf16 v[188:191], a[8:11], a[20:23], v[188:191]\n"
  "ds_read_b64_tr_b16 a[56:57], v12 offset:160\n"
  "v_mfma_f32_16x16x32_bf16 v[192:195], a[8:11], a[24:27], v[192:195]\n"
  "s_add_u32 m0, s64, 69632\n"
  "v_mfma_f32_16x16x32_bf16 v[196:199], a[8:11], a[28:31], v[196:199]\n"
  "buffer_load_dwordx4 v3, s[28:31], s63 offen lds\n"
  "v_mfma_f32_16x16x32_bf16 v[200:203], a[8:11], a[32:35], v[200:203]\n"
  "ds_read_b64_tr_b16 a[58:59], v12 offset:4000\n"
  "v_mfma_f32_16x16x32_bf16 v[204:207], a[8:11], a[36:39], v[204:207]\n"
  "ds_read_b64_tr_b16 a[60:61], v12 offset:320\n"
  "v_mfma_f32_16x16x32_bf16 v[208:211], a[8:11], a[40:43], v[208:211]\n"
  "ds_read_b64_tr_b16 a[62:63], v12 offset:4160\n"
  "v_mfma_f32_16x16x32_bf16 v[212:215], a[8:11], a[44:47], v[212:215]\n"
  "s_add_u32 m0, s64, 73728\n"
  "v_mfma_f32_16x16x32_bf16 v[216:219], a[8:11], a[48:51], v[216:219]\n"
  "buffer_load_dwordx4 v4, s[28:31], s63 offen lds\n"
  "v_mfma_f32_16x16x32_bf16 v[220:223], a[12:15], a[16:19], v[220:223]\n"
  "ds_read_b64_tr_b16 a[64:65], v14 offset:0\n"
  "v_mfma_f32_16x16x32_bf16 v[224:227], a[12:15], a[20:23], v[224:227]\n"
  "ds_read_b64_tr_b16 a[66:67], v14 offset:3840\n"
  "v_mfma_f32_16x16x32_bf16 v[228:231], a[12:15], a[24:27], v[228:231]\n"
  "v_mfma_f32_16x16x32_bf16 v[232:235], a[12:15], a[28:31], v[232:235]\n"
  "s_add_u32 m0, s64, 77824\n"
  "v_mfma_f32_16x16x32_bf16 v[236:239], a[12:15], a[32:35], v[236:239]\n"
  "buffer_load_dwordx4 v5, s[28:31], s63 offen lds\n"
  "v_mfma_f32_16x16x32_bf16 v[240:243], a[12:15], a[36:39], v[240:243]\n"
  "v_mfma_f32_16x16x32_bf16 v[244:247], a[12:15], a[40:43], v[244:247]\n"
  "v_mfma_f32_16x16x32_bf16 v[248:251], a[12:15], a[44:47], v[248:251]\n"
  "v_mfma_f32_16x16x32_bf16 v[252:255], a[12:15], a[48:51], v[252:255]\n"
  "s_waitcnt vmcnt(12) lgkmcnt(0)\n"
  "s_barrier\n"
  "v_cmp_ne_u64_e64 s[66:67], s[44:45], v[40:41]\n"
  "v_cmp_ne_u64_e64 s[68:69], s[44:45], v[42:43]\n"
  "s_nop 0\n"
  "s_or_b64 s[66:67], s[66:67], s[68:69]\n"
  "v_cmp_ne_u64_e64 s[68:69], s[44:45], v[44:45]\n"
  "s_nop 0\n"
  "s_or_b64 s[66:67], s[66:67], s[68:69]\n"
  "v_cmp_ne_u64_e64 s[68:69], s[44:45], v[46:47]\n"
  "s_nop 0\n"
  "s_or_b64 s[66:67], s[66:67], s[68:69]\n"
  "s_cmp_eq_u64 s[66:67], 0\n"
  "s_cbranch_scc1 .Lpok23\n"
  "s_mov_b32 s87, 0x40000\n"
  ".Lpsp23:\n"
  "s_sleep 2\n"
  "s_movk_i32 s70, 4096\n"
  "buffer_load_dwordx4 v[40:43], v18, s[36:39], s70 offen sc1\n"
  "buffer_load_dwordx4 v[44:47], v18, s[36:39], s70 offen offset:1024 sc1\n"
  "s_waitcnt vmcnt(0)\n"
  "s_nop 0\n"
  "v_cmp_ne_u64_e64 s[66:67], s[44:45], v[40:41]\n"
  "v_cmp_ne_u64_e64 s[68:69], s[44:45], v[42:43]\n"
  "s_nop 0\n"
  "s_or_b64 s[66:67], s[66:67], s[68:69]\n"
  "v_cmp_ne_u64_e64 s[68:69], s[44:45], v[44:45]\n"
  "s_nop 0\n"
  "s_or_b64 s[66:67], s[66:67], s[68:69]\n"
  "v_cmp_ne_u64_e64 s[68:69], s[44:45], v[46:47]\n"
  "s_nop 0\n"
  "s_or_b64 s[66:67], s[66:67], s[68:69]\n"
  "s_cmp_eq_u64 s[66:67], 0\n"
  "s_cbranch_scc1 .Lpok23\n"
  "s_sub_u32 s87, s87, 1\n"
  "s_cmp_lg_u32 s87, 0\n"
  "s_cbranch_scc1 .Lpsp23\n"
  ".Lpok23:\n"
  "s_nop 0\n"
  "v_mfma_f32_16x16x32_bf16 v[112:115], a[52:55], a[68:71], v[112:115]\n"
  "ds_read_b128 a[16:19], v16 offset:40960\n"
  "v_and_b32_e64 v21, 15, v0\n"
  "v_mfma_f32_16x16x32_bf16 v[116:119], a[52:55], a[72:75], v[116:119]\n"
  "ds_read_b128 a[20:23], v16 offset:43008\n"
  "v_lshlrev_b32_e64 v20, 3, v21\n"
  "v_mfma_f32_16x16x32_bf16 v[120:123], a[52:55], a[76:79], v[120:123]\n"
  "ds_read_b128 a[24:27], v16 offset:45056\n"
  "v_mfma_f32_16x16x32_bf16 v[124:127], a[52:55], a[80:83], v[124:127]\n"
  "ds_read_b128 a[28:31], v16 offset:47104\n"
  "s_lshl_b32 s70, s47, 10\n"
  "s_nop 0\n"
  "v_mfma_f32_16x16x32_bf16 v[128:131], a[52:55], a[84:87], v[128:131]\n"
  "ds_read_b128 a[32:35], v16 offset:49152\n"
  "s_add_u32 s70, s70, 32768\n"
  "v_mfma_f32_16x16x32_bf16 v[132:135], a[52:55], a[88:91], v[132:135]\n"
  "ds_read_b128 a[36:39], v16 offset:51200\n"
  "v_mfma_f32_16x16x32_bf16 v[136:139], a[52:55], a[92:95], v[136:139]\n"
  "ds_read_b128 a[40:43], v16 offset:53248\n"
  "buffer_load_dwordx2 v[24:25], v20, s[36:39], s70 offen sc1\n"
  "v_mfma_f32_16x16x32_bf16 v[140:143], a[52:55], a[96:99], v[140:143]\n"
  "ds_read_b128 a[44:47], v16 offset:55296\n"
  "buffer_load_dwordx2 v[26:27], v20, s[36:39], s70 offen offset:128 sc1\n"
  "v_mfma_f32_16x16x32_bf16 v[144:147], a[52:55], a[100:103], v[144:147]\n"
  "ds_read_b128 a[48:51], v16 offset:57344\n"
  "v_mfma_f32_16x16x32_bf16 v[148:151], a[56:59], a[68:71], v[148:151]\n"
  "ds_read_b64_tr_b16 a[0:1], v12 offset:16384\n"
  "buffer_load_dwordx2 v[28:29], v20, s[36:39], s70 offen offset:256 sc1\n"
  "v_mfma_f32_16x16x32_bf16 v[152:155], a[56:59], a[72:75], v[152:155]\n"
  "ds_read_b64_tr_b16 a[2:3], v12 offset:20224\n"
  "buffer_load_dwordx2 v[30:31], v20, s[36:39], s70 offen offset:384 sc1\n"
  "v_mfma_f32_16x16x32_bf16 v[156:159], a[56:59], a[76:79], v[156:159]\n"
  "ds_read_b64_tr_b16 a[4:5], v12 offset:16544\n"
  "v_mfma_f32_16x16x32_bf16 v[160:163], a[56:59], a[80:83], v[160:163]\n"
  "ds_read_b64_tr_b16 a[6:7], v12 offset:20384\n"
  "buffer_load_dwordx2 v[32:33], v20, s[36:39], s70 offen offset:512 sc1\n"
  "v_mfma_f32_16x16x32_bf16 v[164:167], a[56:59], a[84:87], v[164:167]\n"
  "ds_read_b64_tr_b16 a[8:9], v12 offset:16704\n"
  "buffer_load_dwordx2 v[34:35], v20, s[36:39], s70 offen offset:640 sc1\n"
  "v_mfma_f32_16x16x32_bf16 v[168:171], a[56:59], a[88:91], v[168:171]\n"
  "ds_read_b64_tr_b16 a[10:11], v12 offset:20544\n"
  "v_mfma_f32_16x16x32_bf16 v[172:175], a[56:59], a[92:95], v[172:175]\n"
  "ds_read_b64_tr_b16 a[12:13], v14 offset:16384\n"
  "buffer_load_dwordx2 v[36:37], v20, s[36:39], s70 offen offset:768 sc1\n"
  "v_mfma_f32_16x16x32_bf16 v[176:179], a[56:59], a[96:99], v[176:179]\n"
  "ds_read_b64_tr_b16 a[14:15], v14 offset:20224\n"
  "buffer_load_dwordx2 v[38:39], v20, s[36:39], s70 offen offset:896 sc1\n"
  "v_mfma_f32_16x16x32_bf16 v[180:183], a[56:59], a[100:103], v[180:183]\n"
  "v_mfma_f32_16x16x32_bf16 v[184:187], a[60:63], a[68:71], v[184:187]\n"
  "v_cmp_gt_u32 vcc, 8, v21\n"
  "s_nop 0\n"
  "v_mfma_f32_16x16x32_bf16 v[188:191], a[60:63], a[72:75], v[188:191]\n"
  "v_add_u32 v22, 192, v20\n"
  "v_mfma_f32_16x16x32_bf16 v[192:195], a[60:63], a[76:79], v[192:195]\n"
  "v_mfma_f32_16x16x32_bf16 v[196:199], a[60:63], a[80:83], v[196:199]\n"
  "v_cndmask_b32 v22, v20, v22, vcc\n"
  "s_nop 0\n"
  "v_mfma_f32_16x16x32_bf16 v[200:203], a[60:63], a[84:87], v[200:203]\n"
  "s_sub_u32 s71, s70, 256\n"
  "v_mfma_f32_16x16x32_bf16 v[204:207], a[60:63], a[88:91], v[204:207]\n"
  "v_mfma_f32_16x16x32_bf16 v[208:211], a[60:63], a[92:95], v[208:211]\n"
  "s_cmp_eq_u32 s47, 0\n"
  "s_nop 0\n"
  "v_mfma_f32_16x16x32_bf16 v[212:215], a[60:63], a[96:99], v[212:215]\n"
  "s_cselect_b32 s71, s70, s71\n"
  "s_nop 0\n"
  "v_mfma_f32_16x16x32_bf16 v[216:219], a[60:63], a[100:103], v[216:219]\n"
  "v_mfma_f32_16x16x32_bf16 v[220:223], a[64:67], a[68:71], v[220:223]\n"
  "buffer_load_dwordx2 v[40:41], v22, s[36:39], s71 offen sc1\n"
  "v_mfma_f32_16x16x32_bf16 v[224:227], a[64:67], a[72:75], v[224:227]\n"
  "s_add_u32 s72, s80, 0\n"
  "s_nop 0\n"
  "v_mfma_f32_16x16x32_bf16 v[228:231], a[64:67], a[76:79], v[228:231]\n"
  "v_mfma_f32_16x16x32_bf16 v[232:235], a[64:67], a[80:83], v[232:235]\n"
  "buffer_load_dwordx4 a[128:131], v18, s[36:39], s72 offen sc1\n"
  "v_mfma_f32_16x16x32_bf16 v[236:239], a[64:67], a[84:87], v[236:239]\n"
  "buffer_load_dwordx4 a[132:135], v18, s[36:39], s72 offen offset:1024 sc1\n"
  "v_mfma_f32_16x16x32_bf16 v[240:243], a[64:67], a[88:91], v[240:243]\n"
  "v_mfma_f32_16x16x32_bf16 v[244:247], a[64:67], a[92:95], v[244:247]\n"
  "buffer_load_dwordx4 a[136:139], v18, s[36:39], s72 offen offset:2048 sc1\n"
  "v_mfma_f32_16x16x32_bf16 v[248:251], a[64:67], a[96:99], v[248:251]\n"
  "v_mfma_f32_16x16x32_bf16 v[252:255], a[64:67], a[100:103], v[252:255]\n"
  "s_waitcnt lgkmcnt(0)\n"
  "s_barrier\n"
  "v_mfma_f32_16x16x32_bf16 v[112:115], a[0:3], a[16:19], v[112:115]\n"
  "ds_read_b128 a[68:71], v17 offset:40960\n"
  "buffer_load_dwordx4 a[140:143], v18, s[36:39], s72 offen offset:3072 sc1\n"
  "v_mfma_f32_16x16x32_bf16 v[116:119], a[0:3], a[20:23], v[116:119]\n"
  "ds_read_b128 a[72:75], v17 offset:43008\n"
  "v_mfma_f32_16x16x32_bf16 v[120:123], a[0:3], a[24:27], v[120:123]\n"
  "ds_read_b128 a[76:79], v17 offset:45056\n"
  "v_mfma_f32_16x16x32_bf16 v[124:127], a[0:3], a[28:31], v[124:127]\n"
  "ds_read_b128 a[80:83], v17 offset:47104\n"
  "v_mfma_f32_16x16x32_bf16 v[128:131], a[0:3], a[32:35], v[128:131]\n"
  "ds_read_b128 a[84:87], v17 offset:49152\n"
  "buffer_load_dwordx4 a[144:147], v19, s[36:39], s72 offen sc1\n"
  "v_mfma_f32_16x16x32_bf16 v[132:135], a[0:3], a[36:39], v[132:135]\n"
  "ds_read_b128 a[88:91], v17 offset:51200\n"
  "v_mfma_f32_16x16x32_bf16 v[136:139], a[0:3], a[40:43], v[136:139]\n"
  "ds_read_b128 a[92:95], v17 offset:53248\n"
  "v_mfma_f32_16x16x32_bf16 v[140:143], a[0:3], a[44:47], v[140:143]\n"
  "ds_read_b128 a[96:99], v17 offset:55296\n"
  "v_mfma_f32_16x16x32_bf16 v[144:147], a[0:3], a[48:51], v[144:147]\n"
  "ds_read_b128 a[100:103], v17 offset:57344\n"
  "s_add_u32 s72, s80, 5120\n"
  "v_mfma_f32_16x16x32_bf16 v[148:151], a[4:7], a[16:19], v[148:151]\n"
  "ds_read_b64_tr_b16 a[52:53], v12 offset:32768\n"
  "v_mfma_f32_16x16x32_bf16 v[152:155], a[4:7], a[20:23], v[152:155]\n"
  "ds_read_b64_tr_b16 a[54:55], v12 offset:36608\n"
  "v_mfma_f32_16x16x32_bf16 v[156:159], a[4:7], a[24:27], v[156:159]\n"
  "ds_read_b64_tr_b16 a[56:57], v12 offset:32928\n"
  "v_mfma_f32_16x16x32_bf16 v[160:163], a[4:7], a[28:31], v[160:163]\n"
  "ds_read_b64_tr_b16 a[58:59], v12 offset:36768\n"
  "v_mfma_f32_16x16x32_bf16 v[164:167], a[4:7], a[32:35], v[164:167]\n"
  "ds_read_b64_tr_b16 a[60:61], v12 offset:33088\n"
  "buffer_load_dwordx4 a[148:151], v18, s[36:39], s72 offen sc1\n"
  "v_mfma_f32_16x16x32_bf16 v[168:171], a[4:7], a[36:39], v[168:171]\n"
  "ds_read_b64_tr_b16 a[62:63], v12 offset:36928\n"
  "v_mfma_f32_16x16x32_bf16 v[172:175], a[4:7], a[40:43], v[172:175]\n"
  "ds_read_b64_tr_b16 a[64:65], v14 offset:32768\n"
  "v_mfma_f32_16x16x32_bf16 v[176:179], a[4:7], a[44:47], v[176:179]\n"
  "ds_read_b64_tr_b16 a[66:67], v14 offset:36608\n"
  "v_mfma_f32_16x16x32_bf16 v[180:183], a[4:7], a[48:51], v[180:183]\n"
  "buffer_load_dwordx4 a[152:155], v18, s[36:39], s72 offen offset:1024 sc1\n"
  "v_mfma_f32_16x16x32_bf16 v[184:187], a[8:11], a[16:19], v[184:187]\n"
  "v_mfma_f32_16x16x32_bf16 v[188:191], a[8:11], a[20:23], v[188:191]\n"
  "v_mfma_f32_16x16x32_bf16 v[192:195], a[8:11], a[24:27], v[192:195]\n"
  "v_mfma_f32_16x16x32_bf16 v[196:199], a[8:11], a[28:31], v[196:199]\n"
  "buffer_load_dwordx4 a[156:159], v18, s[36:39], s72 offen offset:2048 sc1\n"
  "v_mfma_f32_16x16x32_bf16 v[200:203], a[8:11], a[32:35], v[200:203]\n"
  "v_mfma_f32_16x16x32_bf16 v[204:207], a[8:11], a[36:39], v[204:207]\n"
  "v_mfma_f32_16x16x32_bf16 v[208:211], a[8:11], a[40:43], v[208:211]\n"
  "v_mfma_f32_16x16x32_bf16 v[212:215], a[8:11], a[44:47], v[212:215]\n"
  "v_mfma_f32_16x16x32_bf16 v[216:219], a[8:11], a[48:51], v[216:219]\n"
  "buffer_load_dwordx4 a[160:163], v18, s[36:39], s72 offen offset:3072 sc1\n"
  "v_mfma_f32_16x16x32_bf16 v[220:223], a[12:15], a[16:19], v[220:223]\n"
  "v_mfma_f32_16x16x32_bf16 v[224:227], a[12:15], a[20:23], v[224:227]\n"
  "v_mfma_f32_16x16x32_bf16 v[228:231], a[12:15], a[24:27], v[228:231]\n"
  "v_mfma_f32_16x16x32_bf16 v[232:235], a[12:15], a[28:31], v[232:235]\n"
  "buffer_load_dwordx4 a[164:167], v19, s[36:39], s72 offen sc1\n"
  "v_mfma_f32_16x16x32_bf16 v[236:239], a[12:15], a[32:35], v[236:239]\n"
  "v_mfma_f32_16x16x32_bf16 v[240:243], a[12:15], a[36:39], v[240:243]\n"
  "v_mfma_f32_16x16x32_bf16 v[244:247], a[12:15], a[40:43], v[244:247]\n"
  "v_mfma_f32_16x16x32_bf16 v[248:251], a[12:15], a[44:47], v[248:251]\n"
  "v_mfma_f32_16x16x32_bf16 v[252:255], a[12:15], a[48:51], v[252:255]\n"
  "s_waitcnt vmcnt(23) lgkmcnt(0)\n"
  "s_barrier\n"
  "v_mfma_f32_16x16x32_bf16 v[112:115], a[52:55], a[68:71], v[112:115]\n"
  "ds_read_b128 a[16:19], v16 offset:0\n"
  "s_add_u32 s72, s80, 10240\n"
  "v_mfma_f32_16x16x32_bf16 v[116:119], a[52:55], a[72:75], v[116:119]\n"
  "ds_read_b128 a[20:23], v16 offset:2048\n"
  "v_mfma_f32_16x16x32_bf16 v[120:123], a[52:55], a[76:79], v[120:123]\n"
  "ds_read_b128 a[24:27], v16 offset:4096\n"
  "v_mfma_f32_16x16x32_bf16 v[124:127], a[52:55], a[80:83], v[124:127]\n"
  "ds_read_b128 a[28:31], v16 offset:6144\n"
  "buffer_load_dwordx4 a[168:171], v18, s[36:39], s72 offen sc1\n"
  "v_mfma_f32_16x16x32_bf16 v[128:131], a[52:55], a[84:87], v[128:131]\n"
  "ds_read_b128 a[32:35], v16 offset:8192\n"
  "v_mfma_f32_16x16x32_bf16 v[132:135], a[52:55], a[88:91], v[132:135]\n"
  "ds_read_b128 a[36:39], v16 offset:10240\n"
  "v_mfma_f32_16x16x32_bf16 v[136:139], a[52:55], a[92:95], v[136:139]\n"
  "ds_read_b128 a[40:43], v16 offset:12288\n"
  "v_mfma_f32_16x16x32_bf16 v[140:143], a[52:55], a[96:99], v[140:143]\n"
  "ds_read_b128 a[44:47], v16 offset:14336\n"
  "buffer_load_dwordx4 a[172:175], v18, s[36:39], s72 offen offset:1024 sc1\n"
  "v_mfma_f32_16x16x32_bf16 v[144:147], a[52:55], a[100:103], v[144:147]\n"
  "ds_read_b128 a[48:51], v16 offset:16384\n"
  "v_mfma_f32_16x16x32_bf16 v[148:151], a[56:59], a[68:71], v[148:151]\n"
  "ds_read_b64_tr_b16 a[0:1], v12 offset:49152\n"
  "v_mfma_f32_16x16x32_bf16 v[152:155], a[56:59], a[72:75], v[152:155]\n"
  "ds_read_b64_tr_b16 a[2:3], v12 offset:52992\n"
  "v_mfma_f32_16x16x32_bf16 v[156:159], a[56:59], a[76:79], v[156:159]\n"
  "ds_read_b64_tr_b16 a[4:5], v12 offset:49312\n"
  "buffer_load_dwordx4 a[176:179], v18, s[36:39], s72 offen offset:2048 sc1\n"
  "v_mfma_f32_16x16x32_bf16 v[160:163], a[56:59], a[80:83], v[160:163]\n"
  "ds_read_b64_tr_b16 a[6:7], v12 offset:53152\n"
  "v_mfma_f32_16x16x32_bf16 v[164:167], a[56:59], a[84:87], v[164:167]\n"
  "ds_read_b64_tr_b16 a[8:9], v12 offset:49472\n"
  "v_mfma_f32_16x16x32_bf16 v[168:171], a[56:59], a[88:91], v[168:171]\n"
  "ds_read_b64_tr_b16 a[10:11], v12 offset:53312\n"
  "v_mfma_f32_16x16x32_bf16 v[172:175], a[56:59], a[92:95], v[172:175]\n"
  "ds_read_b64_tr_b16 a[12:13], v14 offset:49152\n"
  "buffer_load_dwordx4 a[180:183], v18, s[36:39], s72 offen offset:3072 sc1\n"
  "v_mfma_f32_16x16x32_bf16 v[176:179], a[56:59], a[96:99], v[176:179]\n"
  "ds_read_b64_tr_b16 a[14:15], v14 offset:52992\n"
  "v_mfma_f32_16x16x32_bf16 v[180:183], a[56:59], a[100:103], v[180:183]\n"
  "v_mfma_f32_16x16x32_bf16 v[184:187], a[60:63], a[68:71], v[184:187]\n"
  "v_mfma_f32_16x16x32_bf16 v[188:191], a[60:63], a[72:75], v[188:191]\n"
  "buffer_load_dwordx4 a[184:187], v19, s[36:39], s72 offen sc1\n"
  "v_mfma_f32_16x16x32_bf16 v[192:195], a[60:63], a[76:79], v[192:195]\n"
  "v_mfma_f32_16x16x32_bf16 v[196:199], a[60:63], a[80:83], v[196:199]\n"
  "v_mfma_f32_16x16x32_bf16 v[200:203], a[60:63], a[84:87], v[200:203]\n"
  "v_mfma_f32_16x16x32_bf16 v[204:207], a[60:63], a[88:91], v[204:207]\n"
  "s_add_u32 s72, s80, 15360\n"
  "v_mfma_f32_16x16x32_bf16 v[208:211], a[60:63], a[92:95], v[208:211]\n"
  "v_mfma_f32_16x16x32_bf16 v[212:215], a[60:63], a[96:99], v[212:215]\n"
  "v_mfma_f32_16x16x32_bf16 v[216:219], a[60:63], a[100:103], v[216:219]\n"
  "v_mfma_f32_16x16x32_bf16 v[220:223], a[64:67], a[68:71], v[220:223]\n"
  "buffer_load_dwordx4 a[188:191], v18, s[36:39], s72 offen sc1\n"
  "v_mfma_f32_16x16x32_bf16 v[224:227], a[64:67], a[72:75], v[224:227]\n"
  "v_mfma_f32_16x16x32_bf16 v[228:231], a[64:67], a[76:79], v[228:231]\n"
  "v_mfma_f32_16x16x32_bf16 v[232:235], a[64:67], a[80:83], v[232:235]\n"
  "v_mfma_f32_16x16x32_bf16 v[236:239], a[64:67], a[84:87], v[236:239]\n"
  "buffer_load_dwordx4 a[192:195], v18, s[36:39], s72 offen offset:1024 sc1\n"
  "v_mfma_f32_16x16x32_bf16 v[240:243], a[64:67], a[88:91], v[240:243]\n"
  "v_mfma_f32_16x16x32_bf16 v[244:247], a[64:67], a[92:95], v[244:247]\n"
  "v_mfma_f32_16x16x32_bf16 v[248:251], a[64:67], a[96:99], v[248:251]\n"
  "v_mfma_f32_16x16x32_bf16 v[252:255], a[64:67], a[100:103], v[252:255]\n"
  "s_waitcnt vmcnt(26) lgkmcnt(0)\n"
  "s_barrier\n"
  "v_mfma_f32_16x16x32_bf16 v[112:115], a[0:3], a[16:19], v[112:115]\n"
  "ds_read_b128 a[68:71], v17 offset:0\n"
  "buffer_load_dwordx4 a[196:199], v18, s[36:39], s72 offen offset:2048 sc1\n"
  "v_mfma_f32_16x16x32_bf16 v[116:119], a[0:3], a[20:23], v[116:119]\n"
  "ds_read_b128 a[72:75], v17 offset:2048\n"
  "v_mfma_f32_16x16x32_bf16 v[120:123], a[0:3], a[24:27], v[120:123]\n"
  "ds_read_b128 a[76:79], v17 offset:4096\n"
  "v_mfma_f32_16x16x32_bf16 v[124:127], a[0:3], a[28:31], v[124:127]\n"
  "ds_read_b128 a[80:83], v17 offset:6144\n"
  "v_mfma_f32_16x16x32_bf16 v[128:131], a[0:3], a[32:35], v[128:131]\n"
  "ds_read_b128 a[84:87], v17 offset:8192\n"
  "buffer_load_dwordx4 a[200:203], v18, s[36:39], s72 offen offset:3072 sc1\n"
  "v_mfma_f32_16x16x32_bf16 v[132:135], a[0:3], a[36:39], v[132:135]\n"
  "ds_read_b128 a[88:91], v17 offset:10240\n"
  "v_mfma_f32_16x16x32_bf16 v[136:139], a[0:3], a[40:43], v[136:139]\n"
  "ds_read_b128 a[92:95], v17 offset:12288\n"
  "v_mfma_f32_16x16x32_bf16 v[140:143], a[0:3], a[44:47], v[140:143]\n"
  "ds_read_b128 a[96:99], v17 offset:14336\n"
  "v_mfma_f32_16x16x32_bf16 v[144:147], a[0:3], a[48:51], v[144:147]\n"
  "ds_read_b128 a[100:103], v17 offset:16384\n"
  "buffer_load_dwordx4 a[204:207], v19, s[36:39], s72 offen sc1\n"
  "v_mfma_f32_16x16x32_bf16 v[148:151], a[4:7], a[16:19], v[148:151]\n"
  "ds_read_b64_tr_b16 a[52:53], v13 offset:0\n"
  "v_mfma_f32_16x16x32_bf16 v[152:155], a[4:7], a[20:23], v[152:155]\n"
  "ds_read_b64_tr_b16 a[54:55], v13 offset:3840\n"
  "v_mfma_f32_16x16x32_bf16 v[156:159], a[4:7], a[24:27], v[156:159]\n"
  "ds_read_b64_tr_b16 a[56:57], v13 offset:160\n"
  "v_mfma_f32_16x16x32_bf16 v[160:163], a[4:7], a[28:31], v[160:163]\n"
  "ds_read_b64_tr_b16 a[58:59], v13 offset:4000\n"
  "v_mfma_f32_16x16x32_bf16 v[164:167], a[4:7], a[32:35], v[164:167]\n"
  "ds_read_b64_tr_b16 a[60:61], v13 offset:320\n"
  "s_add_u32 s72, s80, 20480\n"
  "v_mfma_f32_16x16x32_bf16 v[168:171], a[4:7], a[36:39], v[168:171]\n"
  "ds_read_b64_tr_b16 a[62:63], v13 offset:4160\n"
  "v_mfma_f32_16x16x32_bf16 v[172:175], a[4:7], a[40:43], v[172:175]\n"
  "ds_read_b64_tr_b16 a[64:65], v15 offset:0\n"
  "v_mfma_f32_16x16x32_bf16 v[176:179], a[4:7], a[44:47], v[176:179]\n"
  "ds_read_b64_tr_b16 a[66:67], v15 offset:3840\n"
  "v_mfma_f32_16x16x32_bf16 v[180:183], a[4:7], a[48:51], v[180:183]\n"
  "buffer_load_dwordx4 a[208:211], v18, s[36:39], s72 offen sc1\n"
  "v_mfma_f32_16x16x32_bf16 v[184:187], a[8:11], a[16:19], v[184:187]\n"
  "v_mfma_f32_16x16x32_bf16 v[188:191], a[8:11], a[20:23], v[188:191]\n"
  "v_mfma_f32_16x16x32_bf16 v[192:195], a[8:11], a[24:27], v[192:195]\n"
  "v_mfma_f32_16x16x32_bf16 v[196:199], a[8:11], a[28:31], v[196:199]\n"
  "buffer_load_dwordx4 a[212:215], v18, s[36:39], s72 offen offset:1024 sc1\n"
  "v_mfma_f32_16x16x32_bf16 v[200:203], a[8:11], a[32:35], v[200:203]\n"
  "v_mfma_f32_16x16x32_bf16 v[204:207], a[8:11], a[36:39], v[204:207]\n"
  "v_mfma_f32_16x16x32_bf16 v[208:211], a[8:11], a[40:43], v[208:211]\n"
  "v_mfma_f32_16x16x32_bf16 v[212:215], a[8:11], a[44:47], v[212:215]\n"
  "v_mfma_f32_16x16x32_bf16 v[216:219], a[8:11], a[48:51], v[216:219]\n"
  "buffer_load_dwordx4 a[216:219], v18, s[36:39], s72 offen offset:2048 sc1\n"
  "v_mfma_f32_16x16x32_bf16 v[220:223], a[12:15], a[16:19], v[220:223]\n"
  "v_mfma_f32_16x16x32_bf16 v[224:227], a[12:15], a[20:23], v[224:227]\n"
  "v_mfma_f32_16x16x32_bf16 v[228:231], a[12:15], a[24:27], v[228:231]\n"
  "v_mfma_f32_16x16x32_bf16 v[232:235], a[12:15], a[28:31], v[232:235]\n"
  "buffer_load_dwordx4 a[220:223], v18, s[36:39], s72 offen offset:3072 sc1\n"
  "v_mfma_f32_16x16x32_bf16 v[236:239], a[12:15], a[32:35], v[236:239]\n"
  "v_mfma_f32_16x16x32_bf16 v[240:243], a[12:15], a[36:39], v[240:243]\n"
  "v_mfma_f32_16x16x32_bf16 v[244:247], a[12:15], a[40:43], v[244:247]\n"
  "v_mfma_f32_16x16x32_bf16 v[248:251], a[12:15], a[44:47], v[248:251]\n"
  "v_mfma_f32_16x16x32_bf16 v[252:255], a[12:15], a[48:51], v[252:255]\n"
  "s_waitcnt lgkmcnt(0)\n"
  "s_barrier\n"
  "v_mfma_f32_16x16x32_bf16 v[112:115], a[52:55], a[68:71], v[112:115]\n"
  "buffer_load_dwordx4 a[224:227], v19, s[36:39], s72 offen sc1\n"
  "v_mfma_f32_16x16x32_bf16 v[116:119], a[52:55], a[72:75], v[116:119]\n"
  "v_mfma_f32_16x16x32_bf16 v[120:123], a[52:55], a[76:79], v[120:123]\n"
  "s_mul_i32 s72, s81, 5\n"
  "s_nop 0\n"
  "v_mfma_f32_16x16x32_bf16 v[124:127], a[52:55], a[80:83], v[124:127]\n"
  "v_mfma_f32_16x16x32_bf16 v[128:131], a[52:55], a[84:87], v[128:131]\n"
  "v_mfma_f32_16x16x32_bf16 v[132:135], a[52:55], a[88:91], v[132:135]\n"
  "s_add_u32 s72, s72, 25600\n"
  "v_mfma_f32_16x16x32_bf16 v[136:139], a[52:55], a[92:95], v[136:139]\n"
  "v_mfma_f32_16x16x32_bf16 v[140:143], a[52:55], a[96:99], v[140:143]\n"
  "s_add_u32 s72, s72, s80\n"
  "s_nop 0\n"
  "v_mfma_f32_16x16x32_bf16 v[144:147], a[52:55], a[100:103], v[144:147]\n"
  "v_mfma_f32_16x16x32_bf16 v[148:151], a[56:59], a[68:71], v[148:151]\n"
  "v_mfma_f32_16x16x32_bf16 v[152:155], a[56:59], a[72:75], v[152:155]\n"
  "buffer_load_dwordx4 a[228:231], v18, s[36:39], s72 offen sc1\n"
  "v_mfma_f32_16x16x32_bf16 v[156:159], a[56:59], a[76:79], v[156:159]\n"
  "v_mfma_f32_16x16x32_bf16 v[160:163], a[56:59], a[80:83], v[160:163]\n"
  "buffer_load_dwordx4 a[232:235], v18, s[36:39], s72 offen offset:1024 sc1\n"
  "v_mfma_f32_16x16x32_bf16 v[164:167], a[56:59], a[84:87], v[164:167]\n"
  "v_mfma_f32_16x16x32_bf16 v[168:171], a[56:59], a[88:91], v[168:171]\n"
  "v_mfma_f32_16x16x32_bf16 v[172:175], a[56:59], a[92:95], v[172:175]\n"
  "buffer_load_dwordx4 a[236:239], v18, s[36:39], s72 offen offset:2048 sc1\n"
  "v_mfma_f32_16x16x32_bf16 v[176:179], a[56:59], a[96:99], v[176:179]\n"
  "v_mfma_f32_16x16x32_bf16 v[180:183], a[56:59], a[100:103], v[180:183]\n"
  "buffer_load_dwordx4 a[240:243], v18, s[36:39], s72 offen offset:3072 sc1\n"
  "v_mfma_f32_16x16x32_bf16 v[184:187], a[60:63], a[68:71], v[184:187]\n"
  "v_mfma_f32_16x16x32_bf16 v[188:191], a[60:63], a[72:75], v[188:191]\n"
  "v_mfma_f32_16x16x32_bf16 v[192:195], a[60:63], a[76:79], v[192:195]\n"
  "buffer_load_dwordx4 a[244:247], v19, s[36:39], s72 offen sc1\n"
  "v_mfma_f32_16x16x32_bf16 v[196:199], a[60:63], a[80:83], v[196:199]\n"
  "v_mfma_f32_16x16x32_bf16 v[200:203], a[60:63], a[84:87], v[200:203]\n"
  "s_mul_i32 s72, s81, 5\n"
  "s_nop 0\n"
  "v_mfma_f32_16x16x32_bf16 v[204:207], a[60:63], a[88:91], v[204:207]\n"
  "v_mfma_f32_16x16x32_bf16 v[208:211], a[60:63], a[92:95], v[208:211]\n"
  "v_mfma_f32_16x16x32_bf16 v[212:215], a[60:63], a[96:99], v[212:215]\n"
  "s_add_u32 s72, s72, 30720\n"
  "v_mfma_f32_16x16x32_bf16 v[216:219], a[60:63], a[100:103], v[216:219]\n"
  "v_mfma_f32_16x16x32_bf16 v[220:223], a[64:67], a[68:71], v[220:223]\n"
  "s_add_u32 s72, s72, s80\n"
  "s_nop 0\n"
  "v_mfma_f32_16x16x32_bf16 v[224:227], a[64:67], a[72:75], v[224:227]\n"
  "v_mfma_f32_16x16x32_bf16 v[228:231], a[64:67], a[76:79], v[228:231]\n"
  "v_mfma_f32_16x16x32_bf16 v[232:235], a[64:67], a[80:83], v[232:235]\n"
  "buffer_load_dwordx4 a[248:251], v18, s[36:39], s72 offen sc1\n"
  "v_mfma_f32_16x16x32_bf16 v[236:239], a[64:67], a[84:87], v[236:239]\n"
  "v_mfma_f32_16x16x32_bf16 v[240:243], a[64:67], a[88:91], v[240:243]\n"
  "buffer_load_dwordx4 a[252:255], v18, s[36:39], s72 offen offset:1024 sc1\n"
  "v_mfma_f32_16x16x32_bf16 v[244:247], a[64:67], a[92:95], v[244:247]\n"
  "v_mfma_f32_16x16x32_bf16 v[248:251], a[64:67], a[96:99], v[248:251]\n"
  "v_mfma_f32_16x16x32_bf16 v[252:255], a[64:67], a[100:103], v[252:255]\n"
  "s_waitcnt lgkmcnt(0)\n"
  "s_barrier\n"
  "s_mul_i32 s65, s46, 21504\n"
  "s_waitcnt vmcnt(32)\n"
  "s_nop 0\n"
  "v_mov_b32_dpp v2, v144 row_newbcast:0 row_mask:0xf bank_mask:0xf\n"
  "v_mov_b32_dpp v3, v145 row_newbcast:0 row_mask:0xf bank_mask:0xf\n"
  "v_mov_b32_dpp v4, v146 row_newbcast:0 row_mask:0xf bank_mask:0xf\n"
  "v_mov_b32_dpp v5, v147 row_newbcast:0 row_mask:0xf bank_mask:0xf\n"
  "v_mov_b32_dpp v6, v180 row_newbcast:0 row_mask:0xf bank_mask:0xf\n"
  "v_mov_b32_dpp v7, v181 row_newbcast:0 row_mask:0xf bank_mask:0xf\n"
  "v_mov_b32_dpp v8, v182 row_newbcast:0 row_mask:0xf bank_mask:0xf\n"
  "v_mov_b32_dpp v9, v183 row_newbcast:0 row_mask:0xf bank_mask:0xf\n"
  "v_mov_b32_dpp v10, v216 row_newbcast:0 row_mask:0xf bank_mask:0xf\n"
  "v_mov_b32_dpp v11, v217 row_newbcast:0 row_mask:0xf bank_mask:0xf\n"
  "v_mov_b32_dpp v12, v218 row_newbcast:0 row_mask:0xf bank_mask:0xf\n"
  "v_mov_b32_dpp v13, v219 row_newbcast:0 row_mask:0xf bank_mask:0xf\n"
  "v_mov_b32_dpp v14, v252 row_newbcast:0 row_mask:0xf bank_mask:0xf\n"
  "v_mov_b32_dpp v15, v253 row_newbcast:0 row_mask:0xf bank_mask:0xf\n"
  "v_mov_b32_dpp v16, v254 row_newbcast:0 row_mask:0xf bank_mask:0xf\n"
  "v_mov_b32_dpp v17, v255 row_newbcast:0 row_mask:0xf bank_mask:0xf\n"
  "v_and_b32 v20, 15, v0\n"
  "v_lshrrev_b32 v21, 4, v0\n"
  "v_bfe_u32 v22, v0, 2, 2\n"
  "v_and_b32_e64 v23, 3, v0\n"
  "s_mul_i32 s66, s46, 6912\n"
  "s_add_u32 s66, s66, 86016\n"
  "v_mul_u32_u24 v56, 48, v20\n"
  "v_add_u32 v56, s66, v56\n"
  "v_lshl_add_u32 v44, v21, 3, v56\n"
  "v_cmp_gt_u32 vcc, 2, v21\n"
  "v_mov_b32 v57, 40\n"
  "s_nop 0\n"
  "v_cndmask_b32 v57, 32, v57, vcc\n"
  "v_add_u32_e64 v45, v56, v57\n"
  "v_lshl_add_u32 v56, v21, 2, v22\n"
  "v_xor_b32 v57, 8, v56\n"
  "v_mul_u32_u24 v56, 48, v56\n"
  "v_mul_u32_u24 v57, 48, v57\n"
  "v_add_u32 v56, s66, v56\n"
  "v_add_u32_e64 v57, s66, v57\n"
  "v_lshl_add_u32 v46, v23, 3, v56\n"
  "v_add_u32_e64 v47, 32, v56\n"
  "v_lshl_add_u32 v48, v23, 3, v57\n"
  "v_add_u32 v49, 32, v57\n"
  "v_lshlrev_b32 v56, 2, v21\n"
  "v_sub_u32_e64 v56, v20, v56\n"
  "v_mov_b32 v58, 0xf149f2ca\n"
  "s_cmp_eq_u32 s47, 0\n"
  "s_nop 0\n"
  "s_cselect_b32 s66, -100, -9\n"
  "v_subrev_u32 v57, 0, v56\n"
  "v_cmp_lt_u32 vcc, 7, v57\n"
  "s_nop 1\n"
  "v_cndmask_b32 v68, 0, v58, vcc\n"
  "v_cmp_lt_i32 vcc, -9, v57\n"
  "s_nop 1\n"
  "v_cndmask_b32 v64, 0, v58, vcc\n"
  "v_cmp_lt_i32 vcc, s66, v57\n"
  "s_nop 1\n"
  "v_cndmask_b32 v72, 0, v58, vcc\n"
  "v_subrev_u32 v57, 1, v56\n"
  "v_cmp_lt_u32 vcc, 7, v57\n"
  "s_nop 1\n"
  "v_cndmask_b32 v69, 0, v58, vcc\n"
  "v_cmp_lt_i32 vcc, -9, v57\n"
  "s_nop 1\n"
  "v_cndmask_b32 v65, 0, v58, vcc\n"
  "v_cmp_lt_i32 vcc, s66, v57\n"
  "s_nop 1\n"
  "v_cndmask_b32 v73, 0, v58, vcc\n"
  "v_subrev_u32 v57, 2, v56\n"
  "v_cmp_lt_u32 vcc, 7, v57\n"
  "s_nop 1\n"
  "v_cndmask_b32 v70, 0, v58, vcc\n"
  "v_cmp_lt_i32 vcc, -9, v57\n"
  "s_nop 1\n"
  "v_cndmask_b32 v66, 0, v58, vcc\n"
  "v_cmp_lt_i32 vcc, s66, v57\n"
  "s_nop 1\n"
  "v_cndmask_b32 v74, 0, v58, vcc\n"
  "v_subrev_u32 v57, 3, v56\n"
  "v_cmp_lt_u32 vcc, 7, v57\n"
  "s_nop 1\n"
  "v_cndmask_b32 v71, 0, v58, vcc\n"
  "v_cmp_lt_i32 vcc, -9, v57\n"
  "s_nop 1\n"
  "v_cndmask_b32 v67, 0, v58, vcc\n"
  "v_cmp_lt_i32 vcc, s66, v57\n"
  "s_nop 1\n"
  "v_cndmask_b32 v75, 0, v58, vcc\n"
  "s_mov_b32 s90, 0xffff\n"
  "s_mov_b32 s91, 0\n"
  "s_nop 0\n"
  "s_mov_b32 s92, 0xffff0000\n"
  "s_mov_b32 s93, 0\n"
  "s_mov_b32 s94, 0\n"
  "s_mov_b32 s95, 0xffff\n"
  "v_mul_f32 v42, 0x3ea52b5c, v41\n"
  "v_pk_fma_f32 v[180:181], v[40:41], v[6:7], v[180:181] op_sel_hi:[0,1,1] neg_lo:[1,0,0] neg_hi:[1,0,0]\n"
  "v_pk_fma_f32 v[182:183], v[40:41], v[8:9], v[182:183] op_sel_hi:[0,1,1] neg_lo:[1,0,0] neg_hi:[1,0,0]\n"
  "v_pk_fma_f32 v[216:217], v[40:41], v[10:11], v[216:217] op_sel_hi:[0,1,1] neg_lo:[1,0,0] neg_hi:[1,0,0]\n"
  "v_pk_fma_f32 v[218:219], v[40:41], v[12:13], v[218:219] op_sel_hi:[0,1,1] neg_lo:[1,0,0] neg_hi:[1,0,0]\n"
  "v_pk_fma_f32 v[252:253], v[40:41], v[14:15], v[252:253] op_sel_hi:[0,1,1] neg_lo:[1,0,0] neg_hi:[1,0,0]\n"
  "v_pk_fma_f32 v[254:255], v[40:41], v[16:17], v[254:255] op_sel_hi:[0,1,1] neg_lo:[1,0,0] neg_hi:[1,0,0]\n"
  "v_cndmask_b32_e64 v43, v41, v42, s[90:91]\n"
  "s_mul_i32 s72, s81, 5\n"
  "s_nop 0\n"
  "s_add_u32 s72, s72, 30720\n"
  "s_add_u32 s72, s72, s80\n"
  "s_nop 0\n"
  "buffer_load_dwordx4 a[0:3], v18, s[36:39], s72 offen offset:2048 sc1\n"
  "v_pk_mul_f32 v[180:181], v[180:181], v[40:41] op_sel:[0,1] op_sel_hi:[1,1]\n"
  "v_pk_mul_f32 v[182:183], v[182:183], v[40:41] op_sel:[0,1] op_sel_hi:[1,1]\n"
  "v_pk_mul_f32 v[216:217], v[216:217], v[40:41] op_sel:[0,1] op_sel_hi:[1,1]\n"
  "v_pk_mul_f32 v[218:219], v[218:219], v[40:41] op_sel:[0,1] op_sel_hi:[1,1]\n"
  "v_pk_mul_f32 v[252:253], v[252:253], v[42:43] op_sel:[0,1] op_sel_hi:[1,1]\n"
  "v_pk_mul_f32 v[254:255], v[254:255], v[42:43] op_sel:[0,1] op_sel_hi:[1,1]\n"
  "v_cvt_pk_bf16_f32 v180, v180, v181\n"
  "v_cvt_pk_bf16_f32 v181, v182, v183\n"
  "v_cvt_pk_bf16_f32 v216, v216, v217\n"
  "v_cvt_pk_bf16_f32 v217, v218, v219\n"
  "v_cvt_pk_bf16_f32 v252, v252, v253\n"
  "v_cvt_pk_bf16_f32 v253, v254, v255\n"
  "buffer_load_dwordx4 a[4:7], v18, s[36:39], s72 offen offset:3072 sc1\n"
  "ds_write_b64 v44, v[216:217] offset:6144\n"
  "ds_write_b64 v45, v[252:253] offset:6144\n"
  "v_mov_b32 v182, v252\n"
  "v_mov_b32 v183, v253\n"
  "v_mov_b32 v254, v252\n"
  "v_mov_b32 v255, v253\n"
  "s_nop 0\n"
  "v_permlane16_swap_b32 v254, v182\n"
  "v_permlane16_swap_b32 v255, v183\n"
  "s_nop 0\n"
  "v_mul_f32 v42, 0x3ea52b5c, v25\n"
  "v_pk_fma_f32 v[112:113], v[24:25], v[2:3], v[112:113] op_sel_hi:[0,1,1] neg_lo:[1,0,0] neg_hi:[1,0,0]\n"
  "v_pk_fma_f32 v[114:115], v[24:25], v[4:5], v[114:115] op_sel_hi:[0,1,1] neg_lo:[1,0,0] neg_hi:[1,0,0]\n"
  "v_pk_fma_f32 v[148:149], v[24:25], v[6:7], v[148:149] op_sel_hi:[0,1,1] neg_lo:[1,0,0] neg_hi:[1,0,0]\n"
  "v_pk_fma_f32 v[150:151], v[24:25], v[8:9], v[150:151] op_sel_hi:[0,1,1] neg_lo:[1,0,0] neg_hi:[1,0,0]\n"
  "v_pk_fma_f32 v[184:185], v[24:25], v[10:11], v[184:185] op_sel_hi:[0,1,1] neg_lo:[1,0,0] neg_hi:[1,0,0]\n"
  "v_pk_fma_f32 v[186:187], v[24:25], v[12:13], v[186:187] op_sel_hi:[0,1,1] neg_lo:[1,0,0] neg_hi:[1,0,0]\n"
  "v_pk_fma_f32 v[220:221], v[24:25], v[14:15], v[220:221] op_sel_hi:[0,1,1] neg_lo:[1,0,0] neg_hi:[1,0,0]\n"
  "v_pk_fma_f32 v[222:223], v[24:25], v[16:17], v[222:223] op_sel_hi:[0,1,1] neg_lo:[1,0,0] neg_hi:[1,0,0]\n"
  "v_cndmask_b32_e64 v43, v25, v42, s[90:91]\n"
  "buffer_load_dwordx4 a[8:11], v19, s[36:39], s72 offen sc1\n"
  "v_pk_mul_f32 v[112:113], v[112:113], v[42:43] op_sel_hi:[1,0]\n"
  "v_pk_mul_f32 v[114:115], v[114:115], v[42:43] op_sel_hi:[1,0]\n"
  "v_pk_mul_f32 v[148:149], v[148:149], v[24:25] op_sel:[0,1] op_sel_hi:[1,1]\n"
  "v_pk_mul_f32 v[150:151], v[150:151], v[24:25] op_sel:[0,1] op_sel_hi:[1,1]\n"
  "v_pk_mul_f32 v[184:185], v[184:185], v[24:25] op_sel:[0,1] op_sel_hi:[1,1]\n"
  "v_pk_mul_f32 v[186:187], v[186:187], v[24:25] op_sel:[0,1] op_sel_hi:[1,1]\n"
  "v_pk_mul_f32 v[220:221], v[220:221], v[42:43] op_sel:[0,1] op_sel_hi:[1,1]\n"
  "v_pk_mul_f32 v[222:223], v[222:223], v[42:43] op_sel:[0,1] op_sel_hi:[1,1]\n"
  "v_cvt_pk_bf16_f32 v112, v112, v113\n"
  "v_cvt_pk_bf16_f32 v113, v114, v115\n"
  "v_cvt_pk_bf16_f32 v148, v148, v149\n"
  "v_cvt_pk_bf16_f32 v149, v150, v151\n"
  "v_cvt_pk_bf16_f32 v184, v184, v185\n"
  "v_cvt_pk_bf16_f32 v185, v186, v187\n"
  "v_cvt_pk_bf16_f32 v220, v220, v221\n"
  "v_cvt_pk_bf16_f32 v221, v222, v223\n"
  "s_mul_i32 s72, s82, 5\n"
  "s_nop 0\n"
  "s_add_u32 s72, s72, 25600\n"
  "s_add_u32 s72, s72, s80\n"
  "s_nop 0\n"
  "buffer_load_dwordx4 a[12:15], v18, s[36:39], s72 offen sc1\n"
  "ds_write_b64 v44, v[184:185] offset:0\n"
  "ds_write_b64 v45, v[220:221] offset:0\n"
  "v_cndmask_b32_e64 v114, 0, v220, s[90:91]\n"
  "v_cndmask_b32_e64 v115, 0, v221, s[90:91]\n"
  "v_mov_b32 v150, v220\n"
  "v_mov_b32 v151, v221\n"
  "v_mov_b32 v222, v220\n"
  "v_mov_b32 v223, v221\n"
  "s_nop 0\n"
  "v_permlane16_swap_b32 v222, v150\n"
  "v_permlane16_swap_b32 v223, v151\n"
  "s_nop 0\n"
  "v_mul_f32 v42, 0x3ea52b5c, v27\n"
  "v_pk_fma_f32 v[116:117], v[26:27], v[2:3], v[116:117] op_sel_hi:[0,1,1] neg_lo:[1,0,0] neg_hi:[1,0,0]\n"
  "v_pk_fma_f32 v[118:119], v[26:27], v[4:5], v[118:119] op_sel_hi:[0,1,1] neg_lo:[1,0,0] neg_hi:[1,0,0]\n"
  "v_pk_fma_f32 v[152:153], v[26:27], v[6:7], v[152:153] op_sel_hi:[0,1,1] neg_lo:[1,0,0] neg_hi:[1,0,0]\n"
  "v_pk_fma_f32 v[154:155], v[26:27], v[8:9], v[154:155] op_sel_hi:[0,1,1] neg_lo:[1,0,0] neg_hi:[1,0,0]\n"
  "v_pk_fma_f32 v[188:189], v[26:27], v[10:11], v[188:189] op_sel_hi:[0,1,1] neg_lo:[1,0,0] neg_hi:[1,0,0]\n"
  "v_pk_fma_f32 v[190:191], v[26:27], v[12:13], v[190:191] op_sel_hi:[0,1,1] neg_lo:[1,0,0] neg_hi:[1,0,0]\n"
  "v_pk_fma_f32 v[224:225], v[26:27], v[14:15], v[224:225] op_sel_hi:[0,1,1] neg_lo:[1,0,0] neg_hi:[1,0,0]\n"
  "v_pk_fma_f32 v[226:227], v[26:27], v[16:17], v[226:227] op_sel_hi:[0,1,1] neg_lo:[1,0,0] neg_hi:[1,0,0]\n"
  "v_cndmask_b32_e64 v43, v27, v42, s[90:91]\n"
  "buffer_load_dwordx4 a[16:19], v18, s[36:39], s72 offen offset:1024 sc1\n"
  "v_pk_mul_f32 v[116:117], v[116:117], v[42:43] op_sel_hi:[1,0]\n"
  "v_pk_mul_f32 v[118:119], v[118:119], v[42:43] op_sel_hi:[1,0]\n"
  "v_pk_mul_f32 v[152:153], v[152:153], v[26:27] op_sel:[0,1] op_sel_hi:[1,1]\n"
  "v_pk_mul_f32 v[154:155], v[154:155], v[26:27] op_sel:[0,1] op_sel_hi:[1,1]\n"
  "v_pk_mul_f32 v[188:189], v[188:189], v[26:27] op_sel:[0,1] op_sel_hi:[1,1]\n"
  "v_pk_mul_f32 v[190:191], v[190:191], v[26:27] op_sel:[0,1] op_sel_hi:[1,1]\n"
  "v_pk_mul_f32 v[224:225], v[224:225], v[42:43] op_sel:[0,1] op_sel_hi:[1,1]\n"
  "v_pk_mul_f32 v[226:227], v[226:227], v[42:43] op_sel:[0,1] op_sel_hi:[1,1]\n"
  "v_cvt_pk_bf16_f32 v116, v116, v117\n"
  "v_cvt_pk_bf16_f32 v117, v118, v119\n"
  "v_cvt_pk_bf16_f32 v152, v152, v153\n"
  "v_cvt_pk_bf16_f32 v153, v154, v155\n"
  "v_cvt_pk_bf16_f32 v188, v188, v189\n"
  "v_cvt_pk_bf16_f32 v189, v190, v191\n"
  "v_cvt_pk_bf16_f32 v224, v224, v225\n"
  "v_cvt_pk_bf16_f32 v225, v226, v227\n"
  "buffer_load_dwordx4 a[20:23], v18, s[36:39], s72 offen offset:2048 sc1\n"
  "ds_write_b64 v44, v[188:189] offset:768\n"
  "ds_write_b64 v45, v[224:225] offset:768\n"
  "v_cndmask_b32_e64 v118, 0, v224, s[90:91]\n"
  "v_cndmask_b32_e64 v119, 0, v225, s[90:91]\n"
  "v_mov_b32 v154, v224\n"
  "v_mov_b32 v155, v225\n"
  "v_mov_b32 v226, v224\n"
  "v_mov_b32 v227, v225\n"
  "s_nop 0\n"
  "v_permlane16_swap_b32 v226, v154\n"
  "v_permlane16_swap_b32 v227, v155\n"
  "s_nop 0\n"
  "v_mul_f32 v42, 0x3ea52b5c, v29\n"
  "v_pk_fma_f32 v[120:121], v[28:29], v[2:3], v[120:121] op_sel_hi:[0,1,1] neg_lo:[1,0,0] neg_hi:[1,0,0]\n"
  "v_pk_fma_f32 v[122:123], v[28:29], v[4:5], v[122:123] op_sel_hi:[0,1,1] neg_lo:[1,0,0] neg_hi:[1,0,0]\n"
  "v_pk_fma_f32 v[156:157], v[28:29], v[6:7], v[156:157] op_sel_hi:[0,1,1] neg_lo:[1,0,0] neg_hi:[1,0,0]\n"
  "v_pk_fma_f32 v[158:159], v[28:29], v[8:9], v[158:159] op_sel_hi:[0,1,1] neg_lo:[1,0,0] neg_hi:[1,0,0]\n"
  "v_pk_fma_f32 v[192:193], v[28:29], v[10:11], v[192:193] op_sel_hi:[0,1,1] neg_lo:[1,0,0] neg_hi:[1,0,0]\n"
  "v_pk_fma_f32 v[194:195], v[28:29], v[12:13], v[194:195] op_sel_hi:[0,1,1] neg_lo:[1,0,0] neg_hi:[1,0,0]\n"
  "v_pk_fma_f32 v[228:229], v[28:29], v[14:15], v[228:229] op_sel_hi:[0,1,1] neg_lo:[1,0,0] neg_hi:[1,0,0]\n"
  "v_pk_fma_f32 v[230:231], v[28:29], v[16:17], v[230:231] op_sel_hi:[0,1,1] neg_lo:[1,0,0] neg_hi:[1,0,0]\n"
  "v_cndmask_b32_e64 v43, v29, v42, s[90:91]\n"
  "buffer_load_dwordx4 a[24:27], v18, s[36:39], s72 offen offset:3072 sc1\n"
  "v_pk_mul_f32 v[120:121], v[120:121], v[42:43] op_sel_hi:[1,0]\n"
  "v_pk_mul_f32 v[122:123], v[122:123], v[42:43] op_sel_hi:[1,0]\n"
  "v_pk_mul_f32 v[156:157], v[156:157], v[28:29] op_sel:[0,1] op_sel_hi:[1,1]\n"
  "v_pk_mul_f32 v[158:159], v[158:159], v[28:29] op_sel:[0,1] op_sel_hi:[1,1]\n"
  "v_pk_mul_f32 v[192:193], v[192:193], v[28:29] op_sel:[0,1] op_sel_hi:[1,1]\n"
  "v_pk_mul_f32 v[194:195], v[194:195], v[28:29] op_sel:[0,1] op_sel_hi:[1,1]\n"
  "v_pk_mul_f32 v[228:229], v[228:229], v[42:43] op_sel:[0,1] op_sel_hi:[1,1]\n"
  "v_pk_mul_f32 v[230:231], v[230:231], v[42:43] op_sel:[0,1] op_sel_hi:[1,1]\n"
  "v_cvt_pk_bf16_f32 v120, v120, v121\n"
  "v_cvt_pk_bf16_f32 v121, v122, v123\n"
  "v_cvt_pk_bf16_f32 v156, v156, v157\n"
  "v_cvt_pk_bf16_f32 v157, v158, v159\n"
  "v_cvt_pk_bf16_f32 v192, v192, v193\n"
  "v_cvt_pk_bf16_f32 v193, v194, v195\n"
  "v_cvt_pk_bf16_f32 v228, v228, v229\n"
  "v_cvt_pk_bf16_f32 v229, v230, v231\n"
  "buffer_load_dwordx4 a[28:31], v19, s[36:39], s72 offen sc1\n"
  "ds_write_b64 v44, v[192:193] offset:1536\n"
  "ds_write_b64 v45, v[228:229] offset:1536\n"
  "v_cndmask_b32_e64 v122, 0, v228, s[90:91]\n"
  "v_cndmask_b32_e64 v123, 0, v229, s[90:91]\n"
  "v_mov_b32 v158, v228\n"
  "v_mov_b32 v159, v229\n"
  "v_mov_b32 v230, v228\n"
  "v_mov_b32 v231, v229\n"
  "s_nop 0\n"
  "v_permlane16_swap_b32 v230, v158\n"
  "v_permlane16_swap_b32 v231, v159\n"
  "s_nop 0\n"
  "v_mul_f32 v42, 0x3ea52b5c, v31\n"
  "v_pk_fma_f32 v[124:125], v[30:31], v[2:3], v[124:125] op_sel_hi:[0,1,1] neg_lo:[1,0,0] neg_hi:[1,0,0]\n"
  "v_pk_fma_f32 v[126:127], v[30:31], v[4:5], v[126:127] op_sel_hi:[0,1,1] neg_lo:[1,0,0] neg_hi:[1,0,0]\n"
  "v_pk_fma_f32 v[160:161], v[30:31], v[6:7], v[160:161] op_sel_hi:[0,1,1] neg_lo:[1,0,0] neg_hi:[1,0,0]\n"
  "v_pk_fma_f32 v[162:163], v[30:31], v[8:9], v[162:163] op_sel_hi:[0,1,1] neg_lo:[1,0,0] neg_hi:[1,0,0]\n"
  "v_pk_fma_f32 v[196:197], v[30:31], v[10:11], v[196:197] op_sel_hi:[0,1,1] neg_lo:[1,0,0] neg_hi:[1,0,0]\n"
  "v_pk_fma_f32 v[198:199], v[30:31], v[12:13], v[198:199] op_sel_hi:[0,1,1] neg_lo:[1,0,0] neg_hi:[1,0,0]\n"
  "v_pk_fma_f32 v[232:233], v[30:31], v[14:15], v[232:233] op_sel_hi:[0,1,1] neg_lo:[1,0,0] neg_hi:[1,0,0]\n"
  "v_pk_fma_f32 v[234:235], v[30:31], v[16:17], v[234:235] op_sel_hi:[0,1,1] neg_lo:[1,0,0] neg_hi:[1,0,0]\n"
  "v_cndmask_b32_e64 v43, v31, v42, s[90:91]\n"
  "s_mul_i32 s72, s82, 5\n"
  "s_nop 0\n"
  "s_add_u32 s72, s72, 30720\n"
  "s_add_u32 s72, s72, s80\n"
  "s_nop 0\n"
  "buffer_load_dwordx4 a[32:35], v18, s[36:39], s72 offen sc1\n"
  "v_pk_mul_f32 v[124:125], v[124:125], v[42:43] op_sel_hi:[1,0]\n"
  "v_pk_mul_f32 v[126:127], v[126:127], v[42:43] op_sel_hi:[1,0]\n"
  "v_pk_mul_f32 v[160:161], v[160:161], v[30:31] op_sel:[0,1] op_sel_hi:[1,1]\n"
  "v_pk_mul_f32 v[162:163], v[162:163], v[30:31] op_sel:[0,1] op_sel_hi:[1,1]\n"
  "v_pk_mul_f32 v[196:197], v[196:197], v[30:31] op_sel:[0,1] op_sel_hi:[1,1]\n"
  "v_pk_mul_f32 v[198:199], v[198:199], v[30:31] op_sel:[0,1] op_sel_hi:[1,1]\n"
  "v_pk_mul_f32 v[232:233], v[232:233], v[42:43] op_sel:[0,1] op_sel_hi:[1,1]\n"
  "v_pk_mul_f32 v[234:235], v[234:235], v[42:43] op_sel:[0,1] op_sel_hi:[1,1]\n"
  "v_cvt_pk_bf16_f32 v124, v124, v125\n"
  "v_cvt_pk_bf16_f32 v125, v126, v127\n"
  "v_cvt_pk_bf16_f32 v160, v160, v161\n"
  "v_cvt_pk_bf16_f32 v161, v162, v163\n"
  "v_cvt_pk_bf16_f32 v196, v196, v197\n"
  "v_cvt_pk_bf16_f32 v197, v198, v199\n"
  "v_cvt_pk_bf16_f32 v232, v232, v233\n"
  "v_cvt_pk_bf16_f32 v233, v234, v235\n"
  "buffer_load_dwordx4 a[36:39], v18, s[36:39], s72 offen offset:1024 sc1\n"
  "ds_write_b64 v44, v[196:197] offset:2304\n"
  "ds_write_b64 v45, v[232:233] offset:2304\n"
  "v_cndmask_b32_e64 v126, 0, v232, s[90:91]\n"
  "v_cndmask_b32_e64 v127, 0, v233, s[90:91]\n"
  "v_mov_b32 v162, v232\n"
  "v_mov_b32 v163, v233\n"
  "v_mov_b32 v234, v232\n"
  "v_mov_b32 v235, v233\n"
  "s_nop 0\n"
  "v_permlane16_swap_b32 v234, v162\n"
  "v_permlane16_swap_b32 v235, v163\n"
  "s_nop 0\n"
  "v_mul_f32 v42, 0x3ea52b5c, v33\n"
  "v_pk_fma_f32 v[128:129], v[32:33], v[2:3], v[128:129] op_sel_hi:[0,1,1] neg_lo:[1,0,0] neg_hi:[1,0,0]\n"
  "v_pk_fma_f32 v[130:131], v[32:33], v[4:5], v[130:131] op_sel_hi:[0,1,1] neg_lo:[1,0,0] neg_hi:[1,0,0]\n"
  "v_pk_fma_f32 v[164:165], v[32:33], v[6:7], v[164:165] op_sel_hi:[0,1,1] neg_lo:[1,0,0] neg_hi:[1,0,0]\n"
  "v_pk_fma_f32 v[166:167], v[32:33], v[8:9], v[166:167] op_sel_hi:[0,1,1] neg_lo:[1,0,0] neg_hi:[1,0,0]\n"
  "v_pk_fma_f32 v[200:201], v[32:33], v[10:11], v[200:201] op_sel_hi:[0,1,1] neg_lo:[1,0,0] neg_hi:[1,0,0]\n"
  "v_pk_fma_f32 v[202:203], v[32:33], v[12:13], v[202:203] op_sel_hi:[0,1,1] neg_lo:[1,0,0] neg_hi:[1,0,0]\n"
  "v_pk_fma_f32 v[236:237], v[32:33], v[14:15], v[236:237] op_sel_hi:[0,1,1] neg_lo:[1,0,0] neg_hi:[1,0,0]\n"
  "v_pk_fma_f32 v[238:239], v[32:33], v[16:17], v[238:239] op_sel_hi:[0,1,1] neg_lo:[1,0,0] neg_hi:[1,0,0]\n"
  "v_cndmask_b32_e64 v43, v33, v42, s[90:91]\n"
  "buffer_load_dwordx4 a[40:43], v18, s[36:39], s72 offen offset:2048 sc1\n"
  "v_pk_mul_f32 v[128:129], v[128:129], v[42:43] op_sel_hi:[1,0]\n"
  "v_pk_mul_f32 v[130:131], v[130:131], v[42:43] op_sel_hi:[1,0]\n"
  "v_pk_mul_f32 v[164:165], v[164:165], v[32:33] op_sel:[0,1] op_sel_hi:[1,1]\n"
  "v_pk_mul_f32 v[166:167], v[166:167], v[32:33] op_sel:[0,1] op_sel_hi:[1,1]\n"
  "v_pk_mul_f32 v[200:201], v[200:201], v[32:33] op_sel:[0,1] op_sel_hi:[1,1]\n"
  "v_pk_mul_f32 v[202:203], v[202:203], v[32:33] op_sel:[0,1] op_sel_hi:[1,1]\n"
  "v_pk_mul_f32 v[236:237], v[236:237], v[42:43] op_sel:[0,1] op_sel_hi:[1,1]\n"
  "v_pk_mul_f32 v[238:239], v[238:239], v[42:43] op_sel:[0,1] op_sel_hi:[1,1]\n"
  "v_cvt_pk_bf16_f32 v128, v128, v129\n"
  "v_cvt_pk_bf16_f32 v129, v130, v131\n"
  "v_cvt_pk_bf16_f32 v164, v164, v165\n"
  "v_cvt_pk_bf16_f32 v165, v166, v167\n"
  "v_cvt_pk_bf16_f32 v200, v200, v201\n"
  "v_cvt_pk_bf16_f32 v201, v202, v203\n"
  "v_cvt_pk_bf16_f32 v236, v236, v237\n"
  "v_cvt_pk_bf16_f32 v237, v238, v239\n"
  "buffer_load_dwordx4 a[44:47], v18, s[36:39], s72 offen offset:3072 sc1\n"
  "ds_write_b64 v44, v[200:201] offset:3072\n"
  "ds_write_b64 v45, v[236:237] offset:3072\n"
  "v_cndmask_b32_e64 v130, 0, v236, s[90:91]\n"
  "v_cndmask_b32_e64 v131, 0, v237, s[90:91]\n"
  "v_mov_b32 v166, v236\n"
  "v_mov_b32 v167, v237\n"
  "v_mov_b32 v238, v236\n"
  "v_mov_b32 v239, v237\n"
  "s_nop 0\n"
  "v_permlane16_swap_b32 v238, v166\n"
  "v_permlane16_swap_b32 v239, v167\n"
  "s_nop 0\n"
  "v_mul_f32 v42, 0x3ea52b5c, v35\n"
  "v_pk_fma_f32 v[132:133], v[34:35], v[2:3], v[132:133] op_sel_hi:[0,1,1] neg_lo:[1,0,0] neg_hi:[1,0,0]\n"
  "v_pk_fma_f32 v[134:135], v[34:35], v[4:5], v[134:135] op_sel_hi:[0,1,1] neg_lo:[1,0,0] neg_hi:[1,0,0]\n"
  "v_pk_fma_f32 v[168:169], v[34:35], v[6:7], v[168:169] op_sel_hi:[0,1,1] neg_lo:[1,0,0] neg_hi:[1,0,0]\n"
  "v_pk_fma_f32 v[170:171], v[34:35], v[8:9], v[170:171] op_sel_hi:[0,1,1] neg_lo:[1,0,0] neg_hi:[1,0,0]\n"
  "v_pk_fma_f32 v[204:205], v[34:35], v[10:11], v[204:205] op_sel_hi:[0,1,1] neg_lo:[1,0,0] neg_hi:[1,0,0]\n"
  "v_pk_fma_f32 v[206:207], v[34:35], v[12:13], v[206:207] op_sel_hi:[0,1,1] neg_lo:[1,0,0] neg_hi:[1,0,0]\n"
  "v_pk_fma_f32 v[240:241], v[34:35], v[14:15], v[240:241] op_sel_hi:[0,1,1] neg_lo:[1,0,0] neg_hi:[1,0,0]\n"
  "v_pk_fma_f32 v[242:243], v[34:35], v[16:17], v[242:243] op_sel_hi:[0,1,1] neg_lo:[1,0,0] neg_hi:[1,0,0]\n"
  "v_cndmask_b32_e64 v43, v35, v42, s[90:91]\n"
  "buffer_load_dwordx4 a[48:51], v19, s[36:39], s72 offen sc1\n"
  "v_pk_mul_f32 v[132:133], v[132:133], v[42:43] op_sel_hi:[1,0]\n"
  "v_pk_mul_f32 v[134:135], v[134:135], v[42:43] op_sel_hi:[1,0]\n"
  "v_pk_mul_f32 v[168:169], v[168:169], v[34:35] op_sel:[0,1] op_sel_hi:[1,1]\n"
  "v_pk_mul_f32 v[170:171], v[170:171], v[34:35] op_sel:[0,1] op_sel_hi:[1,1]\n"
  "v_pk_mul_f32 v[204:205], v[204:205], v[34:35] op_sel:[0,1] op_sel_hi:[1,1]\n"
  "v_pk_mul_f32 v[206:207], v[206:207], v[34:35] op_sel:[0,1] op_sel_hi:[1,1]\n"
  "v_pk_mul_f32 v[240:241], v[240:241], v[42:43] op_sel:[0,1] op_sel_hi:[1,1]\n"
  "v_pk_mul_f32 v[242:243], v[242:243], v[42:43] op_sel:[0,1] op_sel_hi:[1,1]\n"
  "v_cvt_pk_bf16_f32 v132, v132, v133\n"
  "v_cvt_pk_bf16_f32 v133, v134, v135\n"
  "v_cvt_pk_bf16_f32 v168, v168, v169\n"
  "v_cvt_pk_bf16_f32 v169, v170, v171\n"
  "v_cvt_pk_bf16_f32 v204, v204, v205\n"
  "v_cvt_pk_bf16_f32 v205, v206, v207\n"
  "v_cvt_pk_bf16_f32 v240, v240, v241\n"
  "v_cvt_pk_bf16_f32 v241, v242, v243\n"
  "s_mul_i32 s72, s83, 5\n"
  "s_nop 0\n"
  "s_add_u32 s72, s72, 25600\n"
  "s_add_u32 s72, s72, s80\n"
  "s_nop 0\n"
  "buffer_load_dwordx4 a[52:55], v18, s[36:39], s72 offen sc1\n"
  "ds_write_b64 v44, v[204:205] offset:3840\n"
  "ds_write_b64 v45, v[240:241] offset:3840\n"
  "v_cndmask_b32_e64 v134, 0, v240, s[90:91]\n"
  "v_cndmask_b32_e64 v135, 0, v241, s[90:91]\n"
  "v_mov_b32 v170, v240\n"
  "v_mov_b32 v171, v241\n"
  "v_mov_b32 v242, v240\n"
  "v_mov_b32 v243, v241\n"
  "s_nop 0\n"
  "v_permlane16_swap_b32 v242, v170\n"
  "v_permlane16_swap_b32 v243, v171\n"
  "s_nop 0\n"
  "v_mul_f32 v42, 0x3ea52b5c, v37\n"
  "v_pk_fma_f32 v[136:137], v[36:37], v[2:3], v[136:137] op_sel_hi:[0,1,1] neg_lo:[1,0,0] neg_hi:[1,0,0]\n"
  "v_pk_fma_f32 v[138:139], v[36:37], v[4:5], v[138:139] op_sel_hi:[0,1,1] neg_lo:[1,0,0] neg_hi:[1,0,0]\n"
  "v_pk_fma_f32 v[172:173], v[36:37], v[6:7], v[172:173] op_sel_hi:[0,1,1] neg_lo:[1,0,0] neg_hi:[1,0,0]\n"
  "v_pk_fma_f32 v[174:175], v[36:37], v[8:9], v[174:175] op_sel_hi:[0,1,1] neg_lo:[1,0,0] neg_hi:[1,0,0]\n"
  "v_pk_fma_f32 v[208:209], v[36:37], v[10:11], v[208:209] op_sel_hi:[0,1,1] neg_lo:[1,0,0] neg_hi:[1,0,0]\n"
  "v_pk_fma_f32 v[210:211], v[36:37], v[12:13], v[210:211] op_sel_hi:[0,1,1] neg_lo:[1,0,0] neg_hi:[1,0,0]\n"
  "v_pk_fma_f32 v[244:245], v[36:37], v[14:15], v[244:245] op_sel_hi:[0,1,1] neg_lo:[1,0,0] neg_hi:[1,0,0]\n"
  "v_pk_fma_f32 v[246:247], v[36:37], v[16:17], v[246:247] op_sel_hi:[0,1,1] neg_lo:[1,0,0] neg_hi:[1,0,0]\n"
  "v_cndmask_b32_e64 v43, v37, v42, s[90:91]\n"
  "buffer_load_dwordx4 a[56:59], v18, s[36:39], s72 offen offset:1024 sc1\n"
  "v_pk_mul_f32 v[136:137], v[136:137], v[42:43] op_sel_hi:[1,0]\n"
  "v_pk_mul_f32 v[138:139], v[138:139], v[42:43] op_sel_hi:[1,0]\n"
  "v_pk_mul_f32 v[172:173], v[172:173], v[36:37] op_sel:[0,1] op_sel_hi:[1,1]\n"
  "v_pk_mul_f32 v[174:175], v[174:175], v[36:37] op_sel:[0,1] op_sel_hi:[1,1]\n"
  "v_pk_mul_f32 v[208:209], v[208:209], v[36:37] op_sel:[0,1] op_sel_hi:[1,1]\n"
  "v_pk_mul_f32 v[210:211], v[210:211], v[36:37] op_sel:[0,1] op_sel_hi:[1,1]\n"
  "v_pk_mul_f32 v[244:245], v[244:245], v[42:43] op_sel:[0,1] op_sel_hi:[1,1]\n"
  "v_pk_mul_f32 v[246:247], v[246:247], v[42:43] op_sel:[0,1] op_sel_hi:[1,1]\n"
  "v_cvt_pk_bf16_f32 v136, v136, v137\n"
  "v_cvt_pk_bf16_f32 v137, v138, v139\n"
  "v_cvt_pk_bf16_f32 v172, v172, v173\n"
  "v_cvt_pk_bf16_f32 v173, v174, v175\n"
  "v_cvt_pk_bf16_f32 v208, v208, v209\n"
  "v_cvt_pk_bf16_f32 v209, v210, v211\n"
  "v_cvt_pk_bf16_f32 v244, v244, v245\n"
  "v_cvt_pk_bf16_f32 v245, v246, v247\n"
  "buffer_load_dwordx4 a[60:63], v18, s[36:39], s72 offen offset:2048 sc1\n"
  "ds_write_b64 v44, v[208:209] offset:4608\n"
  "ds_write_b64 v45, v[244:245] offset:4608\n"
  "v_cndmask_b32_e64 v138, 0, v244, s[90:91]\n"
  "v_cndmask_b32_e64 v139, 0, v245, s[90:91]\n"
  "v_mov_b32 v174, v244\n"
  "v_mov_b32 v175, v245\n"
  "v_mov_b32 v246, v244\n"
  "v_mov_b32 v247, v245\n"
  "s_nop 0\n"
  "v_permlane16_swap_b32 v246, v174\n"
  "v_permlane16_swap_b32 v247, v175\n"
  "s_nop 0\n"
  "v_mul_f32 v42, 0x3ea52b5c, v39\n"
  "v_pk_fma_f32 v[140:141], v[38:39], v[2:3], v[140:141] op_sel_hi:[0,1,1] neg_lo:[1,0,0] neg_hi:[1,0,0]\n"
  "v_pk_fma_f32 v[142:143], v[38:39], v[4:5], v[142:143] op_sel_hi:[0,1,1] neg_lo:[1,0,0] neg_hi:[1,0,0]\n"
  "v_pk_fma_f32 v[176:177], v[38:39], v[6:7], v[176:177] op_sel_hi:[0,1,1] neg_lo:[1,0,0] neg_hi:[1,0,0]\n"
  "v_pk_fma_f32 v[178:179], v[38:39], v[8:9], v[178:179] op_sel_hi:[0,1,1] neg_lo:[1,0,0] neg_hi:[1,0,0]\n"
  "v_pk_fma_f32 v[212:213], v[38:39], v[10:11], v[212:213] op_sel_hi:[0,1,1] neg_lo:[1,0,0] neg_hi:[1,0,0]\n"
  "v_pk_fma_f32 v[214:215], v[38:39], v[12:13], v[214:215] op_sel_hi:[0,1,1] neg_lo:[1,0,0] neg_hi:[1,0,0]\n"
  "v_pk_fma_f32 v[248:249], v[38:39], v[14:15], v[248:249] op_sel_hi:[0,1,1] neg_lo:[1,0,0] neg_hi:[1,0,0]\n"
  "v_pk_fma_f32 v[250:251], v[38:39], v[16:17], v[250:251] op_sel_hi:[0,1,1] neg_lo:[1,0,0] neg_hi:[1,0,0]\n"
  "v_cndmask_b32_e64 v43, v39, v42, s[90:91]\n"
  "buffer_load_dwordx4 a[64:67], v18, s[36:39], s72 offen offset:3072 sc1\n"
  "v_pk_mul_f32 v[140:141], v[140:141], v[42:43] op_sel_hi:[1,0]\n"
  "v_pk_mul_f32 v[142:143], v[142:143], v[42:43] op_sel_hi:[1,0]\n"
  "v_pk_mul_f32 v[176:177], v[176:177], v[38:39] op_sel:[0,1] op_sel_hi:[1,1]\n"
  "v_pk_mul_f32 v[178:179], v[178:179], v[38:39] op_sel:[0,1] op_sel_hi:[1,1]\n"
  "v_pk_mul_f32 v[212:213], v[212:213], v[38:39] op_sel:[0,1] op_sel_hi:[1,1]\n"
  "v_pk_mul_f32 v[214:215], v[214:215], v[38:39] op_sel:[0,1] op_sel_hi:[1,1]\n"
  "v_pk_mul_f32 v[248:249], v[248:249], v[42:43] op_sel:[0,1] op_sel_hi:[1,1]\n"
  "v_pk_mul_f32 v[250:251], v[250:251], v[42:43] op_sel:[0,1] op_sel_hi:[1,1]\n"
  "v_cvt_pk_bf16_f32 v140, v140, v141\n"
  "v_cvt_pk_bf16_f32 v141, v142, v143\n"
  "v_cvt_pk_bf16_f32 v176, v176, v177\n"
  "v_cvt_pk_bf16_f32 v177, v178, v179\n"
  "v_cvt_pk_bf16_f32 v212, v212, v213\n"
  "v_cvt_pk_bf16_f32 v213, v214, v215\n"
  "v_cvt_pk_bf16_f32 v248, v248, v249\n"
  "v_cvt_pk_bf16_f32 v249, v250, v251\n"
  "buffer_load_dwordx4 a[68:71], v19, s[36:39], s72 offen sc1\n"
  "ds_write_b64 v44, v[212:213] offset:5376\n"
  "ds_write_b64 v45, v[248:249] offset:5376\n"
  "v_cndmask_b32_e64 v142, 0, v248, s[90:91]\n"
  "v_cndmask_b32_e64 v143, 0, v249, s[90:91]\n"
  "v_mov_b32 v178, v248\n"
  "v_mov_b32 v179, v249\n"
  "v_mov_b32 v250, v248\n"
  "v_mov_b32 v251, v249\n"
  "s_nop 0\n"
  "v_permlane16_swap_b32 v250, v178\n"
  "v_permlane16_swap_b32 v251, v179\n"
  "s_nop 0\n"
  "v_mov_b32_dpp v144, v180 row_ror:8 row_mask:0xf bank_mask:0xf\n"
  "v_mov_b32_dpp v145, v181 row_ror:8 row_mask:0xf bank_mask:0xf\n"
  "v_mov_b32_dpp v146, v182 row_ror:8 row_mask:0xf bank_mask:0xf\n"
  "v_mov_b32_dpp v147, v183 row_ror:8 row_mask:0xf bank_mask:0xf\n"
  "s_waitcnt lgkmcnt(0)\n"
  "s_nop 0\n"
  "ds_read_b64_tr_b16 v[184:185], v46 offset:6144\n"
  "ds_read_b64_tr_b16 v[194:195], v47 offset:6144\n"
  "ds_read_b64_tr_b16 v[186:187], v46 offset:0\n"
  "ds_read_b64_tr_b16 v[196:197], v47 offset:0\n"
  "ds_read_b64_tr_b16 v[188:189], v46 offset:1536\n"
  "ds_read_b64_tr_b16 v[198:199], v47 offset:1536\n"
  "ds_read_b64_tr_b16 v[190:191], v46 offset:3072\n"
  "ds_read_b64_tr_b16 v[200:201], v47 offset:3072\n"
  "ds_read_b64_tr_b16 v[192:193], v46 offset:4608\n"
  "ds_read_b64_tr_b16 v[202:203], v47 offset:4608\n"
  "ds_read_b64_tr_b16 v[204:205], v48 offset:6144\n"
  "ds_read_b64_tr_b16 v[214:215], v49 offset:6144\n"
  "ds_read_b64_tr_b16 v[206:207], v46 offset:768\n"
  "ds_read_b64_tr_b16 v[216:217], v47 offset:768\n"
  "ds_read_b64_tr_b16 v[208:209], v46 offset:2304\n"
  "ds_read_b64_tr_b16 v[218:219], v47 offset:2304\n"
  "ds_read_b64_tr_b16 v[210:211], v46 offset:3840\n"
  "ds_read_b64_tr_b16 v[220:221], v47 offset:3840\n"
  "ds_read_b64_tr_b16 v[212:213], v46 offset:5376\n"
  "ds_read_b64_tr_b16 v[222:223], v47 offset:5376\n"
  "v_and_b32 v56, 1, v21\n"
  "v_lshrrev_b32 v57, 1, v21\n"
  "v_lshlrev_b32_e64 v56, 14, v56\n"
  "v_lshl_add_u32 v56, v57, 8, v56\n"
  "v_lshl_add_u32 v56, v20, 4, v56\n"
  "v_lshlrev_b32_e64 v57, 12, v21\n"
  "v_lshl_add_u32 v57, v20, 3, v57\n"
  "s_lshl_b32 s66, s47, 7\n"
  "s_lshl_b32 s67, s48, 1\n"
  "s_add_u32 s66, s66, s67\n"
  "s_lshr_b32 s67, s46, 1\n"
  "s_add_u32 s66, s66, s67\n"
  "s_lshl_b32 s66, s66, 10\n"
  "s_and_b32 s67, s46, 1\n"
  "s_lshl_b32 s67, s67, 9\n"
  "s_add_u32 s66, s66, s67\n"
  "s_nop 0\n"
  "s_add_u32 s70, s66, 11534336\n"
  "s_lshl_b32 s66, s47, 8\n"
  "s_lshl_b32 s67, s48, 2\n"
  "s_add_u32 s66, s66, s67\n"
  "s_add_u32 s66, s66, s46\n"
  "s_lshl_b32 s66, s66, 7\n"
  "s_nop 0\n"
  "s_add_u32 s71, s66, 14680064\n"
  "v_mfma_f32_16x16x32_bf16 v[76:79], v[180:183], v[112:115], v[72:75]\n"
  "v_mfma_f32_16x16x32_bf16 v[80:83], v[148:151], v[112:115], v[68:71]\n"
  "v_mfma_f32_16x16x32_bf16 v[92:95], v[144:147], v[116:119], v[72:75]\n"
  "v_mfma_f32_16x16x32_bf16 v[96:99], v[152:155], v[116:119], v[68:71]\n"
  "s_mul_i32 s72, s83, 5\n"
  "s_nop 0\n"
  "s_add_u32 s72, s72, 30720\n"
  "s_add_u32 s72, s72, s80\n"
  "s_nop 0\n"
  "buffer_load_dwordx4 a[72:75], v18, s[36:39], s72 offen sc1\n"
  "buffer_load_dwordx4 a[76:79], v18, s[36:39], s72 offen offset:1024 sc1\n"
  "s_waitcnt lgkmcnt(0)\n"
  "s_nop 0\n"
  "v_max3_f32 v88, v76, v77, v78\n"
  "v_max3_f32 v89, v79, v80, v81\n"
  "v_max3_f32 v104, v92, v93, v94\n"
  "v_max3_f32 v105, v95, v96, v97\n"
  "v_max3_f32 v88, v88, v82, v83\n"
  "v_max3_f32 v104, v104, v98, v99\n"
  "v_max_f32 v88, v88, v89\n"
  "v_max_f32 v104, v104, v105\n"
  "v_mov_b32 v89, v88\n"
  "v_mov_b32 v105, v104\n"
  "s_nop 0\n"
  "v_permlane16_swap_b32 v88, v89\n"
  "v_permlane16_swap_b32 v104, v105\n"
  "v_max_f32 v88, v88, v89\n"
  "v_max_f32 v104, v104, v105\n"
  "v_mov_b32 v89, v88\n"
  "v_mov_b32 v105, v104\n"
  "s_nop 0\n"
  "v_permlane32_swap_b32 v88, v89\n"
  "v_permlane32_swap_b32 v104, v105\n"
  "v_max_f32 v88, v88, v89\n"
  "v_max_f32 v104, v104, v105\n"
  "v_pk_add_f32 v[76:77], v[76:77], v[88:89] op_sel_hi:[1,0] neg_lo:[0,1] neg_hi:[0,1]\n"
  "v_pk_add_f32 v[78:79], v[78:79], v[88:89] op_sel_hi:[1,0] neg_lo:[0,1] neg_hi:[0,1]\n"
  "v_pk_add_f32 v[80:81], v[80:81], v[88:89] op_sel_hi:[1,0] neg_lo:[0,1] neg_hi:[0,1]\n"
  "v_pk_add_f32 v[82:83], v[82:83], v[88:89] op_sel_hi:[1,0] neg_lo:[0,1] neg_hi:[0,1]\n"
  "v_pk_add_f32 v[92:93], v[92:93], v[104:105] op_sel_hi:[1,0] neg_lo:[0,1] neg_hi:[0,1]\n"
  "v_pk_add_f32 v[94:95], v[94:95], v[104:105] op_sel_hi:[1,0] neg_lo:[0,1] neg_hi:[0,1]\n"
  "v_pk_add_f32 v[96:97], v[96:97], v[104:105] op_sel_hi:[1,0] neg_lo:[0,1] neg_hi:[0,1]\n"
  "v_pk_add_f32 v[98:99], v[98:99], v[104:105] op_sel_hi:[1,0] neg_lo:[0,1] neg_hi:[0,1]\n"
  "buffer_load_dwordx4 a[80:83], v18, s[36:39], s72 offen offset:2048 sc1\n"
  "v_exp_f32 v76, v76\n"
  "v_exp_f32 v77, v77\n"
  "v_exp_f32 v78, v78\n"
  "v_exp_f32 v79, v79\n"
  "v_exp_f32 v80, v80\n"
  "v_exp_f32 v81, v81\n"
  "v_exp_f32 v82, v82\n"
  "v_exp_f32 v83, v83\n"
  "v_exp_f32 v92, v92\n"
  "v_exp_f32 v93, v93\n"
  "v_exp_f32 v94, v94\n"
  "v_exp_f32 v95, v95\n"
  "v_exp_f32 v96, v96\n"
  "v_exp_f32 v97, v97\n"
  "v_exp_f32 v98, v98\n"
  "v_exp_f32 v99, v99\n"
  "buffer_load_dwordx4 a[84:87], v18, s[36:39], s72 offen offset:3072 sc1\n"
  "v_cvt_pk_bf16_f32 v84, v76, v77\n"
  "v_cvt_pk_bf16_f32 v85, v78, v79\n"
  "v_cvt_pk_bf16_f32 v86, v80, v81\n"
  "v_cvt_pk_bf16_f32 v87, v82, v83\n"
  "v_cvt_pk_bf16_f32 v100, v92, v93\n"
  "v_cvt_pk_bf16_f32 v101, v94, v95\n"
  "v_cvt_pk_bf16_f32 v102, v96, v97\n"
  "v_cvt_pk_bf16_f32 v103, v98, v99\n"
  "v_mfma_f32_16x16x32_bf16 v[240:243], v[52:55], v[84:87], 0\n"
  "v_mfma_f32_16x16x32_bf16 v[224:227], v[184:187], v[84:87], 0\n"
  "v_mfma_f32_16x16x32_bf16 v[232:235], v[194:197], v[84:87], 0\n"
  "v_mfma_f32_16x16x32_bf16 v[244:247], v[52:55], v[100:103], 0\n"
  "v_mfma_f32_16x16x32_bf16 v[228:231], v[204:207], v[100:103], 0\n"
  "v_mfma_f32_16x16x32_bf16 v[236:239], v[214:217], v[100:103], 0\n"
  "buffer_load_dwordx4 a[88:91], v19, s[36:39], s72 offen sc1\n"
  "s_mul_i32 s72, s84, 5\n"
  "s_nop 0\n"
  "s_add_u32 s72, s72, 25600\n"
  "s_add_u32 s72, s72, s80\n"
  "s_nop 0\n"
  "buffer_load_dwordx4 a[92:95], v18, s[36:39], s72 offen sc1\n"
  "v_rcp_f32 v240, v240\n"
  "v_rcp_f32 v244, v244\n"
  "v_pk_mul_f32 v[224:225], v[224:225], v[240:241] op_sel_hi:[1,0]\n"
  "v_pk_mul_f32 v[226:227], v[226:227], v[240:241] op_sel_hi:[1,0]\n"
  "v_pk_mul_f32 v[232:233], v[232:233], v[240:241] op_sel_hi:[1,0]\n"
  "v_pk_mul_f32 v[234:235], v[234:235], v[240:241] op_sel_hi:[1,0]\n"
  "v_pk_mul_f32 v[228:229], v[228:229], v[244:245] op_sel_hi:[1,0]\n"
  "v_pk_mul_f32 v[230:231], v[230:231], v[244:245] op_sel_hi:[1,0]\n"
  "v_pk_mul_f32 v[236:237], v[236:237], v[244:245] op_sel_hi:[1,0]\n"
  "v_pk_mul_f32 v[238:239], v[238:239], v[244:245] op_sel_hi:[1,0]\n"
  "v_cvt_pk_bf16_f32 v250, v224, v225\n"
  "v_cvt_pk_bf16_f32 v251, v226, v227\n"
  "v_cvt_pk_bf16_f32 v252, v228, v229\n"
  "v_cvt_pk_bf16_f32 v253, v230, v231\n"
  "v_cvt_pk_bf16_f32 v248, v232, v233\n"
  "v_cvt_pk_bf16_f32 v249, v234, v235\n"
  "v_cvt_pk_bf16_f32 v236, v236, v237\n"
  "v_cvt_pk_bf16_f32 v237, v238, v239\n"
  "v_cndmask_b32_e64 v248, v248, v236, s[92:93]\n"
  "v_cndmask_b32_e64 v249, v249, v237, s[92:93]\n"
  "v_permlane16_swap_b32 v250, v252\n"
  "v_permlane16_swap_b32 v251, v253\n"
  "s_add_u32 s66, s70, 0\n"
  "s_nop 0\n"
  "buffer_store_dwordx4 v[250:253], v56, s[36:39], s66 offen sc1\n"
  "v_mfma_f32_16x16x32_bf16 v[76:79], v[148:151], v[120:123], v[64:67]\n"
  "v_mfma_f32_16x16x32_bf16 v[80:83], v[156:159], v[120:123], v[68:71]\n"
  "v_mfma_f32_16x16x32_bf16 v[92:95], v[152:155], v[124:127], v[64:67]\n"
  "v_mfma_f32_16x16x32_bf16 v[96:99], v[160:163], v[124:127], v[68:71]\n"
  "buffer_load_dwordx4 a[96:99], v18, s[36:39], s72 offen offset:1024 sc1\n"
  "buffer_load_dwordx4 a[100:103], v18, s[36:39], s72 offen offset:2048 sc1\n"
  "s_nop 2\n"
  "s_nop 0\n"
  "v_max3_f32 v88, v76, v77, v78\n"
  "v_max3_f32 v89, v79, v80, v81\n"
  "v_max3_f32 v104, v92, v93, v94\n"
  "v_max3_f32 v105, v95, v96, v97\n"
  "v_max3_f32 v88, v88, v82, v83\n"
  "v_max3_f32 v104, v104, v98, v99\n"
  "v_max_f32 v88, v88, v89\n"
  "v_max_f32 v104, v104, v105\n"
  "v_mov_b32 v89, v88\n"
  "v_mov_b32 v105, v104\n"
  "s_nop 0\n"
  "v_permlane16_swap_b32 v88, v89\n"
  "v_permlane16_swap_b32 v104, v105\n"
  "v_max_f32 v88, v88, v89\n"
  "v_max_f32 v104, v104, v105\n"
  "v_mov_b32 v89, v88\n"
  "v_mov_b32 v105, v104\n"
  "s_nop 0\n"
  "v_permlane32_swap_b32 v88, v89\n"
  "v_permlane32_swap_b32 v104, v105\n"
  "v_max_f32 v88, v88, v89\n"
  "v_max_f32 v104, v104, v105\n"
  "v_pk_add_f32 v[76:77], v[76:77], v[88:89] op_sel_hi:[1,0] neg_lo:[0,1] neg_hi:[0,1]\n"
  "v_pk_add_f32 v[78:79], v[78:79], v[88:89] op_sel_hi:[1,0] neg_lo:[0,1] neg_hi:[0,1]\n"
  "v_pk_add_f32 v[80:81], v[80:81], v[88:89] op_sel_hi:[1,0] neg_lo:[0,1] neg_hi:[0,1]\n"
  "v_pk_add_f32 v[82:83], v[82:83], v[88:89] op_sel_hi:[1,0] neg_lo:[0,1] neg_hi:[0,1]\n"
  "v_pk_add_f32 v[92:93], v[92:93], v[104:105] op_sel_hi:[1,0] neg_lo:[0,1] neg_hi:[0,1]\n"
  "v_pk_add_f32 v[94:95], v[94:95], v[104:105] op_sel_hi:[1,0] neg_lo:[0,1] neg_hi:[0,1]\n"
  "v_pk_add_f32 v[96:97], v[96:97], v[104:105] op_sel_hi:[1,0] neg_lo:[0,1] neg_hi:[0,1]\n"
  "v_pk_add_f32 v[98:99], v[98:99], v[104:105] op_sel_hi:[1,0] neg_lo:[0,1] neg_hi:[0,1]\n"
  "buffer_load_dwordx4 a[104:107], v18, s[36:39], s72 offen offset:3072 sc1\n"
  "v_exp_f32 v76, v76\n"
  "v_exp_f32 v77, v77\n"
  "v_exp_f32 v78, v78\n"
  "v_exp_f32 v79, v79\n"
  "v_exp_f32 v80, v80\n"
  "v_exp_f32 v81, v81\n"
  "v_exp_f32 v82, v82\n"
  "v_exp_f32 v83, v83\n"
  "v_exp_f32 v92, v92\n"
  "v_exp_f32 v93, v93\n"
  "v_exp_f32 v94, v94\n"
  "v_exp_f32 v95, v95\n"
  "v_exp_f32 v96, v96\n"
  "v_exp_f32 v97, v97\n"
  "v_exp_f32 v98, v98\n"
  "v_exp_f32 v99, v99\n"
  "buffer_load_dwordx4 a[108:111], v19, s[36:39], s72 offen sc1\n"
  "v_cvt_pk_bf16_f32 v84, v76, v77\n"
  "v_cvt_pk_bf16_f32 v85, v78, v79\n"
  "v_cvt_pk_bf16_f32 v86, v80, v81\n"
  "v_cvt_pk_bf16_f32 v87, v82, v83\n"
  "v_cvt_pk_bf16_f32 v100, v92, v93\n"
  "v_cvt_pk_bf16_f32 v101, v94, v95\n"
  "v_cvt_pk_bf16_f32 v102, v96, v97\n"
  "v_cvt_pk_bf16_f32 v103, v98, v99\n"
  "v_mfma_f32_16x16x32_bf16 v[240:243], v[52:55], v[84:87], 0\n"
  "v_mfma_f32_16x16x32_bf16 v[224:227], v[186:189], v[84:87], 0\n"
  "v_mfma_f32_16x16x32_bf16 v[232:235], v[196:199], v[84:87], 0\n"
  "v_mfma_f32_16x16x32_bf16 v[244:247], v[52:55], v[100:103], 0\n"
  "v_mfma_f32_16x16x32_bf16 v[228:231], v[206:209], v[100:103], 0\n"
  "v_mfma_f32_16x16x32_bf16 v[236:239], v[216:219], v[100:103], 0\n"
  "s_mul_i32 s72, s84, 5\n"
  "s_nop 0\n"
  "s_add_u32 s72, s72, 30720\n"
  "s_add_u32 s72, s72, s80\n"
  "s_nop 0\n"
  "buffer_load_dwordx4 a[112:115], v18, s[36:39], s72 offen sc1\n"
  "buffer_load_dwordx4 a[116:119], v18, s[36:39], s72 offen offset:1024 sc1\n"
  "v_rcp_f32 v240, v240\n"
  "v_rcp_f32 v244, v244\n"
  "v_pk_mul_f32 v[224:225], v[224:225], v[240:241] op_sel_hi:[1,0]\n"
  "v_pk_mul_f32 v[226:227], v[226:227], v[240:241] op_sel_hi:[1,0]\n"
  "v_pk_mul_f32 v[232:233], v[232:233], v[240:241] op_sel_hi:[1,0]\n"
  "v_pk_mul_f32 v[234:235], v[234:235], v[240:241] op_sel_hi:[1,0]\n"
  "v_pk_mul_f32 v[228:229], v[228:229], v[244:245] op_sel_hi:[1,0]\n"
  "v_pk_mul_f32 v[230:231], v[230:231], v[244:245] op_sel_hi:[1,0]\n"
  "v_pk_mul_f32 v[236:237], v[236:237], v[244:245] op_sel_hi:[1,0]\n"
  "v_pk_mul_f32 v[238:239], v[238:239], v[244:245] op_sel_hi:[1,0]\n"
  "v_cvt_pk_bf16_f32 v250, v224, v225\n"
  "v_cvt_pk_bf16_f32 v251, v226, v227\n"
  "v_cvt_pk_bf16_f32 v252, v228, v229\n"
  "v_cvt_pk_bf16_f32 v253, v230, v231\n"
  "v_cvt_pk_bf16_f32 v232, v232, v233\n"
  "v_cvt_pk_bf16_f32 v233, v234, v235\n"
  "v_cndmask_b32_e64 v248, v248, v232, s[94:95]\n"
  "v_cndmask_b32_e64 v249, v249, v233, s[94:95]\n"
  "s_mov_b32 vcc_lo, 0\n"
  "s_nop 0\n"
  "s_mov_b32 vcc_hi, 0xffff0000\n"
  "v_cvt_pk_bf16_f32 v236, v236, v237\n"
  "v_cvt_pk_bf16_f32 v237, v238, v239\n"
  "v_cndmask_b32_e64 v248, v248, v236, vcc\n"
  "v_cndmask_b32_e64 v249, v249, v237, vcc\n"
  "v_permlane16_swap_b32 v250, v252\n"
  "v_permlane16_swap_b32 v251, v253\n"
  "s_add_u32 s66, s70, 32768\n"
  "buffer_store_dwordx4 v[250:253], v56, s[36:39], s66 offen sc1\n"
  "s_add_u32 s66, s71, 0\n"
  "s_nop 0\n"
  "buffer_store_dwordx2 v[248:249], v57, s[36:39], s66 offen sc1\n"
  "s_nop 1\n"
  "s_nop 0\n"
  "v_mfma_f32_16x16x32_bf16 v[76:79], v[156:159], v[128:131], v[64:67]\n"
  "v_mfma_f32_16x16x32_bf16 v[80:83], v[164:167], v[128:131], v[68:71]\n"
  "v_mfma_f32_16x16x32_bf16 v[92:95], v[160:163], v[132:135], v[64:67]\n"
  "v_mfma_f32_16x16x32_bf16 v[96:99], v[168:171], v[132:135], v[68:71]\n"
  "buffer_load_dwordx4 a[120:123], v18, s[36:39], s72 offen offset:2048 sc1\n"
  "buffer_load_dwordx4 a[124:127], v18, s[36:39], s72 offen offset:3072 sc1\n"
  "s_nop 2\n"
  "s_nop 0\n"
  "v_max3_f32 v88, v76, v77, v78\n"
  "v_max3_f32 v89, v79, v80, v81\n"
  "v_max3_f32 v104, v92, v93, v94\n"
  "v_max3_f32 v105, v95, v96, v97\n"
  "v_max3_f32 v88, v88, v82, v83\n"
  "v_max3_f32 v104, v104, v98, v99\n"
  "v_max_f32 v88, v88, v89\n"
  "v_max_f32 v104, v104, v105\n"
  "v_mov_b32 v89, v88\n"
  "v_mov_b32 v105, v104\n"
  "s_nop 0\n"
  "v_permlane16_swap_b32 v88, v89\n"
  "v_permlane16_swap_b32 v104, v105\n"
  "v_max_f32 v88, v88, v89\n"
  "v_max_f32 v104, v104, v105\n"
  "v_mov_b32 v89, v88\n"
  "v_mov_b32 v105, v104\n"
  "s_nop 0\n"
  "v_permlane32_swap_b32 v88, v89\n"
  "v_permlane32_swap_b32 v104, v105\n"
  "v_max_f32 v88, v88, v89\n"
  "v_max_f32 v104, v104, v105\n"
  "v_pk_add_f32 v[76:77], v[76:77], v[88:89] op_sel_hi:[1,0] neg_lo:[0,1] neg_hi:[0,1]\n"
  "v_pk_add_f32 v[78:79], v[78:79], v[88:89] op_sel_hi:[1,0] neg_lo:[0,1] neg_hi:[0,1]\n"
  "v_pk_add_f32 v[80:81], v[80:81], v[88:89] op_sel_hi:[1,0] neg_lo:[0,1] neg_hi:[0,1]\n"
  "v_pk_add_f32 v[82:83], v[82:83], v[88:89] op_sel_hi:[1,0] neg_lo:[0,1] neg_hi:[0,1]\n"
  "v_pk_add_f32 v[92:93], v[92:93], v[104:105] op_sel_hi:[1,0] neg_lo:[0,1] neg_hi:[0,1]\n"
  "v_pk_add_f32 v[94:95], v[94:95], v[104:105] op_sel_hi:[1,0] neg_lo:[0,1] neg_hi:[0,1]\n"
  "v_pk_add_f32 v[96:97], v[96:97], v[104:105] op_sel_hi:[1,0] neg_lo:[0,1] neg_hi:[0,1]\n"
  "v_pk_add_f32 v[98:99], v[98:99], v[104:105] op_sel_hi:[1,0] neg_lo:[0,1] neg_hi:[0,1]\n"
  "s_mul_i32 s72, s84, 5\n"
  "s_nop 0\n"
  "s_add_u32 s72, s72, 30720\n"
  "s_add_u32 s72, s72, s80\n"
  "s_add_u32 m0, s65, 0\n"
  "s_nop 0\n"
  "s_nop 0\n"
  "buffer_load_dwordx4 v19, s[36:39], s72 offen sc1 lds\n"
  "v_exp_f32 v76, v76\n"
  "v_exp_f32 v77, v77\n"
  "v_exp_f32 v78, v78\n"
  "v_exp_f32 v79, v79\n"
  "v_exp_f32 v80, v80\n"
  "v_exp_f32 v81, v81\n"
  "v_exp_f32 v82, v82\n"
  "v_exp_f32 v83, v83\n"
  "v_exp_f32 v92, v92\n"
  "v_exp_f32 v93, v93\n"
  "v_exp_f32 v94, v94\n"
  "v_exp_f32 v95, v95\n"
  "v_exp_f32 v96, v96\n"
  "v_exp_f32 v97, v97\n"
  "v_exp_f32 v98, v98\n"
  "v_exp_f32 v99, v99\n"
  "s_mul_i32 s72, s85, 5\n"
  "s_nop 0\n"
  "s_add_u32 s72, s72, 25600\n"
  "s_add_u32 s72, s72, s80\n"
  "s_nop 0\n"
  "s_add_u32 m0, s65, 1024\n"
  "s_nop 0\n"
  "s_nop 0\n"
  "buffer_load_dwordx4 v18, s[36:39], s72 offen sc1 lds\n"
  "v_cvt_pk_bf16_f32 v84, v76, v77\n"
  "v_cvt_pk_bf16_f32 v85, v78, v79\n"
  "v_cvt_pk_bf16_f32 v86, v80, v81\n"
  "v_cvt_pk_bf16_f32 v87, v82, v83\n"
  "v_cvt_pk_bf16_f32 v100, v92, v93\n"
  "v_cvt_pk_bf16_f32 v101, v94, v95\n"
  "v_cvt_pk_bf16_f32 v102, v96, v97\n"
  "v_cvt_pk_bf16_f32 v103, v98, v99\n"
  "v_mfma_f32_16x16x32_bf16 v[240:243], v[52:55], v[84:87], 0\n"
  "v_mfma_f32_16x16x32_bf16 v[224:227], v[188:191], v[84:87], 0\n"
  "v_mfma_f32_16x16x32_bf16 v[232:235], v[198:201], v[84:87], 0\n"
  "v_mfma_f32_16x16x32_bf16 v[244:247], v[52:55], v[100:103], 0\n"
  "v_mfma_f32_16x16x32_bf16 v[228:231], v[208:211], v[100:103], 0\n"
  "v_mfma_f32_16x16x32_bf16 v[236:239], v[218:221], v[100:103], 0\n"
  "s_add_u32 m0, s65, 1024\n"
  "s_nop 0\n"
  "s_nop 0\n"
  "buffer_load_dwordx4 v18, s[36:39], s72 offen offset:1024 sc1 lds\n"
  "s_add_u32 m0, s65, 1024\n"
  "s_nop 0\n"
  "s_nop 0\n"
  "buffer_load_dwordx4 v18, s[36:39], s72 offen offset:2048 sc1 lds\n"
  "v_rcp_f32 v240, v240\n"
  "v_rcp_f32 v244, v244\n"
  "v_pk_mul_f32 v[224:225], v[224:225], v[240:241] op_sel_hi:[1,0]\n"
  "v_pk_mul_f32 v[226:227], v[226:227], v[240:241] op_sel_hi:[1,0]\n"
  "v_pk_mul_f32 v[232:233], v[232:233], v[240:241] op_sel_hi:[1,0]\n"
  "v_pk_mul_f32 v[234:235], v[234:235], v[240:241] op_sel_hi:[1,0]\n"
  "v_pk_mul_f32 v[228:229], v[228:229], v[244:245] op_sel_hi:[1,0]\n"
  "v_pk_mul_f32 v[230:231], v[230:231], v[244:245] op_sel_hi:[1,0]\n"
  "v_pk_mul_f32 v[236:237], v[236:237], v[244:245] op_sel_hi:[1,0]\n"
  "v_pk_mul_f32 v[238:239], v[238:239], v[244:245] op_sel_hi:[1,0]\n"
  "v_cvt_pk_bf16_f32 v250, v224, v225\n"
  "v_cvt_pk_bf16_f32 v251, v226, v227\n"
  "v_cvt_pk_bf16_f32 v252, v228, v229\n"
  "v_cvt_pk_bf16_f32 v253, v230, v231\n"
  "v_cvt_pk_bf16_f32 v248, v232, v233\n"
  "v_cvt_pk_bf16_f32 v249, v234, v235\n"
  "v_cvt_pk_bf16_f32 v236, v236, v237\n"
  "v_cvt_pk_bf16_f32 v237, v238, v239\n"
  "v_cndmask_b32_e64 v248, v248, v236, s[92:93]\n"
  "v_cndmask_b32_e64 v249, v249, v237, s[92:93]\n"
  "v_permlane16_swap_b32 v250, v252\n"
  "v_permlane16_swap_b32 v251, v253\n"
  "s_add_u32 s66, s70, 65536\n"
  "buffer_store_dwordx4 v[250:253], v56, s[36:39], s66 offen sc1\n"
  "v_mfma_f32_16x16x32_bf16 v[76:79], v[164:167], v[136:139], v[64:67]\n"
  "v_mfma_f32_16x16x32_bf16 v[80:83], v[172:175], v[136:139], v[68:71]\n"
  "v_mfma_f32_16x16x32_bf16 v[92:95], v[168:171], v[140:143], v[64:67]\n"
  "v_mfma_f32_16x16x32_bf16 v[96:99], v[176:179], v[140:143], v[68:71]\n"
  "s_add_u32 m0, s65, 1024\n"
  "s_nop 0\n"
  "s_nop 0\n"
  "buffer_load_dwordx4 v18, s[36:39], s72 offen offset:3072 sc1 lds\n"
  "s_add_u32 m0, s65, 5120\n"
  "s_nop 0\n"
  "s_nop 0\n"
  "buffer_load_dwordx4 v19, s[36:39], s72 offen sc1 lds\n"
  "v_max3_f32 v88, v76, v77, v78\n"
  "v_max3_f32 v89, v79, v80, v81\n"
  "v_max3_f32 v104, v92, v93, v94\n"
  "v_max3_f32 v105, v95, v96, v97\n"
  "v_max3_f32 v88, v88, v82, v83\n"
  "v_max3_f32 v104, v104, v98, v99\n"
  "v_max_f32 v88, v88, v89\n"
  "v_max_f32 v104, v104, v105\n"
  "v_mov_b32 v89, v88\n"
  "v_mov_b32 v105, v104\n"
  "s_nop 0\n"
  "v_permlane16_swap_b32 v88, v89\n"
  "v_permlane16_swap_b32 v104, v105\n"
  "v_max_f32 v88, v88, v89\n"
  "v_max_f32 v104, v104, v105\n"
  "v_mov_b32 v89, v88\n"
  "v_mov_b32 v105, v104\n"
  "s_nop 0\n"
  "v_permlane32_swap_b32 v88, v89\n"
  "v_permlane32_swap_b32 v104, v105\n"
  "v_max_f32 v88, v88, v89\n"
  "v_max_f32 v104, v104, v105\n"
  "v_pk_add_f32 v[76:77], v[76:77], v[88:89] op_sel_hi:[1,0] neg_lo:[0,1] neg_hi:[0,1]\n"
  "v_pk_add_f32 v[78:79], v[78:79], v[88:89] op_sel_hi:[1,0] neg_lo:[0,1] neg_hi:[0,1]\n"
  "v_pk_add_f32 v[80:81], v[80:81], v[88:89] op_sel_hi:[1,0] neg_lo:[0,1] neg_hi:[0,1]\n"
  "v_pk_add_f32 v[82:83], v[82:83], v[88:89] op_sel_hi:[1,0] neg_lo:[0,1] neg_hi:[0,1]\n"
  "v_pk_add_f32 v[92:93], v[92:93], v[104:105] op_sel_hi:[1,0] neg_lo:[0,1] neg_hi:[0,1]\n"
  "v_pk_add_f32 v[94:95], v[94:95], v[104:105] op_sel_hi:[1,0] neg_lo:[0,1] neg_hi:[0,1]\n"
  "v_pk_add_f32 v[96:97], v[96:97], v[104:105] op_sel_hi:[1,0] neg_lo:[0,1] neg_hi:[0,1]\n"
  "v_pk_add_f32 v[98:99], v[98:99], v[104:105] op_sel_hi:[1,0] neg_lo:[0,1] neg_hi:[0,1]\n"
  "s_mul_i32 s72, s85, 5\n"
  "s_nop 0\n"
  "s_add_u32 s72, s72, 30720\n"
  "s_add_u32 s72, s72, s80\n"
  "s_nop 0\n"
  "s_add_u32 m0, s65, 6144\n"
  "s_nop 0\n"
  "s_nop 0\n"
  "buffer_load_dwordx4 v18, s[36:39], s72 offen sc1 lds\n"
  "v_exp_f32 v76, v76\n"
  "v_exp_f32 v77, v77\n"
  "v_exp_f32 v78, v78\n"
  "v_exp_f32 v79, v79\n"
  "v_exp_f32 v80, v80\n"
  "v_exp_f32 v81, v81\n"
  "v_exp_f32 v82, v82\n"
  "v_exp_f32 v83, v83\n"
  "v_exp_f32 v92, v92\n"
  "v_exp_f32 v93, v93\n"
  "v_exp_f32 v94, v94\n"
  "v_exp_f32 v95, v95\n"
  "v_exp_f32 v96, v96\n"
  "v_exp_f32 v97, v97\n"
  "v_exp_f32 v98, v98\n"
  "v_exp_f32 v99, v99\n"
  "s_add_u32 m0, s65, 6144\n"
  "s_nop 0\n"
  "s_nop 0\n"
  "buffer_load_dwordx4 v18, s[36:39], s72 offen offset:1024 sc1 lds\n"
  "v_cvt_pk_bf16_f32 v84, v76, v77\n"
  "v_cvt_pk_bf16_f32 v85, v78, v79\n"
  "v_cvt_pk_bf16_f32 v86, v80, v81\n"
  "v_cvt_pk_bf16_f32 v87, v82, v83\n"
  "v_cvt_pk_bf16_f32 v100, v92, v93\n"
  "v_cvt_pk_bf16_f32 v101, v94, v95\n"
  "v_cvt_pk_bf16_f32 v102, v96, v97\n"
  "v_cvt_pk_bf16_f32 v103, v98, v99\n"
  "v_mfma_f32_16x16x32_bf16 v[240:243], v[52:55], v[84:87], 0\n"
  "v_mfma_f32_16x16x32_bf16 v[224:227], v[190:193], v[84:87], 0\n"
  "v_mfma_f32_16x16x32_bf16 v[232:235], v[200:203], v[84:87], 0\n"
  "v_mfma_f32_16x16x32_bf16 v[244:247], v[52:55], v[100:103], 0\n"
  "v_mfma_f32_16x16x32_bf16 v[228:231], v[210:213], v[100:103], 0\n"
  "v_mfma_f32_16x16x32_bf16 v[236:239], v[220:223], v[100:103], 0\n"
  "s_add_u32 m0, s65, 6144\n"
  "s_nop 0\n"
  "s_nop 0\n"
  "buffer_load_dwordx4 v18, s[36:39], s72 offen offset:2048 sc1 lds\n"
  "s_add_u32 m0, s65, 6144\n"
  "s_nop 0\n"
  "s_nop 0\n"
  "buffer_load_dwordx4 v18, s[36:39], s72 offen offset:3072 sc1 lds\n"
  "v_rcp_f32 v240, v240\n"
  "v_rcp_f32 v244, v244\n"
  "v_pk_mul_f32 v[224:225], v[224:225], v[240:241] op_sel_hi:[1,0]\n"
  "v_pk_mul_f32 v[226:227], v[226:227], v[240:241] op_sel_hi:[1,0]\n"
  "v_pk_mul_f32 v[232:233], v[232:233], v[240:241] op_sel_hi:[1,0]\n"
  "v_pk_mul_f32 v[234:235], v[234:235], v[240:241] op_sel_hi:[1,0]\n"
  "v_pk_mul_f32 v[228:229], v[228:229], v[244:245] op_sel_hi:[1,0]\n"
  "v_pk_mul_f32 v[230:231], v[230:231], v[244:245] op_sel_hi:[1,0]\n"
  "v_pk_mul_f32 v[236:237], v[236:237], v[244:245] op_sel_hi:[1,0]\n"
  "v_pk_mul_f32 v[238:239], v[238:239], v[244:245] op_sel_hi:[1,0]\n"
  "v_cvt_pk_bf16_f32 v250, v224, v225\n"
  "v_cvt_pk_bf16_f32 v251, v226, v227\n"
  "v_cvt_pk_bf16_f32 v252, v228, v229\n"
  "v_cvt_pk_bf16_f32 v253, v230, v231\n"
  "v_cvt_pk_bf16_f32 v232, v232, v233\n"
  "v_cvt_pk_bf16_f32 v233, v234, v235\n"
  "v_cndmask_b32_e64 v248, v248, v232, s[94:95]\n"
  "v_cndmask_b32_e64 v249, v249, v233, s[94:95]\n"
  "s_mov_b32 vcc_lo, 0\n"
  "s_nop 0\n"
  "s_mov_b32 vcc_hi, 0xffff0000\n"
  "v_cvt_pk_bf16_f32 v236, v236, v237\n"
  "v_cvt_pk_bf16_f32 v237, v238, v239\n"
  "v_cndmask_b32_e64 v248, v248, v236, vcc\n"
  "v_cndmask_b32_e64 v249, v249, v237, vcc\n"
  "v_permlane16_swap_b32 v250, v252\n"
  "v_permlane16_swap_b32 v251, v253\n"
  "s_add_u32 s66, s70, 98304\n"
  "buffer_store_dwordx4 v[250:253], v56, s[36:39], s66 offen sc1\n"
  "s_add_u32 s66, s71, 16384\n"
  "buffer_store_dwordx2 v[248:249], v57, s[36:39], s66 offen sc1\n"
  "s_nop 1\n"
  "s_lshl_b32 s70, s47, 7\n"
  "s_lshl_b32 s71, s48, 3\n"
  "s_add_u32 s70, s70, s71\n"
  "s_add_u32 s70, s70, 16384\n"
  "s_waitcnt vmcnt(0)\n"
  "s_barrier\n"
  "v_mov_b32 v20, s44\n"
  "v_mov_b32 v21, s45\n"
  "s_mov_b64 exec, s[88:89]\n"
  "s_nop 0\n"
  "buffer_store_dwordx2 v[20:21], off, s[36:39], s70 sc1\n"
  "s_mov_b64 exec, -1\n"
  "s_nop 0\n"
  "s_add_u32 m0, s65, 10240\n"
  "s_nop 0\n"
  "s_nop 0\n"
  "buffer_load_dwordx4 v19, s[36:39], s72 offen sc1 lds\n"
  "s_mul_i32 s72, s86, 5\n"
  "s_nop 0\n"
  "s_add_u32 s72, s72, 25600\n"
  "s_add_u32 s72, s72, s80\n"
  "s_nop 0\n"
  "s_add_u32 m0, s65, 11264\n"
  "s_nop 0\n"
  "s_nop 0\n"
  "buffer_load_dwordx4 v18, s[36:39], s72 offen sc1 lds\n"
  "s_add_u32 m0, s65, 11264\n"
  "s_nop 0\n"
  "s_nop 0\n"
  "buffer_load_dwordx4 v18, s[36:39], s72 offen offset:1024 sc1 lds\n"
  "s_add_u32 m0, s65, 11264\n"
  "s_nop 0\n"
  "s_nop 0\n"
  "buffer_load_dwordx4 v18, s[36:39], s72 offen offset:2048 sc1 lds\n"
  "s_add_u32 m0, s65, 11264\n"
  "s_nop 0\n"
  "s_nop 0\n"
  "buffer_load_dwordx4 v18, s[36:39], s72 offen offset:3072 sc1 lds\n"
  "s_add_u32 m0, s65, 15360\n"
  "s_nop 0\n"
  "s_nop 0\n"
  "buffer_load_dwordx4 v19, s[36:39], s72 offen sc1 lds\n"
  "s_mul_i32 s72, s86, 5\n"
  "s_nop 0\n"
  "s_add_u32 s72, s72, 30720\n"
  "s_add_u32 s72, s72, s80\n"
  "s_nop 0\n"
  "s_add_u32 m0, s65, 16384\n"
  "s_nop 0\n"
  "s_nop 0\n"
  "buffer_load_dwordx4 v18, s[36:39], s72 offen sc1 lds\n"
  "s_add_u32 m0, s65, 16384\n"
  "s_nop 0\n"
  "s_nop 0\n"
  "buffer_load_dwordx4 v18, s[36:39], s72 offen offset:1024 sc1 lds\n"
  "s_add_u32 m0, s65, 16384\n"
  "s_nop 0\n"
  "s_nop 0\n"
  "buffer_load_dwordx4 v18, s[36:39], s72 offen offset:2048 sc1 lds\n"
  "s_add_u32 m0, s65, 16384\n"
  "s_nop 0\n"
  "s_nop 0\n"
  "buffer_load_dwordx4 v18, s[36:39], s72 offen offset:3072 sc1 lds\n"
  "s_add_u32 m0, s65, 20480\n"
  "s_nop 0\n"
  "s_nop 0\n"
  "buffer_load_dwordx4 v19, s[36:39], s72 offen sc1 lds\n"
  ".LP2:\n"
  "s_lshr_b32 s70, s78, 1\n"
  "s_lshl_b32 s70, s70, 7\n"
  "s_lshl_b32 s71, s46, 4\n"
  "s_add_u32 s70, s70, s71\n"
  "s_add_u32 s70, s70, 16384\n"
  "buffer_load_dwordx4 v[40:43], off, s[36:39], s70 sc1\n"
  "v_lshlrev_b32 v20, 3, v0\n"
  "s_add_u32 s66, s46, 1\n"
  "s_and_b32 s66, s66, 3\n"
  "s_lshl_b32 s67, s78, 2\n"
  "s_add_u32 s66, s66, s67\n"
  "s_lshl_b32 s67, s66, 14\n"
  "v_add_u32 v2, s67, v18\n"
  "s_lshl_b32 s67, s66, 12\n"
  "v_add_u32_e64 v6, s67, v20\n"
  "s_mul_i32 s67, s66, 49152\n"
  "v_add_u32 v10, s67, v18\n"
  "s_add_u32 s66, s46, 2\n"
  "s_and_b32 s66, s66, 3\n"
  "s_lshl_b32 s67, s78, 2\n"
  "s_add_u32 s66, s66, s67\n"
  "s_lshl_b32 s67, s66, 14\n"
  "v_add_u32 v3, s67, v18\n"
  "s_lshl_b32 s67, s66, 12\n"
  "v_add_u32_e64 v7, s67, v20\n"
  "s_mul_i32 s67, s66, 49152\n"
  "v_add_u32 v11, s67, v18\n"
  "s_add_u32 s66, s46, 3\n"
  "s_and_b32 s66, s66, 3\n"
  "s_lshl_b32 s67, s78, 2\n"
  "s_add_u32 s66, s66, s67\n"
  "s_lshl_b32 s67, s66, 14\n"
  "v_add_u32 v4, s67, v18\n"
  "s_lshl_b32 s67, s66, 12\n"
  "v_add_u32_e64 v8, s67, v20\n"
  "s_mul_i32 s67, s66, 49152\n"
  "v_add_u32 v12, s67, v18\n"
  "s_add_u32 s66, s46, 4\n"
  "s_and_b32 s66, s66, 3\n"
  "s_lshl_b32 s67, s78, 2\n"
  "s_add_u32 s66, s66, s67\n"
  "s_lshl_b32 s67, s66, 14\n"
  "v_add_u32 v5, s67, v18\n"
  "s_lshl_b32 s67, s66, 12\n"
  "v_add_u32_e64 v9, s67, v20\n"
  "s_mul_i32 s67, s66, 49152\n"
  "v_add_u32 v13, s67, v18\n"
  "s_lshl_b32 s52, s46, 12\n"
  "s_add_u32 s52, s52, 11534336\n"
  "s_lshl_b32 s53, s46, 10\n"
  "s_nop 0\n"
  "s_add_u32 s53, s53, 14680064\n"
  "s_mul_i32 s66, s46, 12288\n"
  "s_add_u32 s66, s66, 4194304\n"
  "s_add_u32 s54, s66, s81\n"
  "s_add_u32 s55, s66, s82\n"
  "s_add_u32 s56, s66, s83\n"
  "s_add_u32 s57, s66, s84\n"
  "s_add_u32 s58, s66, s85\n"
  "s_add_u32 s59, s66, s86\n"
  "s_lshl_b32 s66, s78, 6\n"
  "s_lshr_b32 s67, s46, 1\n"
  "s_lshl_b32 s67, s67, 5\n"
  "s_add_u32 s66, s66, s67\n"
  "s_and_b32 s67, s46, 1\n"
  "s_add_u32 s66, s66, s67\n"
  "s_mul_i32 s66, s66, 1280\n"
  "s_mul_i32 s67, s79, 160\n"
  "s_add_u32 s66, s66, s67\n"
  "v_and_b32 v21, 15, v0\n"
  "v_lshrrev_b32 v22, 4, v0\n"
  "s_movk_i32 s67, 2560\n"
  "v_mul_u32_u24 v21, s67, v21\n"
  "v_mul_u32_u24 v22, 40, v22\n"
  "v_add3_u32 v14, v21, v22, s66\n"
  "s_mov_b32 s40, s20\n"
  "s_nop 0\n"
  "s_and_b32 s41, s21, 0xffff\n"
  "s_mov_b32 s42, 2621440\n"
  "s_mov_b32 s43, 0x20000\n"
  "v_add_u32 v15, s65, v18\n"
  "s_add_u32 s66, s46, 1\n"
  "s_and_b32 s66, s66, 3\n"
  "s_nop 0\n"
  "s_mul_i32 s66, s66, 15360\n"
  "s_add_u32 s66, s66, 86016\n"
  "v_add_u32 v34, s66, v18\n"
  "s_add_u32 s66, s46, 2\n"
  "s_and_b32 s66, s66, 3\n"
  "s_nop 0\n"
  "s_mul_i32 s66, s66, 15360\n"
  "s_add_u32 s66, s66, 91136\n"
  "v_add_u32 v35, s66, v18\n"
  "s_add_u32 s66, s46, 3\n"
  "s_and_b32 s66, s66, 3\n"
  "s_nop 0\n"
  "s_mul_i32 s66, s66, 15360\n"
  "s_add_u32 s66, s66, 96256\n"
  "v_add_u32_e64 v36, s66, v18\n"
  "s_mul_i32 s66, s46, 15360\n"
  "s_add_u32 s66, s66, 86016\n"
  "v_add_u32 v37, s66, v18\n"
  "s_waitcnt vmcnt(0)\n"
  "v_cmp_ne_u64_e64 s[66:67], s[44:45], v[40:41]\n"
  "v_cmp_ne_u64_e64 s[68:69], s[44:45], v[42:43]\n"
  "s_nop 0\n"
  "s_or_b64 s[66:67], s[66:67], s[68:69]\n"
  "s_cmp_eq_u64 s[66:67], 0\n"
  "s_cbranch_scc1 .Lpok24\n"
  "s_mov_b32 s87, 0x40000\n"
  ".Lpsp24:\n"
  "s_sleep 2\n"
  "s_lshr_b32 s70, s78, 1\n"
  "s_lshl_b32 s70, s70, 7\n"
  "s_lshl_b32 s71, s46, 4\n"
  "s_add_u32 s70, s70, s71\n"
  "s_nop 0\n"
  "s_add_u32 s70, s70, 16384\n"
  "buffer_load_dwordx4 v[40:43], off, s[36:39], s70 sc1\n"
  "s_waitcnt vmcnt(0)\n"
  "s_nop 0\n"
  "v_cmp_ne_u64_e64 s[66:67], s[44:45], v[40:41]\n"
  "v_cmp_ne_u64_e64 s[68:69], s[44:45], v[42:43]\n"
  "s_nop 0\n"
  "s_or_b64 s[66:67], s[66:67], s[68:69]\n"
  "s_cmp_eq_u64 s[66:67], 0\n"
  "s_cbranch_scc1 .Lpok24\n"
  "s_sub_u32 s87, s87, 1\n"
  "s_cmp_lg_u32 s87, 0\n"
  "s_cbranch_scc1 .Lpsp24\n"
  ".Lpok24:\n"
  "s_nop 0\n"
  "buffer_load_dwordx4 v[64:67], v2, s[36:39], s52 offen sc1\n"
  "buffer_load_dwordx4 v[68:71], v3, s[36:39], s52 offen sc1\n"
  "buffer_load_dwordx4 v[72:75], v4, s[36:39], s52 offen sc1\n"
  "buffer_load_dwordx4 v[76:79], v5, s[36:39], s52 offen sc1\n"
  "buffer_load_dwordx4 v[80:83], v2, s[36:39], s52 offen offset:1024 sc1\n"
  "buffer_load_dwordx4 v[84:87], v3, s[36:39], s52 offen offset:1024 sc1\n"
  "buffer_load_dwordx4 v[88:91], v4, s[36:39], s52 offen offset:1024 sc1\n"
  "buffer_load_dwordx4 v[92:95], v5, s[36:39], s52 offen offset:1024 sc1\n"
  "buffer_load_dwordx4 v[96:99], v2, s[36:39], s52 offen offset:2048 sc1\n"
  "buffer_load_dwordx4 v[100:103], v3, s[36:39], s52 offen offset:2048 sc1\n"
  "buffer_load_dwordx4 v[104:107], v4, s[36:39], s52 offen offset:2048 sc1\n"
  "buffer_load_dwordx4 v[108:111], v5, s[36:39], s52 offen offset:2048 sc1\n"
  "buffer_load_dwordx4 v[112:115], v2, s[36:39], s52 offen offset:3072 sc1\n"
  "buffer_load_dwordx4 v[116:119], v3, s[36:39], s52 offen offset:3072 sc1\n"
  "buffer_load_dwordx4 v[120:123], v4, s[36:39], s52 offen offset:3072 sc1\n"
  "buffer_load_dwordx4 v[124:127], v5, s[36:39], s52 offen offset:3072 sc1\n"
  "buffer_load_dwordx2 v[128:129], v6, s[36:39], s53 offen sc1\n"
  "buffer_load_dwordx2 v[130:131], v6, s[36:39], s53 offen offset:512 sc1\n"
  "buffer_load_dwordx2 v[132:133], v7, s[36:39], s53 offen sc1\n"
  "buffer_load_dwordx2 v[134:135], v7, s[36:39], s53 offen offset:512 sc1\n"
  "buffer_load_dwordx2 v[136:137], v8, s[36:39], s53 offen sc1\n"
  "buffer_load_dwordx2 v[138:139], v8, s[36:39], s53 offen offset:512 sc1\n"
  "buffer_load_dwordx2 v[140:141], v9, s[36:39], s53 offen sc1\n"
  "buffer_load_dwordx2 v[142:143], v9, s[36:39], s53 offen offset:512 sc1\n"
  "buffer_load_dwordx4 v[24:27], v14, s[24:27], 0 offen\n"
  "buffer_load_dwordx4 v[28:31], v14, s[24:27], 0 offen offset:16\n"
  "buffer_load_dwordx2 v[32:33], v14, s[24:27], 0 offen offset:32\n"
  "s_lshr_b32 s70, s78, 1\n"
  "s_lshl_b32 s70, s70, 7\n"
  "s_lshl_b32 s71, s46, 4\n"
  "s_add_u32 s70, s70, s71\n"
  "s_add_u32 s70, s70, 12288\n"
  "buffer_load_dwordx4 v[40:43], off, s[36:39], s70 sc1\n"
  "s_waitcnt vmcnt(27)\n"
  "s_nop 0\n"
  "v_mfma_f32_16x16x32_bf16 v[176:179], a[128:131], v[64:67], 0\n"
  "v_mfma_f32_16x16x32_bf16 v[180:183], a[132:135], v[64:67], 0\n"
  "v_mfma_f32_16x16x32_bf16 v[184:187], a[136:139], v[64:67], 0\n"
  "v_mfma_f32_16x16x32_bf16 v[188:191], a[140:143], v[64:67], 0\n"
  "v_mfma_f32_16x16x32_bf16 v[192:195], a[144:147], v[64:67], 0\n"
  "s_waitcnt vmcnt(26)\n"
  "s_nop 0\n"
  "v_mfma_f32_16x16x32_bf16 v[196:199], a[128:131], v[68:71], 0\n"
  "v_mfma_f32_16x16x32_bf16 v[200:203], a[132:135], v[68:71], 0\n"
  "v_mfma_f32_16x16x32_bf16 v[204:207], a[136:139], v[68:71], 0\n"
  "v_mfma_f32_16x16x32_bf16 v[208:211], a[140:143], v[68:71], 0\n"
  "v_mfma_f32_16x16x32_bf16 v[212:215], a[144:147], v[68:71], 0\n"
  "s_waitcnt vmcnt(25)\n"
  "s_nop 0\n"
  "v_mfma_f32_16x16x32_bf16 v[216:219], a[128:131], v[72:75], 0\n"
  "v_mfma_f32_16x16x32_bf16 v[220:223], a[132:135], v[72:75], 0\n"
  "v_mfma_f32_16x16x32_bf16 v[224:227], a[136:139], v[72:75], 0\n"
  "v_mfma_f32_16x16x32_bf16 v[228:231], a[140:143], v[72:75], 0\n"
  "v_mfma_f32_16x16x32_bf16 v[232:235], a[144:147], v[72:75], 0\n"
  "s_waitcnt vmcnt(24)\n"
  "s_nop 0\n"
  "v_mfma_f32_16x16x32_bf16 v[236:239], a[128:131], v[76:79], 0\n"
  "v_mfma_f32_16x16x32_bf16 v[240:243], a[132:135], v[76:79], 0\n"
  "v_mfma_f32_16x16x32_bf16 v[244:247], a[136:139], v[76:79], 0\n"
  "v_mfma_f32_16x16x32_bf16 v[248:251], a[140:143], v[76:79], 0\n"
  "v_mfma_f32_16x16x32_bf16 v[252:255], a[144:147], v[76:79], 0\n"
  "s_waitcnt vmcnt(23)\n"
  "s_nop 0\n"
  "v_mfma_f32_16x16x32_bf16 v[176:179], a[148:151], v[80:83], v[176:179]\n"
  "v_mfma_f32_16x16x32_bf16 v[180:183], a[152:155], v[80:83], v[180:183]\n"
  "v_mfma_f32_16x16x32_bf16 v[184:187], a[156:159], v[80:83], v[184:187]\n"
  "v_mfma_f32_16x16x32_bf16 v[188:191], a[160:163], v[80:83], v[188:191]\n"
  "v_mfma_f32_16x16x32_bf16 v[192:195], a[164:167], v[80:83], v[192:195]\n"
  "s_waitcnt vmcnt(22)\n"
  "s_nop 0\n"
  "v_mfma_f32_16x16x32_bf16 v[196:199], a[148:151], v[84:87], v[196:199]\n"
  "v_mfma_f32_16x16x32_bf16 v[200:203], a[152:155], v[84:87], v[200:203]\n"
  "v_mfma_f32_16x16x32_bf16 v[204:207], a[156:159], v[84:87], v[204:207]\n"
  "v_mfma_f32_16x16x32_bf16 v[208:211], a[160:163], v[84:87], v[208:211]\n"
  "v_mfma_f32_16x16x32_bf16 v[212:215], a[164:167], v[84:87], v[212:215]\n"
  "s_waitcnt vmcnt(21)\n"
  "s_nop 0\n"
  "v_mfma_f32_16x16x32_bf16 v[216:219], a[148:151], v[88:91], v[216:219]\n"
  "v_mfma_f32_16x16x32_bf16 v[220:223], a[152:155], v[88:91], v[220:223]\n"
  "v_mfma_f32_16x16x32_bf16 v[224:227], a[156:159], v[88:91], v[224:227]\n"
  "v_mfma_f32_16x16x32_bf16 v[228:231], a[160:163], v[88:91], v[228:231]\n"
  "v_mfma_f32_16x16x32_bf16 v[232:235], a[164:167], v[88:91], v[232:235]\n"
  "s_waitcnt vmcnt(20)\n"
  "s_nop 0\n"
  "v_mfma_f32_16x16x32_bf16 v[236:239], a[148:151], v[92:95], v[236:239]\n"
  "v_mfma_f32_16x16x32_bf16 v[240:243], a[152:155], v[92:95], v[240:243]\n"
  "v_mfma_f32_16x16x32_bf16 v[244:247], a[156:159], v[92:95], v[244:247]\n"
  "v_mfma_f32_16x16x32_bf16 v[248:251], a[160:163], v[92:95], v[248:251]\n"
  "v_mfma_f32_16x16x32_bf16 v[252:255], a[164:167], v[92:95], v[252:255]\n"
  "s_waitcnt vmcnt(19)\n"
  "s_nop 0\n"
  "v_mfma_f32_16x16x32_bf16 v[176:179], a[168:171], v[96:99], v[176:179]\n"
  "v_mfma_f32_16x16x32_bf16 v[180:183], a[172:175], v[96:99], v[180:183]\n"
  "v_mfma_f32_16x16x32_bf16 v[184:187], a[176:179], v[96:99], v[184:187]\n"
  "v_mfma_f32_16x16x32_bf16 v[188:191], a[180:183], v[96:99], v[188:191]\n"
  "v_mfma_f32_16x16x32_bf16 v[192:195], a[184:187], v[96:99], v[192:195]\n"
  "s_waitcnt vmcnt(18)\n"
  "s_nop 0\n"
  "v_mfma_f32_16x16x32_bf16 v[196:199], a[168:171], v[100:103], v[196:199]\n"
  "v_mfma_f32_16x16x32_bf16 v[200:203], a[172:175], v[100:103], v[200:203]\n"
  "v_mfma_f32_16x16x32_bf16 v[204:207], a[176:179], v[100:103], v[204:207]\n"
  "v_mfma_f32_16x16x32_bf16 v[208:211], a[180:183], v[100:103], v[208:211]\n"
  "v_mfma_f32_16x16x32_bf16 v[212:215], a[184:187], v[100:103], v[212:215]\n"
  "s_waitcnt vmcnt(17)\n"
  "s_nop 0\n"
  "v_mfma_f32_16x16x32_bf16 v[216:219], a[168:171], v[104:107], v[216:219]\n"
  "v_mfma_f32_16x16x32_bf16 v[220:223], a[172:175], v[104:107], v[220:223]\n"
  "v_mfma_f32_16x16x32_bf16 v[224:227], a[176:179], v[104:107], v[224:227]\n"
  "v_mfma_f32_16x16x32_bf16 v[228:231], a[180:183], v[104:107], v[228:231]\n"
  "v_mfma_f32_16x16x32_bf16 v[232:235], a[184:187], v[104:107], v[232:235]\n"
  "s_waitcnt vmcnt(16)\n"
  "s_nop 0\n"
  "v_mfma_f32_16x16x32_bf16 v[236:239], a[168:171], v[108:111], v[236:239]\n"
  "v_mfma_f32_16x16x32_bf16 v[240:243], a[172:175], v[108:111], v[240:243]\n"
  "v_mfma_f32_16x16x32_bf16 v[244:247], a[176:179], v[108:111], v[244:247]\n"
  "v_mfma_f32_16x16x32_bf16 v[248:251], a[180:183], v[108:111], v[248:251]\n"
  "v_mfma_f32_16x16x32_bf16 v[252:255], a[184:187], v[108:111], v[252:255]\n"
  "s_waitcnt vmcnt(0)\n"
  "s_nop 0\n"
  "v_cmp_ne_u64_e64 s[66:67], s[44:45], v[40:41]\n"
  "v_cmp_ne_u64_e64 s[68:69], s[44:45], v[42:43]\n"
  "s_nop 0\n"
  "s_or_b64 s[66:67], s[66:67], s[68:69]\n"
  "s_cmp_eq_u64 s[66:67], 0\n"
  "s_cbranch_scc1 .Lpok25\n"
  "s_mov_b32 s87, 0x40000\n"
  ".Lpsp25:\n"
  "s_sleep 2\n"
  "s_lshr_b32 s70, s78, 1\n"
  "s_lshl_b32 s70, s70, 7\n"
  "s_lshl_b32 s71, s46, 4\n"
  "s_add_u32 s70, s70, s71\n"
  "s_nop 0\n"
  "s_add_u32 s70, s70, 12288\n"
  "buffer_load_dwordx4 v[40:43], off, s[36:39], s70 sc1\n"
  "s_waitcnt vmcnt(0)\n"
  "s_nop 0\n"
  "v_cmp_ne_u64_e64 s[66:67], s[44:45], v[40:41]\n"
  "v_cmp_ne_u64_e64 s[68:69], s[44:45], v[42:43]\n"
  "s_nop 0\n"
  "s_or_b64 s[66:67], s[66:67], s[68:69]\n"
  "s_cmp_eq_u64 s[66:67], 0\n"
  "s_cbranch_scc1 .Lpok25\n"
  "s_sub_u32 s87, s87, 1\n"
  "s_cmp_lg_u32 s87, 0\n"
  "s_cbranch_scc1 .Lpsp25\n"
  ".Lpok25:\n"
  "s_nop 0\n"
  "v_mfma_f32_16x16x32_bf16 v[176:179], a[188:191], v[112:115], v[176:179]\n"
  "buffer_load_dwordx4 v[144:147], v10, s[36:39], s54 offen sc1\n"
  "v_mfma_f32_16x16x32_bf16 v[180:183], a[192:195], v[112:115], v[180:183]\n"
  "v_mfma_f32_16x16x32_bf16 v[184:187], a[196:199], v[112:115], v[184:187]\n"
  "buffer_load_dwordx4 v[148:151], v11, s[36:39], s54 offen sc1\n"
  "v_mfma_f32_16x16x32_bf16 v[188:191], a[200:203], v[112:115], v[188:191]\n"
  "v_mfma_f32_16x16x32_bf16 v[192:195], a[204:207], v[112:115], v[192:195]\n"
  "v_mfma_f32_16x16x32_bf16 v[196:199], a[188:191], v[116:119], v[196:199]\n"
  "buffer_load_dwordx4 v[152:155], v12, s[36:39], s54 offen sc1\n"
  "v_mfma_f32_16x16x32_bf16 v[200:203], a[192:195], v[116:119], v[200:203]\n"
  "v_mfma_f32_16x16x32_bf16 v[204:207], a[196:199], v[116:119], v[204:207]\n"
  "buffer_load_dwordx4 v[156:159], v13, s[36:39], s54 offen sc1\n"
  "v_mfma_f32_16x16x32_bf16 v[208:211], a[200:203], v[116:119], v[208:211]\n"
  "v_mfma_f32_16x16x32_bf16 v[212:215], a[204:207], v[116:119], v[212:215]\n"
  "v_mfma_f32_16x16x32_bf16 v[216:219], a[188:191], v[120:123], v[216:219]\n"
  "buffer_load_dwordx4 v[160:163], v10, s[36:39], s54 offen offset:1024 sc1\n"
  "v_mfma_f32_16x16x32_bf16 v[220:223], a[192:195], v[120:123], v[220:223]\n"
  "v_mfma_f32_16x16x32_bf16 v[224:227], a[196:199], v[120:123], v[224:227]\n"
  "buffer_load_dwordx4 v[164:167], v11, s[36:39], s54 offen offset:1024 sc1\n"
  "v_mfma_f32_16x16x32_bf16 v[228:231], a[200:203], v[120:123], v[228:231]\n"
  "v_mfma_f32_16x16x32_bf16 v[232:235], a[204:207], v[120:123], v[232:235]\n"
  "v_mfma_f32_16x16x32_bf16 v[236:239], a[188:191], v[124:127], v[236:239]\n"
  "buffer_load_dwordx4 v[168:171], v12, s[36:39], s54 offen offset:1024 sc1\n"
  "v_mfma_f32_16x16x32_bf16 v[240:243], a[192:195], v[124:127], v[240:243]\n"
  "v_mfma_f32_16x16x32_bf16 v[244:247], a[196:199], v[124:127], v[244:247]\n"
  "buffer_load_dwordx4 v[172:175], v13, s[36:39], s54 offen offset:1024 sc1\n"
  "v_mfma_f32_16x16x32_bf16 v[248:251], a[200:203], v[124:127], v[248:251]\n"
  "v_mfma_f32_16x16x32_bf16 v[252:255], a[204:207], v[124:127], v[252:255]\n"
  "v_mfma_f32_16x16x32_bf16 v[176:179], a[208:211], v[128:131], v[176:179]\n"
  "buffer_load_dwordx4 v[64:67], v10, s[36:39], s55 offen sc1\n"
  "v_mfma_f32_16x16x32_bf16 v[180:183], a[212:215], v[128:131], v[180:183]\n"
  "v_mfma_f32_16x16x32_bf16 v[184:187], a[216:219], v[128:131], v[184:187]\n"
  "buffer_load_dwordx4 v[68:71], v11, s[36:39], s55 offen sc1\n"
  "v_mfma_f32_16x16x32_bf16 v[188:191], a[220:223], v[128:131], v[188:191]\n"
  "v_mfma_f32_16x16x32_bf16 v[192:195], a[224:227], v[128:131], v[192:195]\n"
  "v_mfma_f32_16x16x32_bf16 v[196:199], a[208:211], v[132:135], v[196:199]\n"
  "buffer_load_dwordx4 v[72:75], v12, s[36:39], s55 offen sc1\n"
  "v_mfma_f32_16x16x32_bf16 v[200:203], a[212:215], v[132:135], v[200:203]\n"
  "v_mfma_f32_16x16x32_bf16 v[204:207], a[216:219], v[132:135], v[204:207]\n"
  "buffer_load_dwordx4 v[76:79], v13, s[36:39], s55 offen sc1\n"
  "v_mfma_f32_16x16x32_bf16 v[208:211], a[220:223], v[132:135], v[208:211]\n"
  "v_mfma_f32_16x16x32_bf16 v[212:215], a[224:227], v[132:135], v[212:215]\n"
  "v_mfma_f32_16x16x32_bf16 v[216:219], a[208:211], v[136:139], v[216:219]\n"
  "buffer_load_dwordx4 v[80:83], v10, s[36:39], s55 offen offset:1024 sc1\n"
  "v_mfma_f32_16x16x32_bf16 v[220:223], a[212:215], v[136:139], v[220:223]\n"
  "v_mfma_f32_16x16x32_bf16 v[224:227], a[216:219], v[136:139], v[224:227]\n"
  "buffer_load_dwordx4 v[84:87], v11, s[36:39], s55 offen offset:1024 sc1\n"
  "v_mfma_f32_16x16x32_bf16 v[228:231], a[220:223], v[136:139], v[228:231]\n"
  "v_mfma_f32_16x16x32_bf16 v[232:235], a[224:227], v[136:139], v[232:235]\n"
  "v_mfma_f32_16x16x32_bf16 v[236:239], a[208:211], v[140:143], v[236:239]\n"
  "buffer_load_dwordx4 v[88:91], v12, s[36:39], s55 offen offset:1024 sc1\n"
  "v_mfma_f32_16x16x32_bf16 v[240:243], a[212:215], v[140:143], v[240:243]\n"
  "v_mfma_f32_16x16x32_bf16 v[244:247], a[216:219], v[140:143], v[244:247]\n"
  "buffer_load_dwordx4 v[92:95], v13, s[36:39], s55 offen offset:1024 sc1\n"
  "v_mfma_f32_16x16x32_bf16 v[248:251], a[220:223], v[140:143], v[248:251]\n"
  "v_mfma_f32_16x16x32_bf16 v[252:255], a[224:227], v[140:143], v[252:255]\n"
  "s_waitcnt vmcnt(15)\n"
  "s_nop 0\n"
  "v_mfma_f32_16x16x32_bf16 v[176:179], a[228:231], v[144:147], v[176:179]\n"
  "buffer_load_dwordx4 v[96:99], v10, s[36:39], s56 offen sc1\n"
  "v_mfma_f32_16x16x32_bf16 v[180:183], a[232:235], v[144:147], v[180:183]\n"
  "v_mfma_f32_16x16x32_bf16 v[184:187], a[236:239], v[144:147], v[184:187]\n"
  "buffer_load_dwordx4 v[100:103], v11, s[36:39], s56 offen sc1\n"
  "v_mfma_f32_16x16x32_bf16 v[188:191], a[240:243], v[144:147], v[188:191]\n"
  "v_mfma_f32_16x16x32_bf16 v[192:195], a[244:247], v[144:147], v[192:195]\n"
  "s_waitcnt vmcnt(16)\n"
  "s_nop 0\n"
  "v_mfma_f32_16x16x32_bf16 v[196:199], a[228:231], v[148:151], v[196:199]\n"
  "buffer_load_dwordx4 v[104:107], v12, s[36:39], s56 offen sc1\n"
  "v_mfma_f32_16x16x32_bf16 v[200:203], a[232:235], v[148:151], v[200:203]\n"
  "v_mfma_f32_16x16x32_bf16 v[204:207], a[236:239], v[148:151], v[204:207]\n"
  "buffer_load_dwordx4 v[108:111], v13, s[36:39], s56 offen sc1\n"
  "v_mfma_f32_16x16x32_bf16 v[208:211], a[240:243], v[148:151], v[208:211]\n"
  "v_mfma_f32_16x16x32_bf16 v[212:215], a[244:247], v[148:151], v[212:215]\n"
  "s_waitcnt vmcnt(17)\n"
  "s_nop 0\n"
  "v_mfma_f32_16x16x32_bf16 v[216:219], a[228:231], v[152:155], v[216:219]\n"
  "buffer_load_dwordx4 v[112:115], v10, s[36:39], s56 offen offset:1024 sc1\n"
  "v_mfma_f32_16x16x32_bf16 v[220:223], a[232:235], v[152:155], v[220:223]\n"
  "v_mfma_f32_16x16x32_bf16 v[224:227], a[236:239], v[152:155], v[224:227]\n"
  "buffer_load_dwordx4 v[116:119], v11, s[36:39], s56 offen offset:1024 sc1\n"
  "v_mfma_f32_16x16x32_bf16 v[228:231], a[240:243], v[152:155], v[228:231]\n"
  "v_mfma_f32_16x16x32_bf16 v[232:235], a[244:247], v[152:155], v[232:235]\n"
  "s_waitcnt vmcnt(18)\n"
  "s_nop 0\n"
  "v_mfma_f32_16x16x32_bf16 v[236:239], a[228:231], v[156:159], v[236:239]\n"
  "buffer_load_dwordx4 v[120:123], v12, s[36:39], s56 offen offset:1024 sc1\n"
  "v_mfma_f32_16x16x32_bf16 v[240:243], a[232:235], v[156:159], v[240:243]\n"
  "v_mfma_f32_16x16x32_bf16 v[244:247], a[236:239], v[156:159], v[244:247]\n"
  "buffer_load_dwordx4 v[124:127], v13, s[36:39], s56 offen offset:1024 sc1\n"
  "v_mfma_f32_16x16x32_bf16 v[248:251], a[240:243], v[156:159], v[248:251]\n"
  "v_mfma_f32_16x16x32_bf16 v[252:255], a[244:247], v[156:159], v[252:255]\n"
  "s_waitcnt vmcnt(19)\n"
  "s_nop 0\n"
  "v_mfma_f32_16x16x32_bf16 v[176:179], a[248:251], v[160:163], v[176:179]\n"
  "buffer_load_dwordx4 v[128:131], v10, s[36:39], s57 offen sc1\n"
  "v_mfma_f32_16x16x32_bf16 v[180:183], a[252:255], v[160:163], v[180:183]\n"
  "ds_read_b128 a[128:131], v15 offset:0\n"
  "v_mfma_f32_16x16x32_bf16 v[184:187], a[0:3], v[160:163], v[184:187]\n"
  "buffer_load_dwordx4 v[132:135], v11, s[36:39], s57 offen sc1\n"
  "v_mfma_f32_16x16x32_bf16 v[188:191], a[4:7], v[160:163], v[188:191]\n"
  "v_mfma_f32_16x16x32_bf16 v[192:195], a[8:11], v[160:163], v[192:195]\n"
  "s_waitcnt vmcnt(20)\n"
  "s_nop 0\n"
  "v_mfma_f32_16x16x32_bf16 v[196:199], a[248:251], v[164:167], v[196:199]\n"
  "buffer_load_dwordx4 v[136:139], v12, s[36:39], s57 offen sc1\n"
  "v_mfma_f32_16x16x32_bf16 v[200:203], a[252:255], v[164:167], v[200:203]\n"
  "ds_read_b128 a[132:135], v15 offset:1024\n"
  "v_mfma_f32_16x16x32_bf16 v[204:207], a[0:3], v[164:167], v[204:207]\n"
  "buffer_load_dwordx4 v[140:143], v13, s[36:39], s57 offen sc1\n"
  "v_mfma_f32_16x16x32_bf16 v[208:211], a[4:7], v[164:167], v[208:211]\n"
  "v_mfma_f32_16x16x32_bf16 v[212:215], a[8:11], v[164:167], v[212:215]\n"
  "s_waitcnt vmcnt(21)\n"
  "s_nop 0\n"
  "v_mfma_f32_16x16x32_bf16 v[216:219], a[248:251], v[168:171], v[216:219]\n"
  "buffer_load_dwordx4 v[144:147], v10, s[36:39], s57 offen offset:1024 sc1\n"
  "v_mfma_f32_16x16x32_bf16 v[220:223], a[252:255], v[168:171], v[220:223]\n"
  "ds_read_b128 a[136:139], v15 offset:2048\n"
  "v_mfma_f32_16x16x32_bf16 v[224:227], a[0:3], v[168:171], v[224:227]\n"
  "buffer_load_dwordx4 v[148:151], v11, s[36:39], s57 offen offset:1024 sc1\n"
  "v_mfma_f32_16x16x32_bf16 v[228:231], a[4:7], v[168:171], v[228:231]\n"
  "v_mfma_f32_16x16x32_bf16 v[232:235], a[8:11], v[168:171], v[232:235]\n"
  "s_waitcnt vmcnt(22)\n"
  "s_nop 0\n"
  "v_mfma_f32_16x16x32_bf16 v[236:239], a[248:251], v[172:175], v[236:239]\n"
  "buffer_load_dwordx4 v[152:155], v12, s[36:39], s57 offen offset:1024 sc1\n"
  "v_mfma_f32_16x16x32_bf16 v[240:243], a[252:255], v[172:175], v[240:243]\n"
  "ds_read_b128 a[140:143], v15 offset:3072\n"
  "v_mfma_f32_16x16x32_bf16 v[244:247], a[0:3], v[172:175], v[244:247]\n"
  "buffer_load_dwordx4 v[156:159], v13, s[36:39], s57 offen offset:1024 sc1\n"
  "v_mfma_f32_16x16x32_bf16 v[248:251], a[4:7], v[172:175], v[248:251]\n"
  "v_mfma_f32_16x16x32_bf16 v[252:255], a[8:11], v[172:175], v[252:255]\n"
  "s_waitcnt vmcnt(23)\n"
  "s_nop 0\n"
  "v_mfma_f32_16x16x32_bf16 v[176:179], a[12:15], v[64:67], v[176:179]\n"
  "buffer_load_dwordx4 v[160:163], v10, s[36:39], s58 offen sc1\n"
  "v_mfma_f32_16x16x32_bf16 v[180:183], a[16:19], v[64:67], v[180:183]\n"
  "ds_read_b128 a[144:147], v15 offset:4096\n"
  "v_mfma_f32_16x16x32_bf16 v[184:187], a[20:23], v[64:67], v[184:187]\n"
  "buffer_load_dwordx4 v[164:167], v10, s[36:39], s58 offen offset:1024 sc1\n"
  "v_mfma_f32_16x16x32_bf16 v[188:191], a[24:27], v[64:67], v[188:191]\n"
  "v_mfma_f32_16x16x32_bf16 v[192:195], a[28:31], v[64:67], v[192:195]\n"
  "s_waitcnt vmcnt(24)\n"
  "s_nop 0\n"
  "v_mfma_f32_16x16x32_bf16 v[196:199], a[12:15], v[68:71], v[196:199]\n"
  "buffer_load_dwordx4 v[168:171], v10, s[36:39], s59 offen sc1\n"
  "v_mfma_f32_16x16x32_bf16 v[200:203], a[16:19], v[68:71], v[200:203]\n"
  "ds_read_b128 a[148:151], v15 offset:5120\n"
  "v_mfma_f32_16x16x32_bf16 v[204:207], a[20:23], v[68:71], v[204:207]\n"
  "buffer_load_dwordx4 v[172:175], v10, s[36:39], s59 offen offset:1024 sc1\n"
  "v_mfma_f32_16x16x32_bf16 v[208:211], a[24:27], v[68:71], v[208:211]\n"
  "v_mfma_f32_16x16x32_bf16 v[212:215], a[28:31], v[68:71], v[212:215]\n"
  "s_waitcnt vmcnt(25)\n"
  "s_nop 0\n"
  "v_mfma_f32_16x16x32_bf16 v[216:219], a[12:15], v[72:75], v[216:219]\n"
  "buffer_load_dwordx4 v[64:67], v11, s[36:39], s58 offen sc1\n"
  "v_mfma_f32_16x16x32_bf16 v[220:223], a[16:19], v[72:75], v[220:223]\n"
  "ds_read_b128 a[152:155], v15 offset:6144\n"
  "v_mfma_f32_16x16x32_bf16 v[224:227], a[20:23], v[72:75], v[224:227]\n"
  "buffer_load_dwordx4 v[68:71], v11, s[36:39], s58 offen offset:1024 sc1\n"
  "v_mfma_f32_16x16x32_bf16 v[228:231], a[24:27], v[72:75], v[228:231]\n"
  "v_mfma_f32_16x16x32_bf16 v[232:235], a[28:31], v[72:75], v[232:235]\n"
  "s_waitcnt vmcnt(26)\n"
  "s_nop 0\n"
  "v_mfma_f32_16x16x32_bf16 v[236:239], a[12:15], v[76:79], v[236:239]\n"
  "buffer_load_dwordx4 v[72:75], v11, s[36:39], s59 offen sc1\n"
  "v_mfma_f32_16x16x32_bf16 v[240:243], a[16:19], v[76:79], v[240:243]\n"
  "ds_read_b128 a[156:159], v15 offset:7168\n"
  "v_mfma_f32_16x16x32_bf16 v[244:247], a[20:23], v[76:79], v[244:247]\n"
  "v_mfma_f32_16x16x32_bf16 v[248:251], a[24:27], v[76:79], v[248:251]\n"
  "v_mfma_f32_16x16x32_bf16 v[252:255], a[28:31], v[76:79], v[252:255]\n"
  "s_waitcnt vmcnt(26)\n"
  "s_nop 0\n"
  "v_mfma_f32_16x16x32_bf16 v[176:179], a[32:35], v[80:83], v[176:179]\n"
  "buffer_load_dwordx4 v[76:79], v11, s[36:39], s59 offen offset:1024 sc1\n"
  "v_mfma_f32_16x16x32_bf16 v[180:183], a[36:39], v[80:83], v[180:183]\n"
  "ds_read_b128 a[160:163], v15 offset:8192\n"
  "v_mfma_f32_16x16x32_bf16 v[184:187], a[40:43], v[80:83], v[184:187]\n"
  "v_mfma_f32_16x16x32_bf16 v[188:191], a[44:47], v[80:83], v[188:191]\n"
  "v_mfma_f32_16x16x32_bf16 v[192:195], a[48:51], v[80:83], v[192:195]\n"
  "s_waitcnt vmcnt(26)\n"
  "s_nop 0\n"
  "v_mfma_f32_16x16x32_bf16 v[196:199], a[32:35], v[84:87], v[196:199]\n"
  "buffer_load_dwordx4 v[80:83], v12, s[36:39], s58 offen sc1\n"
  "v_mfma_f32_16x16x32_bf16 v[200:203], a[36:39], v[84:87], v[200:203]\n"
  "ds_read_b128 a[164:167], v15 offset:9216\n"
  "v_mfma_f32_16x16x32_bf16 v[204:207], a[40:43], v[84:87], v[204:207]\n"
  "v_mfma_f32_16x16x32_bf16 v[208:211], a[44:47], v[84:87], v[208:211]\n"
  "v_mfma_f32_16x16x32_bf16 v[212:215], a[48:51], v[84:87], v[212:215]\n"
  "s_waitcnt vmcnt(26)\n"
  "s_nop 0\n"
  "v_mfma_f32_16x16x32_bf16 v[216:219], a[32:35], v[88:91], v[216:219]\n"
  "buffer_load_dwordx4 v[84:87], v12, s[36:39], s58 offen offset:1024 sc1\n"
  "v_mfma_f32_16x16x32_bf16 v[220:223], a[36:39], v[88:91], v[220:223]\n"
  "ds_read_b128 a[168:171], v15 offset:10240\n"
  "v_mfma_f32_16x16x32_bf16 v[224:227], a[40:43], v[88:91], v[224:227]\n"
  "v_mfma_f32_16x16x32_bf16 v[228:231], a[44:47], v[88:91], v[228:231]\n"
  "v_mfma_f32_16x16x32_bf16 v[232:235], a[48:51], v[88:91], v[232:235]\n"
  "s_waitcnt vmcnt(26)\n"
  "s_nop 0\n"
  "v_mfma_f32_16x16x32_bf16 v[236:239], a[32:35], v[92:95], v[236:239]\n"
  "buffer_load_dwordx4 v[88:91], v12, s[36:39], s59 offen sc1\n"
  "v_mfma_f32_16x16x32_bf16 v[240:243], a[36:39], v[92:95], v[240:243]\n"
  "ds_read_b128 a[172:175], v15 offset:11264\n"
  "v_mfma_f32_16x16x32_bf16 v[244:247], a[40:43], v[92:95], v[244:247]\n"
  "v_mfma_f32_16x16x32_bf16 v[248:251], a[44:47], v[92:95], v[248:251]\n"
  "v_mfma_f32_16x16x32_bf16 v[252:255], a[48:51], v[92:95], v[252:255]\n"
  "s_waitcnt vmcnt(26)\n"
  "s_nop 0\n"
  "v_mfma_f32_16x16x32_bf16 v[176:179], a[52:55], v[96:99], v[176:179]\n"
  "buffer_load_dwordx4 v[92:95], v12, s[36:39], s59 offen offset:1024 sc1\n"
  "v_mfma_f32_16x16x32_bf16 v[180:183], a[56:59], v[96:99], v[180:183]\n"
  "ds_read_b128 a[176:179], v15 offset:12288\n"
  "v_mfma_f32_16x16x32_bf16 v[184:187], a[60:63], v[96:99], v[184:187]\n"
  "v_mfma_f32_16x16x32_bf16 v[188:191], a[64:67], v[96:99], v[188:191]\n"
  "v_mfma_f32_16x16x32_bf16 v[192:195], a[68:71], v[96:99], v[192:195]\n"
  "s_waitcnt vmcnt(26)\n"
  "s_nop 0\n"
  "v_mfma_f32_16x16x32_bf16 v[196:199], a[52:55], v[100:103], v[196:199]\n"
  "buffer_load_dwordx4 v[96:99], v13, s[36:39], s58 offen sc1\n"
  "v_mfma_f32_16x16x32_bf16 v[200:203], a[56:59], v[100:103], v[200:203]\n"
  "ds_read_b128 a[180:183], v15 offset:13312\n"
  "v_mfma_f32_16x16x32_bf16 v[204:207], a[60:63], v[100:103], v[204:207]\n"
  "v_mfma_f32_16x16x32_bf16 v[208:211], a[64:67], v[100:103], v[208:211]\n"
  "v_mfma_f32_16x16x32_bf16 v[212:215], a[68:71], v[100:103], v[212:215]\n"
  "s_waitcnt vmcnt(26)\n"
  "s_nop 0\n"
  "v_mfma_f32_16x16x32_bf16 v[216:219], a[52:55], v[104:107], v[216:219]\n"
  "buffer_load_dwordx4 v[100:103], v13, s[36:39], s58 offen offset:1024 sc1\n"
  "v_mfma_f32_16x16x32_bf16 v[220:223], a[56:59], v[104:107], v[220:223]\n"
  "ds_read_b128 a[184:187], v15 offset:14336\n"
  "v_mfma_f32_16x16x32_bf16 v[224:227], a[60:63], v[104:107], v[224:227]\n"
  "v_mfma_f32_16x16x32_bf16 v[228:231], a[64:67], v[104:107], v[228:231]\n"
  "v_mfma_f32_16x16x32_bf16 v[232:235], a[68:71], v[104:107], v[232:235]\n"
  "s_waitcnt vmcnt(26)\n"
  "s_nop 0\n"
  "v_mfma_f32_16x16x32_bf16 v[236:239], a[52:55], v[108:111], v[236:239]\n"
  "buffer_load_dwordx4 v[104:107], v13, s[36:39], s59 offen sc1\n"
  "v_mfma_f32_16x16x32_bf16 v[240:243], a[56:59], v[108:111], v[240:243]\n"
  "ds_read_b128 a[188:191], v15 offset:15360\n"
  "v_mfma_f32_16x16x32_bf16 v[244:247], a[60:63], v[108:111], v[244:247]\n"
  "v_mfma_f32_16x16x32_bf16 v[248:251], a[64:67], v[108:111], v[248:251]\n"
  "v_mfma_f32_16x16x32_bf16 v[252:255], a[68:71], v[108:111], v[252:255]\n"
  "s_waitcnt vmcnt(26)\n"
  "s_nop 0\n"
  "v_mfma_f32_16x16x32_bf16 v[176:179], a[72:75], v[112:115], v[176:179]\n"
  "buffer_load_dwordx4 v[108:111], v13, s[36:39], s59 offen offset:1024 sc1\n"
  "v_mfma_f32_16x16x32_bf16 v[180:183], a[76:79], v[112:115], v[180:183]\n"
  "ds_read_b128 a[192:195], v15 offset:16384\n"
  "v_mfma_f32_16x16x32_bf16 v[184:187], a[80:83], v[112:115], v[184:187]\n"
  "v_mfma_f32_16x16x32_bf16 v[188:191], a[84:87], v[112:115], v[188:191]\n"
  "v_mfma_f32_16x16x32_bf16 v[192:195], a[88:91], v[112:115], v[192:195]\n"
  "s_waitcnt vmcnt(26)\n"
  "s_nop 0\n"
  "v_mfma_f32_16x16x32_bf16 v[196:199], a[72:75], v[116:119], v[196:199]\n"
  "v_mfma_f32_16x16x32_bf16 v[200:203], a[76:79], v[116:119], v[200:203]\n"
  "ds_read_b128 a[196:199], v15 offset:17408\n"
  "v_mfma_f32_16x16x32_bf16 v[204:207], a[80:83], v[116:119], v[204:207]\n"
  "v_mfma_f32_16x16x32_bf16 v[208:211], a[84:87], v[116:119], v[208:211]\n"
  "v_mfma_f32_16x16x32_bf16 v[212:215], a[88:91], v[116:119], v[212:215]\n"
  "s_waitcnt vmcnt(25)\n"
  "s_nop 0\n"
  "v_mfma_f32_16x16x32_bf16 v[216:219], a[72:75], v[120:123], v[216:219]\n"
  "v_mfma_f32_16x16x32_bf16 v[220:223], a[76:79], v[120:123], v[220:223]\n"
  "ds_read_b128 a[200:203], v15 offset:18432\n"
  "v_mfma_f32_16x16x32_bf16 v[224:227], a[80:83], v[120:123], v[224:227]\n"
  "v_mfma_f32_16x16x32_bf16 v[228:231], a[84:87], v[120:123], v[228:231]\n"
  "v_mfma_f32_16x16x32_bf16 v[232:235], a[88:91], v[120:123], v[232:235]\n"
  "s_waitcnt vmcnt(24)\n"
  "s_nop 0\n"
  "v_mfma_f32_16x16x32_bf16 v[236:239], a[72:75], v[124:127], v[236:239]\n"
  "v_mfma_f32_16x16x32_bf16 v[240:243], a[76:79], v[124:127], v[240:243]\n"
  "ds_read_b128 a[204:207], v15 offset:19456\n"
  "v_mfma_f32_16x16x32_bf16 v[244:247], a[80:83], v[124:127], v[244:247]\n"
  "v_mfma_f32_16x16x32_bf16 v[248:251], a[84:87], v[124:127], v[248:251]\n"
  "v_mfma_f32_16x16x32_bf16 v[252:255], a[88:91], v[124:127], v[252:255]\n"
  "s_waitcnt vmcnt(23)\n"
  "s_nop 0\n"
  "v_mfma_f32_16x16x32_bf16 v[176:179], a[92:95], v[128:131], v[176:179]\n"
  "v_mfma_f32_16x16x32_bf16 v[180:183], a[96:99], v[128:131], v[180:183]\n"
  "ds_read_b128 a[208:211], v15 offset:20480\n"
  "v_mfma_f32_16x16x32_bf16 v[184:187], a[100:103], v[128:131], v[184:187]\n"
  "v_mfma_f32_16x16x32_bf16 v[188:191], a[104:107], v[128:131], v[188:191]\n"
  "v_mfma_f32_16x16x32_bf16 v[192:195], a[108:111], v[128:131], v[192:195]\n"
  "s_waitcnt vmcnt(22)\n"
  "s_nop 0\n"
  "v_mfma_f32_16x16x32_bf16 v[196:199], a[92:95], v[132:135], v[196:199]\n"
  "v_mfma_f32_16x16x32_bf16 v[200:203], a[96:99], v[132:135], v[200:203]\n"
  "v_mfma_f32_16x16x32_bf16 v[204:207], a[100:103], v[132:135], v[204:207]\n"
  "v_mfma_f32_16x16x32_bf16 v[208:211], a[104:107], v[132:135], v[208:211]\n"
  "v_mfma_f32_16x16x32_bf16 v[212:215], a[108:111], v[132:135], v[212:215]\n"
  "s_waitcnt vmcnt(21)\n"
  "s_nop 0\n"
  "v_mfma_f32_16x16x32_bf16 v[216:219], a[92:95], v[136:139], v[216:219]\n"
  "v_mfma_f32_16x16x32_bf16 v[220:223], a[96:99], v[136:139], v[220:223]\n"
  "v_mfma_f32_16x16x32_bf16 v[224:227], a[100:103], v[136:139], v[224:227]\n"
  "v_mfma_f32_16x16x32_bf16 v[228:231], a[104:107], v[136:139], v[228:231]\n"
  "v_mfma_f32_16x16x32_bf16 v[232:235], a[108:111], v[136:139], v[232:235]\n"
  "s_waitcnt vmcnt(20)\n"
  "s_nop 0\n"
  "v_mfma_f32_16x16x32_bf16 v[236:239], a[92:95], v[140:143], v[236:239]\n"
  "v_mfma_f32_16x16x32_bf16 v[240:243], a[96:99], v[140:143], v[240:243]\n"
  "v_mfma_f32_16x16x32_bf16 v[244:247], a[100:103], v[140:143], v[244:247]\n"
  "v_mfma_f32_16x16x32_bf16 v[248:251], a[104:107], v[140:143], v[248:251]\n"
  "v_mfma_f32_16x16x32_bf16 v[252:255], a[108:111], v[140:143], v[252:255]\n"
  "s_waitcnt vmcnt(19)\n"
  "s_waitcnt lgkmcnt(0)\n"
  "v_mfma_f32_16x16x32_bf16 v[176:179], a[112:115], v[144:147], v[176:179]\n"
  "v_mfma_f32_16x16x32_bf16 v[180:183], a[116:119], v[144:147], v[180:183]\n"
  "v_mfma_f32_16x16x32_bf16 v[184:187], a[120:123], v[144:147], v[184:187]\n"
  "v_mfma_f32_16x16x32_bf16 v[188:191], a[124:127], v[144:147], v[188:191]\n"
  "v_mfma_f32_16x16x32_bf16 v[192:195], a[128:131], v[144:147], v[192:195]\n"
  "s_waitcnt vmcnt(18)\n"
  "s_nop 0\n"
  "v_mfma_f32_16x16x32_bf16 v[196:199], a[112:115], v[148:151], v[196:199]\n"
  "v_mfma_f32_16x16x32_bf16 v[200:203], a[116:119], v[148:151], v[200:203]\n"
  "v_mfma_f32_16x16x32_bf16 v[204:207], a[120:123], v[148:151], v[204:207]\n"
  "v_mfma_f32_16x16x32_bf16 v[208:211], a[124:127], v[148:151], v[208:211]\n"
  "v_mfma_f32_16x16x32_bf16 v[212:215], a[128:131], v[148:151], v[212:215]\n"
  "s_waitcnt vmcnt(17)\n"
  "s_nop 0\n"
  "v_mfma_f32_16x16x32_bf16 v[216:219], a[112:115], v[152:155], v[216:219]\n"
  "v_mfma_f32_16x16x32_bf16 v[220:223], a[116:119], v[152:155], v[220:223]\n"
  "v_mfma_f32_16x16x32_bf16 v[224:227], a[120:123], v[152:155], v[224:227]\n"
  "v_mfma_f32_16x16x32_bf16 v[228:231], a[124:127], v[152:155], v[228:231]\n"
  "v_mfma_f32_16x16x32_bf16 v[232:235], a[128:131], v[152:155], v[232:235]\n"
  "s_waitcnt vmcnt(16)\n"
  "s_nop 0\n"
  "v_mfma_f32_16x16x32_bf16 v[236:239], a[112:115], v[156:159], v[236:239]\n"
  "v_mfma_f32_16x16x32_bf16 v[240:243], a[116:119], v[156:159], v[240:243]\n"
  "v_mfma_f32_16x16x32_bf16 v[244:247], a[120:123], v[156:159], v[244:247]\n"
  "v_mfma_f32_16x16x32_bf16 v[248:251], a[124:127], v[156:159], v[248:251]\n"
  "v_mfma_f32_16x16x32_bf16 v[252:255], a[128:131], v[156:159], v[252:255]\n"
  "s_waitcnt vmcnt(15)\n"
  "s_nop 0\n"
  "v_mfma_f32_16x16x32_bf16 v[176:179], a[132:135], v[160:163], v[176:179]\n"
  "v_mfma_f32_16x16x32_bf16 v[180:183], a[136:139], v[160:163], v[180:183]\n"
  "v_mfma_f32_16x16x32_bf16 v[184:187], a[140:143], v[160:163], v[184:187]\n"
  "v_mfma_f32_16x16x32_bf16 v[188:191], a[144:147], v[160:163], v[188:191]\n"
  "v_mfma_f32_16x16x32_bf16 v[192:195], a[148:151], v[160:163], v[192:195]\n"
  "s_waitcnt vmcnt(14)\n"
  "s_nop 0\n"
  "v_mfma_f32_16x16x32_bf16 v[176:179], a[152:155], v[164:167], v[176:179]\n"
  "v_mfma_f32_16x16x32_bf16 v[180:183], a[156:159], v[164:167], v[180:183]\n"
  "v_mfma_f32_16x16x32_bf16 v[184:187], a[160:163], v[164:167], v[184:187]\n"
  "v_mfma_f32_16x16x32_bf16 v[188:191], a[164:167], v[164:167], v[188:191]\n"
  "v_mfma_f32_16x16x32_bf16 v[192:195], a[168:171], v[164:167], v[192:195]\n"
  "s_waitcnt vmcnt(13)\n"
  "s_nop 0\n"
  "v_mfma_f32_16x16x32_bf16 v[176:179], a[172:175], v[168:171], v[176:179]\n"
  "v_mfma_f32_16x16x32_bf16 v[180:183], a[176:179], v[168:171], v[180:183]\n"
  "v_mfma_f32_16x16x32_bf16 v[184:187], a[180:183], v[168:171], v[184:187]\n"
  "v_mfma_f32_16x16x32_bf16 v[188:191], a[184:187], v[168:171], v[188:191]\n"
  "v_mfma_f32_16x16x32_bf16 v[192:195], a[188:191], v[168:171], v[192:195]\n"
  "s_waitcnt vmcnt(12)\n"
  "s_nop 0\n"
  "v_mfma_f32_16x16x32_bf16 v[176:179], a[192:195], v[172:175], v[176:179]\n"
  "v_mfma_f32_16x16x32_bf16 v[180:183], a[196:199], v[172:175], v[180:183]\n"
  "v_mfma_f32_16x16x32_bf16 v[184:187], a[200:203], v[172:175], v[184:187]\n"
  "v_mfma_f32_16x16x32_bf16 v[188:191], a[204:207], v[172:175], v[188:191]\n"
  "v_mfma_f32_16x16x32_bf16 v[192:195], a[208:211], v[172:175], v[192:195]\n"
  "s_waitcnt vmcnt(11)\n"
  "s_nop 0\n"
  "v_mfma_f32_16x16x32_bf16 v[196:199], a[132:135], v[64:67], v[196:199]\n"
  "v_mfma_f32_16x16x32_bf16 v[200:203], a[136:139], v[64:67], v[200:203]\n"
  "v_mfma_f32_16x16x32_bf16 v[204:207], a[140:143], v[64:67], v[204:207]\n"
  "v_mfma_f32_16x16x32_bf16 v[208:211], a[144:147], v[64:67], v[208:211]\n"
  "v_mfma_f32_16x16x32_bf16 v[212:215], a[148:151], v[64:67], v[212:215]\n"
  "s_waitcnt vmcnt(10)\n"
  "s_nop 0\n"
  "v_mfma_f32_16x16x32_bf16 v[196:199], a[152:155], v[68:71], v[196:199]\n"
  "ds_write_b128 v34, v[176:179] offset:0\n"
  "v_mfma_f32_16x16x32_bf16 v[200:203], a[156:159], v[68:71], v[200:203]\n"
  "ds_write_b128 v34, v[180:183] offset:1024\n"
  "v_mfma_f32_16x16x32_bf16 v[204:207], a[160:163], v[68:71], v[204:207]\n"
  "ds_write_b128 v34, v[184:187] offset:2048\n"
  "v_mfma_f32_16x16x32_bf16 v[208:211], a[164:167], v[68:71], v[208:211]\n"
  "ds_write_b128 v34, v[188:191] offset:3072\n"
  "v_mfma_f32_16x16x32_bf16 v[212:215], a[168:171], v[68:71], v[212:215]\n"
  "ds_write_b128 v34, v[192:195] offset:4096\n"
  "s_waitcnt vmcnt(9)\n"
  "s_nop 0\n"
  "v_mfma_f32_16x16x32_bf16 v[196:199], a[172:175], v[72:75], v[196:199]\n"
  "v_mfma_f32_16x16x32_bf16 v[200:203], a[176:179], v[72:75], v[200:203]\n"
  "v_mfma_f32_16x16x32_bf16 v[204:207], a[180:183], v[72:75], v[204:207]\n"
  "v_mfma_f32_16x16x32_bf16 v[208:211], a[184:187], v[72:75], v[208:211]\n"
  "v_mfma_f32_16x16x32_bf16 v[212:215], a[188:191], v[72:75], v[212:215]\n"
  "s_waitcnt vmcnt(8)\n"
  "s_nop 0\n"
  "v_mfma_f32_16x16x32_bf16 v[196:199], a[192:195], v[76:79], v[196:199]\n"
  "v_mfma_f32_16x16x32_bf16 v[200:203], a[196:199], v[76:79], v[200:203]\n"
  "v_mfma_f32_16x16x32_bf16 v[204:207], a[200:203], v[76:79], v[204:207]\n"
  "v_mfma_f32_16x16x32_bf16 v[208:211], a[204:207], v[76:79], v[208:211]\n"
  "v_mfma_f32_16x16x32_bf16 v[212:215], a[208:211], v[76:79], v[212:215]\n"
  "s_waitcnt vmcnt(7)\n"
  "s_nop 0\n"
  "v_mfma_f32_16x16x32_bf16 v[216:219], a[132:135], v[80:83], v[216:219]\n"
  "v_mfma_f32_16x16x32_bf16 v[220:223], a[136:139], v[80:83], v[220:223]\n"
  "v_mfma_f32_16x16x32_bf16 v[224:227], a[140:143], v[80:83], v[224:227]\n"
  "v_mfma_f32_16x16x32_bf16 v[228:231], a[144:147], v[80:83], v[228:231]\n"
  "v_mfma_f32_16x16x32_bf16 v[232:235], a[148:151], v[80:83], v[232:235]\n"
  "s_waitcnt vmcnt(6)\n"
  "s_nop 0\n"
  "v_mfma_f32_16x16x32_bf16 v[216:219], a[152:155], v[84:87], v[216:219]\n"
  "ds_write_b128 v35, v[196:199] offset:0\n"
  "v_mfma_f32_16x16x32_bf16 v[220:223], a[156:159], v[84:87], v[220:223]\n"
  "ds_write_b128 v35, v[200:203] offset:1024\n"
  "v_mfma_f32_16x16x32_bf16 v[224:227], a[160:163], v[84:87], v[224:227]\n"
  "ds_write_b128 v35, v[204:207] offset:2048\n"
  "v_mfma_f32_16x16x32_bf16 v[228:231], a[164:167], v[84:87], v[228:231]\n"
  "ds_write_b128 v35, v[208:211] offset:3072\n"
  "v_mfma_f32_16x16x32_bf16 v[232:235], a[168:171], v[84:87], v[232:235]\n"
  "ds_write_b128 v35, v[212:215] offset:4096\n"
  "s_waitcnt vmcnt(5)\n"
  "s_nop 0\n"
  "v_mfma_f32_16x16x32_bf16 v[216:219], a[172:175], v[88:91], v[216:219]\n"
  "v_mfma_f32_16x16x32_bf16 v[220:223], a[176:179], v[88:91], v[220:223]\n"
  "v_mfma_f32_16x16x32_bf16 v[224:227], a[180:183], v[88:91], v[224:227]\n"
  "v_mfma_f32_16x16x32_bf16 v[228:231], a[184:187], v[88:91], v[228:231]\n"
  "v_mfma_f32_16x16x32_bf16 v[232:235], a[188:191], v[88:91], v[232:235]\n"
  "s_waitcnt vmcnt(4)\n"
  "s_nop 0\n"
  "v_mfma_f32_16x16x32_bf16 v[216:219], a[192:195], v[92:95], v[216:219]\n"
  "v_mfma_f32_16x16x32_bf16 v[220:223], a[196:199], v[92:95], v[220:223]\n"
  "v_mfma_f32_16x16x32_bf16 v[224:227], a[200:203], v[92:95], v[224:227]\n"
  "v_mfma_f32_16x16x32_bf16 v[228:231], a[204:207], v[92:95], v[228:231]\n"
  "v_mfma_f32_16x16x32_bf16 v[232:235], a[208:211], v[92:95], v[232:235]\n"
  "s_waitcnt vmcnt(3)\n"
  "s_nop 0\n"
  "v_mfma_f32_16x16x32_bf16 v[236:239], a[132:135], v[96:99], v[236:239]\n"
  "v_mfma_f32_16x16x32_bf16 v[240:243], a[136:139], v[96:99], v[240:243]\n"
  "v_mfma_f32_16x16x32_bf16 v[244:247], a[140:143], v[96:99], v[244:247]\n"
  "v_mfma_f32_16x16x32_bf16 v[248:251], a[144:147], v[96:99], v[248:251]\n"
  "v_mfma_f32_16x16x32_bf16 v[252:255], a[148:151], v[96:99], v[252:255]\n"
  "s_waitcnt vmcnt(2)\n"
  "s_nop 0\n"
  "v_mfma_f32_16x16x32_bf16 v[236:239], a[152:155], v[100:103], v[236:239]\n"
  "ds_write_b128 v36, v[216:219] offset:0\n"
  "v_mfma_f32_16x16x32_bf16 v[240:243], a[156:159], v[100:103], v[240:243]\n"
  "ds_write_b128 v36, v[220:223] offset:1024\n"
  "v_mfma_f32_16x16x32_bf16 v[244:247], a[160:163], v[100:103], v[244:247]\n"
  "ds_write_b128 v36, v[224:227] offset:2048\n"
  "v_mfma_f32_16x16x32_bf16 v[248:251], a[164:167], v[100:103], v[248:251]\n"
  "ds_write_b128 v36, v[228:231] offset:3072\n"
  "v_mfma_f32_16x16x32_bf16 v[252:255], a[168:171], v[100:103], v[252:255]\n"
  "ds_write_b128 v36, v[232:235] offset:4096\n"
  "s_waitcnt vmcnt(1)\n"
  "s_nop 0\n"
  "v_mfma_f32_16x16x32_bf16 v[236:239], a[172:175], v[104:107], v[236:239]\n"
  "v_mfma_f32_16x16x32_bf16 v[240:243], a[176:179], v[104:107], v[240:243]\n"
  "v_mfma_f32_16x16x32_bf16 v[244:247], a[180:183], v[104:107], v[244:247]\n"
  "v_mfma_f32_16x16x32_bf16 v[248:251], a[184:187], v[104:107], v[248:251]\n"
  "v_mfma_f32_16x16x32_bf16 v[252:255], a[188:191], v[104:107], v[252:255]\n"
  "s_waitcnt vmcnt(0)\n"
  "s_nop 0\n"
  "v_mfma_f32_16x16x32_bf16 v[236:239], a[192:195], v[108:111], v[236:239]\n"
  "v_mfma_f32_16x16x32_bf16 v[240:243], a[196:199], v[108:111], v[240:243]\n"
  "v_mfma_f32_16x16x32_bf16 v[244:247], a[200:203], v[108:111], v[244:247]\n"
  "v_mfma_f32_16x16x32_bf16 v[248:251], a[204:207], v[108:111], v[248:251]\n"
  "v_mfma_f32_16x16x32_bf16 v[252:255], a[208:211], v[108:111], v[252:255]\n"
  "s_waitcnt lgkmcnt(0)\n"
  "s_barrier\n"
  "ds_read_b128 v[64:67], v37 offset:0\n"
  "ds_read_b128 v[68:71], v37 offset:1024\n"
  "ds_read_b128 v[72:75], v37 offset:2048\n"
  "ds_read_b128 v[76:79], v37 offset:3072\n"
  "ds_read_b128 v[80:83], v37 offset:4096\n"
  "ds_read_b128 v[84:87], v37 offset:5120\n"
  "ds_read_b128 v[88:91], v37 offset:6144\n"
  "ds_read_b128 v[92:95], v37 offset:7168\n"
  "ds_read_b128 v[96:99], v37 offset:8192\n"
  "ds_read_b128 v[100:103], v37 offset:9216\n"
  "ds_read_b128 v[104:107], v37 offset:10240\n"
  "ds_read_b128 v[108:111], v37 offset:11264\n"
  "ds_read_b128 v[112:115], v37 offset:12288\n"
  "ds_read_b128 v[116:119], v37 offset:13312\n"
  "ds_read_b128 v[120:123], v37 offset:14336\n"
  "v_lshlrev_b32_e64 v40, 16, v24\n"
  "v_and_b32 v41, 0xffff0000, v24\n"
  "v_lshlrev_b32_e64 v42, 16, v25\n"
  "v_and_b32 v43, 0xffff0000, v25\n"
  "v_pk_add_f32 v[236:237], v[236:237], v[40:41]\n"
  "v_pk_add_f32 v[238:239], v[238:239], v[42:43]\n"
  "v_lshlrev_b32_e64 v40, 16, v26\n"
  "v_and_b32 v41, 0xffff0000, v26\n"
  "v_lshlrev_b32_e64 v42, 16, v27\n"
  "v_and_b32 v43, 0xffff0000, v27\n"
  "v_pk_add_f32 v[240:241], v[240:241], v[40:41]\n"
  "v_pk_add_f32 v[242:243], v[242:243], v[42:43]\n"
  "v_lshlrev_b32_e64 v40, 16, v28\n"
  "v_and_b32 v41, 0xffff0000, v28\n"
  "v_lshlrev_b32_e64 v42, 16, v29\n"
  "v_and_b32 v43, 0xffff0000, v29\n"
  "v_pk_add_f32 v[244:245], v[244:245], v[40:41]\n"
  "v_pk_add_f32 v[246:247], v[246:247], v[42:43]\n"
  "v_lshlrev_b32_e64 v40, 16, v30\n"
  "v_and_b32 v41, 0xffff0000, v30\n"
  "v_lshlrev_b32_e64 v42, 16, v31\n"
  "v_and_b32 v43, 0xffff0000, v31\n"
  "v_pk_add_f32 v[248:249], v[248:249], v[40:41]\n"
  "v_pk_add_f32 v[250:251], v[250:251], v[42:43]\n"
  "v_lshlrev_b32_e64 v40, 16, v32\n"
  "v_and_b32 v41, 0xffff0000, v32\n"
  "v_lshlrev_b32_e64 v42, 16, v33\n"
  "v_and_b32 v43, 0xffff0000, v33\n"
  "v_pk_add_f32 v[252:253], v[252:253], v[40:41]\n"
  "v_pk_add_f32 v[254:255], v[254:255], v[42:43]\n"
  "s_waitcnt lgkmcnt(10)\n"
  "s_nop 0\n"
  "v_pk_add_f32 v[236:237], v[236:237], v[64:65]\n"
  "v_pk_add_f32 v[238:239], v[238:239], v[66:67]\n"
  "v_pk_add_f32 v[240:241], v[240:241], v[68:69]\n"
  "v_pk_add_f32 v[242:243], v[242:243], v[70:71]\n"
  "v_pk_add_f32 v[244:245], v[244:245], v[72:73]\n"
  "v_pk_add_f32 v[246:247], v[246:247], v[74:75]\n"
  "v_pk_add_f32 v[248:249], v[248:249], v[76:77]\n"
  "v_pk_add_f32 v[250:251], v[250:251], v[78:79]\n"
  "v_pk_add_f32 v[252:253], v[252:253], v[80:81]\n"
  "v_pk_add_f32 v[254:255], v[254:255], v[82:83]\n"
  "s_waitcnt lgkmcnt(5)\n"
  "s_nop 0\n"
  "v_pk_add_f32 v[236:237], v[236:237], v[84:85]\n"
  "v_pk_add_f32 v[238:239], v[238:239], v[86:87]\n"
  "v_pk_add_f32 v[240:241], v[240:241], v[88:89]\n"
  "v_pk_add_f32 v[242:243], v[242:243], v[90:91]\n"
  "v_pk_add_f32 v[244:245], v[244:245], v[92:93]\n"
  "v_pk_add_f32 v[246:247], v[246:247], v[94:95]\n"
  "v_pk_add_f32 v[248:249], v[248:249], v[96:97]\n"
  "v_pk_add_f32 v[250:251], v[250:251], v[98:99]\n"
  "v_pk_add_f32 v[252:253], v[252:253], v[100:101]\n"
  "v_pk_add_f32 v[254:255], v[254:255], v[102:103]\n"
  "s_waitcnt lgkmcnt(0)\n"
  "s_nop 0\n"
  "v_pk_add_f32 v[236:237], v[236:237], v[104:105]\n"
  "v_pk_add_f32 v[238:239], v[238:239], v[106:107]\n"
  "v_pk_add_f32 v[240:241], v[240:241], v[108:109]\n"
  "v_pk_add_f32 v[242:243], v[242:243], v[110:111]\n"
  "v_pk_add_f32 v[244:245], v[244:245], v[112:113]\n"
  "v_pk_add_f32 v[246:247], v[246:247], v[114:115]\n"
  "v_pk_add_f32 v[248:249], v[248:249], v[116:117]\n"
  "v_pk_add_f32 v[250:251], v[250:251], v[118:119]\n"
  "v_pk_add_f32 v[252:253], v[252:253], v[120:121]\n"
  "v_pk_add_f32 v[254:255], v[254:255], v[122:123]\n"
  "v_cvt_pk_bf16_f32 v24, v236, v237\n"
  "v_cvt_pk_bf16_f32 v25, v238, v239\n"
  "v_cvt_pk_bf16_f32 v26, v240, v241\n"
  "v_cvt_pk_bf16_f32 v27, v242, v243\n"
  "v_cvt_pk_bf16_f32 v28, v244, v245\n"
  "v_cvt_pk_bf16_f32 v29, v246, v247\n"
  "v_cvt_pk_bf16_f32 v30, v248, v249\n"
  "v_cvt_pk_bf16_f32 v31, v250, v251\n"
  "v_cvt_pk_bf16_f32 v32, v252, v253\n"
  "v_cvt_pk_bf16_f32 v33, v254, v255\n"
  "buffer_store_dwordx4 v[24:27], v14, s[40:43], 0 offen\n"
  "buffer_store_dwordx4 v[28:31], v14, s[40:43], 0 offen offset:16\n"
  "buffer_store_dwordx2 v[32:33], v14, s[40:43], 0 offen offset:32\n"
  "s_cmp_eq_u32 s50, 0\n"
  "s_cbranch_scc0 .Lnoep\n"
  "v_mov_b32 v20, s44\n"
  "v_mov_b32 v21, s45\n"
  "s_mov_b64 exec, s[88:89]\n"
  "s_nop 0\n"
  "buffer_store_dwordx2 v[20:21], off, s[36:39], 0 sc1\n"
  "s_mov_b64 exec, -1\n"
  ".Lnoep:\n"
  ".Lcode_end:\n"
  :: "{s[0:1]}"(__builtin_amdgcn_kernarg_segment_ptr()), "{s2}"(blockIdx.x), "{v0}"(threadIdx.x), "{s3}"((unsigned)(size_t)lds)
  : "v1","v2","v3","v4","v5","v6","v7","v8","v9","v10","v11","v12","v13","v14","v15","v16","v17","v18","v19","v20","v21","v22","v23","v24","v25","v26","v27","v28","v29","v30","v31","v32","v33","v34","v35","v36","v37","v38","v39","v40","v41","v42","v43","v44","v45","v46","v47","v48","v49","v50","v51","v52","v53","v54","v55","v56","v57","v58","v59","v60","v61","v62","v63","v64","v65","v66","v67","v68","v69","v70","v71","v72","v73","v74","v75","v76","v77","v78","v79","v80","v81","v82","v83","v84","v85","v86","v87","v88","v89","v90","v91","v92","v93","v94","v95","v96","v97","v98","v99","v100","v101","v102","v103","v104","v105","v106","v107","v108","v109","v110","v111","v112","v113","v114","v115","v116","v117","v118","v119","v120","v121","v122","v123","v124","v125","v126","v127","v128","v129","v130","v131","v132","v133","v134","v135","v136","v137","v138","v139","v140","v141","v142","v143","v144","v145","v146","v147","v148","v149","v150","v151","v152","v153","v154","v155","v156","v157","v158","v159","v160","v161","v162","v163","v164","v165","v166","v167","v168","v169","v170","v171","v172","v173","v174","v175","v176","v177","v178","v179","v180","v181","v182","v183","v184","v185","v186","v187","v188","v189","v190","v191","v192","v193","v194","v195","v196","v197","v198","v199","v200","v201","v202","v203","v204","v205","v206","v207","v208","v209","v210","v211","v212","v213","v214","v215","v216","v217","v218","v219","v220","v221","v222","v223","v224","v225","v226","v227","v228","v229","v230","v231","v232","v233","v234","v235","v236","v237","v238","v239","v240","v241","v242","v243","v244","v245","v246","v247","v248","v249","v250","v251","v252","v253","v254","v255","a0","a1","a2","a3","a4","a5","a6","a7","a8","a9","a10","a11","a12","a13","a14","a15","a16","a17","a18","a19","a20","a21","a22","a23","a24","a25","a26","a27","a28","a29","a30","a31","a32","a33","a34","a35","a36","a37","a38","a39","a40","a41","a42","a43","a44","a45","a46","a47","a48","a49","a50","a51","a52","a53","a54","a55","a56","a57","a58","a59","a60","a61","a62","a63","a64","a65","a66","a67","a68","a69","a70","a71","a72","a73","a74","a75","a76","a77","a78","a79","a80","a81","a82","a83","a84","a85","a86","a87","a88","a89","a90","a91","a92","a93","a94","a95","a96","a97","a98","a99","a100","a101","a102","a103","a104","a105","a106","a107","a108","a109","a110","a111","a112","a113","a114","a115","a116","a117","a118","a119","a120","a121","a122","a123","a124","a125","a126","a127","a128","a129","a130","a131","a132","a133","a134","a135","a136","a137","a138","a139","a140","a141","a142","a143","a144","a145","a146","a147","a148","a149","a150","a151","a152","a153","a154","a155","a156","a157","a158","a159","a160","a161","a162","a163","a164","a165","a166","a167","a168","a169","a170","a171","a172","a173","a174","a175","a176","a177","a178","a179","a180","a181","a182","a183","a184","a185","a186","a187","a188","a189","a190","a191","a192","a193","a194","a195","a196","a197","a198","a199","a200","a201","a202","a203","a204","a205","a206","a207","a208","a209","a210","a211","a212","a213","a214","a215","a216","a217","a218","a219","a220","a221","a222","a223","a224","a225","a226","a227","a228","a229","a230","a231","a232","a233","a234","a235","a236","a237","a238","a239","a240","a241","a242","a243","a244","a245","a246","a247","a248","a249","a250","a251","a252","a253","a254","a255","s4","s5","s6","s7","s8","s9","s10","s11","s12","s13","s14","s15","s16","s17","s18","s19","s20","s21","s22","s23","s24","s25","s26","s27","s28","s29","s30","s31","s33","s34","s35","s36","s37","s38","s39","s40","s41","s42","s43","s44","s45","s46","s47","s48","s49","s50","s51","s52","s53","s54","s55","s56","s57","s58","s59","s60","s61","s62","s63","s64","s65","s66","s67","s68","s69","s70","s71","s72","s73","s74","s75","s76","s77","s78","s79","s80","s81","s82","s83","s84","s85","s86","s87","s88","s89","s90","s91","s92","s93","s94","s95","s96","s97","s98","s99","vcc","memory");
}
extern "C" void kernel_launch(void* const* d_in, const int* in_sizes, int n_in, void* d_out, int out_size, void* d_ws, size_t ws_size, hipStream_t stream) {
  KArgs a; a.p[0]=d_in[0]; a.p[1]=d_in[1]; a.p[2]=d_in[2]; a.p[3]=d_in[4]; a.p[4]=d_in[5]; a.p[5]=d_in[6]; a.p[6]=d_out; a.p[7]=d_ws;
  fused_k<<<256,256,0,stream>>>(a);
}
```
